```python
import torch

def _softmax(x, axis=-1):
    return torch.softmax(x, dim=axis)
def _gelu(x):
    return torch.nn.functional.gelu(x, approximate="tanh")
def _silu(x):
    return torch.nn.functional.silu(x)
def _sigmoid(x):
    return torch.sigmoid(x)
def _layernorm(x, axis=-1, eps=1e-5):
    m = x.mean(dim=axis, keepdim=True)
    v = x.var(dim=axis, keepdim=True, unbiased=False)
    return (x - m) / torch.sqrt(v + eps)
def _scatter_add(tgt, idx, src, axis=0):
    return tgt.clone().index_add_(axis, idx, src)
def _segment_sum(src, seg, n):
    out = torch.zeros((n,) + src.shape[1:], dtype=src.dtype)
    return out.index_add_(0, seg, src)
def _pad(x, pw):
    flat = tuple(v for pair in reversed(pw) for v in pair)
    return torch.nn.functional.pad(x, flat)

kernel_name = 'concat_heads__gqa_block_cached__gqa_block_cached__near_ridge'

def setup_inputs():

    _w = (lambda a: (lambda t: t.to(torch.bfloat16) if t.is_floating_point() else t)(torch.as_tensor(a)))
    def _A_setup():
        B, Sq, D = 16, 96, 640
        Hq, Hkv, Sk, Dh = 32, 32, 512, 128
        assert Hq % Hkv == 0, (Hq, Hkv)
        return {
            "x": _w((lambda *s: torch.randn(*s, dtype=torch.bfloat16) * 0.8)(B, Sq, D)),
            "k_cache": _w((lambda *s: torch.randn(*s, dtype=torch.bfloat16) * 0.8)(B, Hkv, Sk, Dh)),
            "v_cache": _w((lambda *s: torch.randn(*s, dtype=torch.bfloat16) * 0.05)(B, Hkv, Sk, Dh)),
            "wq": _w((lambda *s: torch.randn(*s, dtype=torch.bfloat16) * 0.8)(D, Hq * Dh) * (D ** -0.5)),
            "wo": _w((lambda *s: torch.randn(*s, dtype=torch.bfloat16) * 0.05)(Hq * Dh, D) * ((Hq * Dh) ** -0.5)),
        }
    _A_in = _A_setup()

    def _B_setup():
        B, Sq, D = 16, 96, 640
        Hq, Hkv, Sk, Dh = 32, 32, 512, 128
        assert Hq % Hkv == 0, (Hq, Hkv)
        return {
            "x": _w((lambda *s: torch.randn(*s, dtype=torch.bfloat16) * 0.8)(B, Sq, D)),
            "k_cache": _w((lambda *s: torch.randn(*s, dtype=torch.bfloat16) * 0.8)(B, Hkv, Sk, Dh)),
            "v_cache": _w((lambda *s: torch.randn(*s, dtype=torch.bfloat16) * 0.05)(B, Hkv, Sk, Dh)),
            "wq": _w((lambda *s: torch.randn(*s, dtype=torch.bfloat16) * 0.8)(D, Hq * Dh) * (D ** -0.5)),
            "wo": _w((lambda *s: torch.randn(*s, dtype=torch.bfloat16) * 0.05)(Hq * Dh, D) * ((Hq * Dh) ** -0.5)),
        }
    _B_in = _B_setup()

    return {
        "A_x": _A_in["x"],
        "A_k_cache": _A_in["k_cache"],
        "A_v_cache": _A_in["v_cache"],
        "A_wq": _A_in["wq"],
        "A_wo": _A_in["wo"],
        "B_x": _B_in["x"],
        "B_k_cache": _B_in["k_cache"],
        "B_v_cache": _B_in["v_cache"],
        "B_wq": _B_in["wq"],
        "B_wo": _B_in["wo"],
    }

def reference(A_x, A_k_cache, A_v_cache, A_wq, A_wo, B_x, B_k_cache, B_v_cache, B_wq, B_wo):
    def _A_ref(x, k_cache, v_cache, wq, wo):
        B, Sq, D = x.shape
        Hq, Dh = 32, 128
        Hkv = k_cache.shape[1]
        rep = Hq // Hkv
        q = (lambda x, *d: x.permute(*d))((x @ wq).reshape(B, Sq, Hq, Dh), 0, 2, 1, 3)
        k = (lambda x, n, axis: torch.repeat_interleave(x, n, dim=axis))(k_cache, rep, axis=1)
        v = (lambda x, n, axis: torch.repeat_interleave(x, n, dim=axis))(v_cache, rep, axis=1)
        scores = torch.einsum("bhqd,bhkd->bhqk", q, k) * (Dh ** -0.5)
        p = _softmax(scores, axis=-1)
        o = torch.einsum("bhqk,bhkd->bhqd", p, v)
        return (lambda x, *d: x.permute(*d))(o, 0, 2, 1, 3).reshape(B, Sq, Hq * Dh) @ wo

    def _B_ref(x, k_cache, v_cache, wq, wo):
        B, Sq, D = x.shape
        Hq, Dh = 32, 128
        Hkv = k_cache.shape[1]
        rep = Hq // Hkv
        q = (lambda x, *d: x.permute(*d))((x @ wq).reshape(B, Sq, Hq, Dh), 0, 2, 1, 3)
        k = (lambda x, n, axis: torch.repeat_interleave(x, n, dim=axis))(k_cache, rep, axis=1)
        v = (lambda x, n, axis: torch.repeat_interleave(x, n, dim=axis))(v_cache, rep, axis=1)
        scores = torch.einsum("bhqd,bhkd->bhqk", q, k) * (Dh ** -0.5)
        p = _softmax(scores, axis=-1)
        o = torch.einsum("bhqk,bhkd->bhqd", p, v)
        return (lambda x, *d: x.permute(*d))(o, 0, 2, 1, 3).reshape(B, Sq, Hq * Dh) @ wo

    a = _A_ref(A_x, A_k_cache, A_v_cache, A_wq, A_wo)
    b = _B_ref(B_x, B_k_cache, B_v_cache, B_wq, B_wo)
    return torch.concatenate([a, b], axis=-1)
```

```cpp
#include <hip/hip_runtime.h>
#include <hip/hip_bf16.h>
typedef unsigned short u16;
__device__ __forceinline__ float bf2f(u16 v){ return __uint_as_float(((unsigned)v)<<16); }
__device__ __forceinline__ u16 f2bf(float f){ unsigned u=__float_as_uint(f); u += 0x7FFF + ((u>>16)&1); return (u16)(u>>16); }
constexpr int B=16,SQ=96,D=640,H=32,SK=512,DH=128;

__global__ void gemm_naive(const u16* __restrict__ A, const u16* __restrict__ W, u16* __restrict__ C, int M, int N, int K, int ldc){
  __shared__ float sa[32][33], sw[32][33];
  int tx=threadIdx.x&31, ty=threadIdx.x>>5;
  int m0=blockIdx.y*32, n0=blockIdx.x*32;
  float acc[4]={0,0,0,0};
  for(int k0=0;k0<K;k0+=32){
    for(int i=0;i<4;i++){ int r=ty+8*i; sa[r][tx]=bf2f(A[(size_t)(m0+r)*K+k0+tx]); sw[r][tx]=bf2f(W[(size_t)(k0+r)*N+n0+tx]); }
    __syncthreads();
    for(int k=0;k<32;k++){ float w=sw[k][tx]; for(int i=0;i<4;i++) acc[i]+=sa[ty+8*i][k]*w; }
    __syncthreads();
  }
  for(int i=0;i<4;i++) C[(size_t)(m0+ty+8*i)*ldc+n0+tx]=f2bf(acc[i]);
}
__global__ void attn_naive(const u16* __restrict__ Q, const u16* __restrict__ Kc, const u16* __restrict__ Vc, u16* __restrict__ O){
  __shared__ float q[DH], p[SK], red[256];
  int row=blockIdx.x, h=blockIdx.y, b=row/SQ, t=threadIdx.x;
  if(t<DH) q[t]=bf2f(Q[(size_t)row*H*DH+h*DH+t]);
  __syncthreads();
  const u16* K=Kc+((size_t)b*H+h)*SK*DH; const u16* V=Vc+((size_t)b*H+h)*SK*DH;
  float mx=-1e30f;
  for(int k=t;k<SK;k+=256){ float s=0; for(int d=0;d<DH;d++) s+=q[d]*bf2f(K[k*DH+d]); s*=0.08838834764831845f; p[k]=s; mx=fmaxf(mx,s);}
  red[t]=mx; __syncthreads();
  for(int s=128;s>0;s>>=1){ if(t<s) red[t]=fmaxf(red[t],red[t+s]); __syncthreads(); }
  mx=red[0]; __syncthreads();
  float sm=0; for(int k=t;k<SK;k+=256){ float e=__expf(p[k]-mx); p[k]=e; sm+=e; }
  red[t]=sm; __syncthreads();
  for(int s=128;s>0;s>>=1){ if(t<s) red[t]+=red[t+s]; __syncthreads(); }
  sm=red[0];
  if(t<DH){ float o=0; for(int k=0;k<SK;k++) o+=p[k]*bf2f(V[k*DH+t]); O[(size_t)row*H*DH+h*DH+t]=f2bf(o/sm); }
}
extern "C" void kernel_launch(void* const* d_in, const int* in_sizes, int n_in, void* d_out, int out_size, void* d_ws, size_t ws_size, hipStream_t stream){
  u16* Q=(u16*)d_ws; u16* O=Q+(size_t)B*SQ*H*DH;
  for(int br=0;br<2;br++){
    const u16* x=(const u16*)d_in[br*5+0]; const u16* k=(const u16*)d_in[br*5+1]; const u16* v=(const u16*)d_in[br*5+2];
    const u16* wq=(const u16*)d_in[br*5+3]; const u16* wo=(const u16*)d_in[br*5+4];
    gemm_naive<<<dim3(H*DH/32,B*SQ/32),256,0,stream>>>(x,wq,Q,B*SQ,H*DH,D,H*DH);
    attn_naive<<<dim3(B*SQ,H),256,0,stream>>>(Q,k,v,O);
    gemm_naive<<<dim3(D/32,B*SQ/32),256,0,stream>>>(O,wo,(u16*)d_out+br*D,B*SQ,D,H*DH,2*D);
  }
}
```

```cpp
#include <hip/hip_runtime.h>

struct Ptrs { const char* p[10]; char* ws; char* out; };
__global__ __launch_bounds__(256, 1) void fused_attn(Ptrs a) {
  __shared__ __attribute__((aligned(16))) char lds[163840];
  const unsigned w = __builtin_amdgcn_readfirstlane(threadIdx.x >> 6);
  asm volatile(
    "s_bfe_u32 s14, %1, 0x10002\n"
    "s_mul_i32 s15, s14, 40\n"
    "s_load_dwordx8 s[4:11], %0, s15\n"
    "s_add_u32 s15, s15, 32\n"
    "s_load_dwordx2 s[12:13], %0, s15\n"
    "s_load_dwordx4 s[16:19], %0, 80\n"
    "s_mov_b32 s20, %2\n"
    "s_lshr_b32 s22, %1, 3\n"
    "s_lshr_b32 s21, %1, 4\n"
    "s_and_b32 s15, s22, 1\n"
    "s_and_b32 s36, %1, 3\n"
    "s_lshl_b32 s36, s36, 1\n"
    "s_or_b32 s36, s36, s15\n"
    "s_mul_i32 s15, s15, 5\n"
    "s_add_u32 s24, s15, s21\n"
    "s_and_b32 s15, %1, 3\n"
    "s_mul_i32 s15, s15, 3\n"
    "s_add_u32 s24, s24, s15\n"
    "s_add_u32 s24, s24, s21\n"
    "s_mul_i32 s15, s24, 205\n"
    "s_lshr_b32 s15, s15, 11\n"
    "s_mul_i32 s15, s15, 10\n"
    "s_sub_u32 s24, s24, s15\n"
    "s_lshr_b32 s25, s20, 1\n"
    "s_and_b32 s23, s21, 3\n"
    "s_add_u32 s23, s23, 1\n"
    "s_mov_b32 s42, 122880\n"
    "s_mov_b32 s43, 0x20000\n"
    "s_mov_b32 s46, 5235456\n"
    "s_mov_b32 s47, 0x20000\n"
    "v_mbcnt_lo_u32_b32 v208, -1, 0\n"
    "v_mbcnt_hi_u32_b32 v208, -1, v208\n"
    "s_lshl_b32 s64, s20, 12\n"
    "s_lshl_b32 s65, s20, 10\n"
    "s_and_b32 s82, s20, 1\n"
    "v_lshrrev_b32 v209, 4, v208\n"
    "v_and_b32 v210, 15, v208\n"
    "s_lshl_b32 s71, s82, 2\n"
    "v_or_b32 v211, s71, v209\n"
    "v_and_b32 v212, 7, v208\n"
    "v_xor_b32 v211, v211, v212\n"
    "v_lshrrev_b32 v213, 3, v208\n"
    "v_mul_u32_u24 v213, 1280, v213\n"
    "v_lshl_add_u32 v211, v211, 4, v213\n"
    "s_mul_i32 s79, s20, 10240\n"
    "v_add_u32 v20, s79, v211\n"
    "v_lshlrev_b32 v211, 1, v209\n"
    "v_xor_b32 v211, v211, v212\n"
    "v_lshl_add_u32 v211, v211, 4, v213\n"
    "v_accvgpr_write_b32 a210, v211\n"
    "s_lshl_b32 s69, s24, 7\n"
    "s_lshl_b32 s67, s24, 19\n"
    "s_mov_b32 s68, 0\n"
    "s_add_u32 s70, s65, 98304\n"
    "v_lshlrev_b32 v213, 2, v209\n"
    "v_xor_b32 v213, v213, v210\n"
    "v_lshlrev_b32 v213, 4, v213\n"
    "v_lshl_add_u32 v211, v209, 13, v213\n"
    "s_lshl_b32 s71, s82, 17\n"
    "v_add_u32 v18, s71, v211\n"
    "v_xor_b32 v211, 32, v18\n"
    "v_add_u32 v19, 63488, v211\n"
    "s_lshl_b32 s26, s36, 2\n"
    "s_add_u32 s26, s26, s25\n"
    "s_mul_i32 s15, s21, 122880\n"
    "s_lshl_b32 s27, s26, 8\n"
    "s_waitcnt lgkmcnt(0)\n"
    "s_add_u32 s40, s4, s15\n"
    "s_addc_u32 s41, s5, 0\n"
    "s_mov_b32 m0, s70\n"
    "s_add_u32 s71, s69, 40960\n"
    "buffer_load_dwordx4 v20, s[40:43], s69 offen offset:0  lds\n"
    "s_add_u32 m0, s70, 4096\n"
    "s_add_u32 s72, s69, 81920\n"
    "buffer_load_dwordx4 v20, s[40:43], s71 offen offset:0  lds\n"
    "s_add_u32 m0, s70, 8192\n"
    "s_add_u32 s69, s69, 128\n"
    "buffer_load_dwordx4 v20, s[40:43], s72 offen offset:0  lds\n"
    "s_cmp_eq_u32 s69, 1280\n"
    "s_cselect_b32 s69, 0, s69\n"
    "s_xor_b32 s70, s70, 16384\n"
    "s_add_u32 s44, s10, s27\n"
    "s_addc_u32 s45, s11, 0\n"
    "s_add_u32 m0, s64, 65536\n"
    "s_add_u32 s71, s67, s68\n"
    "buffer_load_dwordx4 v18, s[44:47], s71 offen offset:0  lds\n"
    "s_add_u32 s72, s71, 31744\n"
    "buffer_load_dwordx4 v19, s[44:47], s71 offen offset:2048  lds\n"
    "s_add_u32 s67, s67, 262144\n"
    "buffer_load_dwordx4 v18, s[44:47], s72 offen offset:1024  lds\n"
    "buffer_load_dwordx4 v19, s[44:47], s72 offen offset:3072  lds\n"
    "s_add_u32 m0, s64, 81920\n"
    "s_add_u32 s71, s67, s68\n"
    "buffer_load_dwordx4 v18, s[44:47], s71 offen offset:0  lds\n"
    "s_add_u32 s72, s71, 31744\n"
    "buffer_load_dwordx4 v19, s[44:47], s71 offen offset:2048  lds\n"
    "s_add_u32 s67, s67, 262144\n"
    "buffer_load_dwordx4 v18, s[44:47], s72 offen offset:1024  lds\n"
    "s_cmp_eq_u32 s67, 5242880\n"
    "s_cselect_b32 s67, 0, s67\n"
    "buffer_load_dwordx4 v19, s[44:47], s72 offen offset:3072  lds\n"
    "s_lshl_b32 s15, s21, 5\n"
    "s_add_u32 s15, s15, s26\n"
    "s_lshl_b32 s15, s15, 17\n"
    "s_add_u32 s48, s6, s15\n"
    "s_addc_u32 s49, s7, 0\n"
    "s_add_u32 s52, s8, s15\n"
    "s_addc_u32 s53, s9, 0\n"
    "s_mul_i32 s15, s36, 655360\n"
    "s_add_u32 s56, s12, s15\n"
    "s_addc_u32 s57, s13, 0\n"
    "s_lshl_b32 s26, s14, 4\n"
    "s_add_u32 s26, s26, s21\n"
    "s_mul_i32 s15, s26, 1015808\n"
    "s_add_u32 s60, s16, s15\n"
    "s_addc_u32 s61, s17, 0\n"
    "s_lshl_b32 s26, s26, 2\n"
    "s_add_u32 s26, s26, s20\n"
    "s_lshl_b32 s26, s26, 11\n"
    "s_add_u32 s26, s26, 41943040\n"
    "s_add_u32 s92, s16, s26\n"
    "s_addc_u32 s93, s17, 0\n"
    "s_mul_i32 s15, s21, 245760\n"
    "s_mul_i32 s14, s14, 1280\n"
    "s_add_u32 s15, s15, s14\n"
    "s_add_u32 s26, s18, s15\n"
    "s_addc_u32 s27, s19, 0\n"
    "s_mov_b32 s50, 393216\n"
    "s_mov_b32 s51, 0x20000\n"
    "s_mov_b32 s54, 393216\n"
    "s_mov_b32 s55, 0x20000\n"
    "s_mov_b32 s58, 655360\n"
    "s_mov_b32 s59, 0x20000\n"
    "s_mov_b32 s62, 1015808\n"
    "s_mov_b32 s63, 0x20000\n"
    "s_mov_b32 s94, 2048\n"
    "s_mov_b32 s95, 0x20000\n"
    "s_lshl_b32 s71, s82, 12\n"
    "v_lshlrev_b32 v212, 8, v209\n"
    "v_add_u32 v212, s71, v212\n"
    "v_add_u32 v14, v212, v213\n"
    "v_xor_b32 v15, 32, v14\n"
    "s_lshl_b32 s71, s82, 3\n"
    "v_or_b32 v211, s71, v209\n"
    "v_xor_b32 v211, v211, v210\n"
    "v_lshl_add_u32 v12, v211, 4, v212\n"
    "v_xor_b32 v13, 64, v12\n"
    "s_lshl_b32 s73, s25, 13\n"
    "s_mov_b32 s74, 0\n"
    "s_getpc_b64 s[88:89]\n"
    "L_pc_%=:\n"
    "s_and_b32 s88, s88, 0xffffff80\n"
    "s_lshl_b32 s85, s22, 11\n"
    "s_lshl_b32 s71, s20, 9\n"
    "s_add_u32 s85, s85, s71\n"
    "v_mbcnt_lo_u32_b32 v37, -1, 0\n"
    "v_and_b32 v37, 3, v37\n"
    "v_lshlrev_b32 v37, 7, v37\n"
    "v_add_u32 v37, s85, v37\n"
    "v_min_u32 v37, L_end_%=-L_pc_%=-4, v37\n"
    "global_load_dword a215, v37, s[88:89]\n"
    "v_bfe_u32 v214, v208, 2, 2\n"
    "v_and_b32 v215, 3, v208\n"
    "s_lshr_b32 s72, s20, 1\n"
    "s_lshl_b32 s72, s72, 13\n"
    "v_mul_u32_u24 v211, 320, v214\n"
    "v_lshl_add_u32 v211, v209, 11, v211\n"
    "v_and_b32 v212, 1, v209\n"
    "v_lshl_add_u32 v211, v212, 5, v211\n"
    "v_lshl_add_u32 v211, v215, 3, v211\n"
    "s_add_u32 s71, s72, 32768\n"
    "v_add_u32 v0, s71, v211\n"
    "v_xor_b32 v1, 32, v0\n"
    "v_xor_b32 v2, 64, v0\n"
    "v_xor_b32 v3, 96, v0\n"
    "v_xor_b32 v4, 128, v0\n"
    "v_xor_b32 v5, 160, v0\n"
    "v_xor_b32 v6, 192, v0\n"
    "v_xor_b32 v7, 224, v0\n"
    "v_add_u32 v240, 98304, v0\n"
    "v_add_u32 v241, 98304, v1\n"
    "v_add_u32 v242, 98304, v2\n"
    "v_add_u32 v243, 98304, v3\n"
    "v_add_u32 v244, 98304, v4\n"
    "v_add_u32 v245, 98304, v5\n"
    "v_add_u32 v246, 98304, v6\n"
    "v_add_u32 v247, 98304, v7\n"
    "v_lshlrev_b32 v212, 1, v212\n"
    "v_lshrrev_b32 v211, 1, v209\n"
    "v_or_b32 v211, v211, v212\n"
    "v_xor_b32 v211, v211, v210\n"
    "v_lshlrev_b32 v211, 4, v211\n"
    "v_lshl_add_u32 v211, v214, 11, v211\n"
    "v_lshl_add_u32 v211, v215, 8, v211\n"
    "v_add_u32 v8, s72, v211\n"
    "v_xor_b32 v9, 64, v8\n"
    "v_xor_b32 v10, 128, v8\n"
    "v_xor_b32 v11, 192, v8\n"
    "v_lshrrev_b32 v211, 1, v214\n"
    "v_or_b32 v211, v211, v212\n"
    "v_xor_b32 v211, s20, v211\n"
    "v_lshlrev_b32 v211, 5, v211\n"
    "v_lshl_add_u32 v211, v209, 9, v211\n"
    "v_lshl_add_u32 v211, v214, 7, v211\n"
    "v_lshl_add_u32 v211, v215, 3, v211\n"
    "v_add_u32 v211, 114688, v211\n"
    "v_accvgpr_write_b32 a208, v211\n"
    "v_lshrrev_b32 v211, 1, v210\n"
    "v_xor_b32 v211, v211, v209\n"
    "v_lshlrev_b32 v211, 4, v211\n"
    "v_lshl_add_u32 v211, v210, 7, v211\n"
    "s_mul_i32 s71, s82, 6144\n"
    "s_add_u32 s71, s71, 98304\n"
    "v_add_u32 v16, s71, v211\n"
    "v_xor_b32 v17, 64, v16\n"
    "v_lshlrev_b32 v211, 4, v208\n"
    "v_add_u32 v211, 65536, v211\n"
    "v_accvgpr_write_b32 a209, v211\n"
    "v_mbcnt_lo_u32_b32 v212, -1, 0\n"
    "v_mbcnt_hi_u32_b32 v212, -1, v212\n"
    "v_mul_u32_u24 v212, 513, v212\n"
    "v_and_b32 v212, 0x7007, v212\n"
    "s_lshl_b32 s38, s36, 5\n"
    "v_lshl_add_u32 v211, v212, 2, s38\n"
    "s_lshl_b32 s38, s36, 2\n"
    "v_lshl_add_u32 v212, v212, 5, s38\n"
    "buffer_load_dword a211, v212, s[92:95], 0 offen offset:256 sc1\n"
    "v_accvgpr_write_b32 a213, v211\n"
    "v_accvgpr_write_b32 a214, v212\n"
    "v_mov_b32 v213, 0\n"
    "v_mbcnt_lo_u32_b32 v214, -1, 0\n"
    "v_mbcnt_hi_u32_b32 v214, -1, v214\n"
    "v_mul_u32_u24 v214, 513, v214\n"
    "v_and_b32 v214, 0x7007, v214\n"
    "s_lshl_b32 s38, s36, 2\n"
    "v_lshl_add_u32 v211, v214, 12, s38\n"
    "v_lshlrev_b32 v214, 2, v214\n"
    "buffer_store_dword v213, v211, s[92:95], 0 offen offset:512 sc1\n"
    "s_lshl_b32 s71, s21, 2\n"
    "s_add_u32 s71, s71, s20\n"
    "s_mul_i32 s71, s71, 5120\n"
    "s_add_u32 s81, s71, 327680\n"
    "s_mul_i32 s80, s20, 40960\n"
    "s_add_u32 s80, s80, 327680\n"
    "s_mov_b32 s76, 0x3e0293ee\n"
    "s_mov_b32 s77, 0x41000000\n"
    "s_lshr_b32 s71, s20, 1\n"
    "s_mul_i32 s71, s71, 24576\n"
    "s_mul_i32 s72, s82, 3072\n"
    "s_add_u32 s86, s71, s72\n"
    "v_mov_b32 v21, 0\n"
    "v_mov_b32 v22, 0\n"
    "v_mov_b32 v23, 0\n"
    "v_mov_b32 v184, 0\n"
    "v_mov_b32 v185, 0\n"
    "v_mov_b32 v186, 0\n"
    "v_mov_b32 v187, 0\n"
    "s_nop 1\n"
    "v_mfma_f32_32x32x16_bf16 v[88:103], v[184:187], v[184:187], 0\n"
    "v_mfma_f32_32x32x16_bf16 v[104:119], v[184:187], v[184:187], 0\n"
    "v_mfma_f32_32x32x16_bf16 v[120:135], v[184:187], v[184:187], 0\n"
    "v_mfma_f32_32x32x16_bf16 v[136:151], v[184:187], v[184:187], 0\n"
    "v_mfma_f32_32x32x16_bf16 v[152:167], v[184:187], v[184:187], 0\n"
    "v_mfma_f32_32x32x16_bf16 v[168:183], v[184:187], v[184:187], 0\n"
    "s_waitcnt vmcnt(7)\n"
    "s_barrier\n"
    "ds_read_b128 a[160:163], v16 offset:0\n"
    "ds_read_b128 a[164:167], v16 offset:2048\n"
    "ds_read_b128 a[168:171], v16 offset:4096\n"
    "ds_read_b64_tr_b16 a[96:97], v0 offset:32768\n"
    "ds_read_b64_tr_b16 a[98:99], v0 offset:33792\n"
    "ds_read_b64_tr_b16 a[100:101], v1 offset:32768\n"
    "ds_read_b64_tr_b16 a[102:103], v1 offset:33792\n"
    "ds_read_b64_tr_b16 a[104:105], v2 offset:32768\n"
    "ds_read_b64_tr_b16 a[106:107], v2 offset:33792\n"
    "ds_read_b64_tr_b16 a[108:109], v3 offset:32768\n"
    "ds_read_b64_tr_b16 a[110:111], v3 offset:33792\n"
    "ds_read_b64_tr_b16 a[112:113], v4 offset:32768\n"
    "ds_read_b64_tr_b16 a[114:115], v4 offset:33792\n"
    "ds_read_b64_tr_b16 a[116:117], v5 offset:32768\n"
    "ds_read_b64_tr_b16 a[118:119], v5 offset:33792\n"
    "ds_read_b64_tr_b16 a[120:121], v6 offset:32768\n"
    "ds_read_b64_tr_b16 a[122:123], v6 offset:33792\n"
    "ds_read_b64_tr_b16 a[124:125], v7 offset:32768\n"
    "ds_read_b64_tr_b16 a[126:127], v7 offset:33792\n"
    "ds_read_b128 a[172:175], v17 offset:0\n"
    "ds_read_b128 a[176:179], v17 offset:2048\n"
    "ds_read_b128 a[180:183], v17 offset:4096\n"
    "s_add_u32 m0, s64, 131072\n"
    "s_add_u32 s71, s67, s68\n"
    "buffer_load_dwordx4 v18, s[44:47], s71 offen offset:0  lds\n"
    "s_add_u32 s72, s71, 31744\n"
    "buffer_load_dwordx4 v19, s[44:47], s71 offen offset:2048  lds\n"
    "s_add_u32 s67, s67, 262144\n"
    "buffer_load_dwordx4 v18, s[44:47], s72 offen offset:1024  lds\n"
    "buffer_load_dwordx4 v19, s[44:47], s72 offen offset:3072  lds\n"
    "s_mov_b32 m0, s70\n"
    "s_add_u32 s71, s69, 40960\n"
    "buffer_load_dwordx4 v20, s[40:43], s69 offen offset:0  lds\n"
    "s_add_u32 m0, s70, 4096\n"
    "s_add_u32 s72, s69, 81920\n"
    "buffer_load_dwordx4 v20, s[40:43], s71 offen offset:0  lds\n"
    "s_add_u32 m0, s70, 8192\n"
    "s_add_u32 s69, s69, 128\n"
    "buffer_load_dwordx4 v20, s[40:43], s72 offen offset:0  lds\n"
    "s_cmp_eq_u32 s69, 1280\n"
    "s_cselect_b32 s69, 0, s69\n"
    "s_xor_b32 s70, s70, 16384\n"
    "s_add_u32 m0, s64, 147456\n"
    "s_add_u32 s71, s67, s68\n"
    "buffer_load_dwordx4 v18, s[44:47], s71 offen offset:0  lds\n"
    "s_add_u32 s72, s71, 31744\n"
    "buffer_load_dwordx4 v19, s[44:47], s71 offen offset:2048  lds\n"
    "s_add_u32 s67, s67, 262144\n"
    "buffer_load_dwordx4 v18, s[44:47], s72 offen offset:1024  lds\n"
    "s_cmp_eq_u32 s67, 5242880\n"
    "s_cselect_b32 s67, 0, s67\n"
    "buffer_load_dwordx4 v19, s[44:47], s72 offen offset:3072  lds\n"
    "s_mov_b32 s66, 0\n"
    "L_r0_%=:\n"
    "s_cmp_eq_u32 s66, 4\n"
    "s_cselect_b32 s68, 512, 0\n"
    "s_cmp_lg_u32 s66, s23\n"
    "s_cbranch_scc1 L_kv_%=\n"
    "s_add_u32 m0, s64, 16384\n"
    "s_add_u32 s75, s73, s74\n"
    "buffer_load_dwordx4 v12, s[48:51], s75 offen offset:0 nt lds\n"
    "buffer_load_dwordx4 v12, s[48:51], s75 offen offset:1024 nt lds\n"
    "buffer_load_dwordx4 v13, s[48:51], s75 offen offset:2048 nt lds\n"
    "buffer_load_dwordx4 v13, s[48:51], s75 offen offset:3072 nt lds\n"
    "s_add_u32 m0, s64, 49152\n"
    "s_add_u32 s73, s73, 8192\n"
    "buffer_load_dwordx4 v14, s[52:55], s75 offen offset:0 nt lds\n"
    "buffer_load_dwordx4 v14, s[52:55], s75 offen offset:1024 nt lds\n"
    "buffer_load_dwordx4 v15, s[52:55], s75 offen offset:2048 nt lds\n"
    "buffer_load_dwordx4 v15, s[52:55], s75 offen offset:3072 nt lds\n"
    "s_and_b32 s73, s73, 0x1ffff\n"
    "s_add_u32 m0, s64, 0\n"
    "s_add_u32 s75, s73, s74\n"
    "buffer_load_dwordx4 v12, s[48:51], s75 offen offset:0 nt lds\n"
    "buffer_load_dwordx4 v12, s[48:51], s75 offen offset:1024 nt lds\n"
    "buffer_load_dwordx4 v13, s[48:51], s75 offen offset:2048 nt lds\n"
    "buffer_load_dwordx4 v13, s[48:51], s75 offen offset:3072 nt lds\n"
    "s_add_u32 m0, s64, 32768\n"
    "s_add_u32 s73, s73, 8192\n"
    "buffer_load_dwordx4 v14, s[52:55], s75 offen offset:0 nt lds\n"
    "buffer_load_dwordx4 v14, s[52:55], s75 offen offset:1024 nt lds\n"
    "buffer_load_dwordx4 v15, s[52:55], s75 offen offset:2048 nt lds\n"
    "buffer_load_dwordx4 v15, s[52:55], s75 offen offset:3072 nt lds\n"
    "s_and_b32 s73, s73, 0x1ffff\n"
    "L_kv_%=:\n"
    "s_waitcnt vmcnt(11) lgkmcnt(0)\n"
    "s_barrier\n"
    "v_mfma_f32_16x16x32_bf16 v[88:91], a[96:99], a[160:163], v[88:91]\n"
    "ds_read_b64_tr_b16 a[128:129], v0 offset:49152\n"
    "v_mfma_f32_16x16x32_bf16 v[92:95], a[96:99], a[164:167], v[92:95]\n"
    "ds_read_b64_tr_b16 a[130:131], v0 offset:50176\n"
    "v_mfma_f32_16x16x32_bf16 v[96:99], a[96:99], a[168:171], v[96:99]\n"
    "s_add_u32 m0, s64, 65536\n"
    "s_add_u32 s71, s67, s68\n"
    "buffer_load_dwordx4 v18, s[44:47], s71 offen offset:0  lds\n"
    "v_mfma_f32_16x16x32_bf16 v[108:111], a[100:103], a[168:171], v[108:111]\n"
    "s_add_u32 s72, s71, 31744\n"
    "buffer_load_dwordx4 v19, s[44:47], s71 offen offset:2048  lds\n"
    "ds_read_b64_tr_b16 a[132:133], v1 offset:49152\n"
    "v_mfma_f32_16x16x32_bf16 v[104:107], a[100:103], a[164:167], v[104:107]\n"
    "ds_read_b64_tr_b16 a[134:135], v1 offset:50176\n"
    "v_mfma_f32_16x16x32_bf16 v[100:103], a[100:103], a[160:163], v[100:103]\n"
    "ds_read_b64_tr_b16 a[136:137], v2 offset:49152\n"
    "v_mfma_f32_16x16x32_bf16 v[112:115], a[104:107], a[160:163], v[112:115]\n"
    "s_add_u32 s67, s67, 262144\n"
    "buffer_load_dwordx4 v18, s[44:47], s72 offen offset:1024  lds\n"
    "ds_read_b64_tr_b16 a[138:139], v2 offset:50176\n"
    "v_mfma_f32_16x16x32_bf16 v[116:119], a[104:107], a[164:167], v[116:119]\n"
    "ds_read_b64_tr_b16 a[140:141], v3 offset:49152\n"
    "v_mfma_f32_16x16x32_bf16 v[120:123], a[104:107], a[168:171], v[120:123]\n"
    "ds_read_b64_tr_b16 a[142:143], v3 offset:50176\n"
    "buffer_load_dwordx4 v19, s[44:47], s72 offen offset:3072  lds\n"
    "v_mfma_f32_16x16x32_bf16 v[132:135], a[108:111], a[168:171], v[132:135]\n"
    "ds_read_b64_tr_b16 a[144:145], v4 offset:49152\n"
    "v_mfma_f32_16x16x32_bf16 v[128:131], a[108:111], a[164:167], v[128:131]\n"
    "ds_read_b64_tr_b16 a[146:147], v4 offset:50176\n"
    "v_mfma_f32_16x16x32_bf16 v[124:127], a[108:111], a[160:163], v[124:127]\n"
    "ds_read_b64_tr_b16 a[148:149], v5 offset:49152\n"
    "v_mfma_f32_16x16x32_bf16 v[136:139], a[112:115], a[160:163], v[136:139]\n"
    "ds_read_b64_tr_b16 a[150:151], v5 offset:50176\n"
    "v_mfma_f32_16x16x32_bf16 v[140:143], a[112:115], a[164:167], v[140:143]\n"
    "s_mov_b32 m0, s70\n"
    "s_add_u32 s71, s69, 40960\n"
    "buffer_load_dwordx4 v20, s[40:43], s69 offen offset:0  lds\n"
    "v_mfma_f32_16x16x32_bf16 v[144:147], a[112:115], a[168:171], v[144:147]\n"
    "ds_read_b64_tr_b16 a[152:153], v6 offset:49152\n"
    "v_mfma_f32_16x16x32_bf16 v[156:159], a[116:119], a[168:171], v[156:159]\n"
    "ds_read_b64_tr_b16 a[154:155], v6 offset:50176\n"
    "v_mfma_f32_16x16x32_bf16 v[152:155], a[116:119], a[164:167], v[152:155]\n"
    "s_add_u32 m0, s70, 4096\n"
    "s_add_u32 s72, s69, 81920\n"
    "buffer_load_dwordx4 v20, s[40:43], s71 offen offset:0  lds\n"
    "ds_read_b64_tr_b16 a[156:157], v7 offset:49152\n"
    "v_mfma_f32_16x16x32_bf16 v[148:151], a[116:119], a[160:163], v[148:151]\n"
    "ds_read_b64_tr_b16 a[158:159], v7 offset:50176\n"
    "v_mfma_f32_16x16x32_bf16 v[160:163], a[120:123], a[160:163], v[160:163]\n"
    "s_add_u32 m0, s70, 8192\n"
    "s_add_u32 s69, s69, 128\n"
    "buffer_load_dwordx4 v20, s[40:43], s72 offen offset:0  lds\n"
    "s_cmp_eq_u32 s69, 1280\n"
    "s_cselect_b32 s69, 0, s69\n"
    "s_xor_b32 s70, s70, 16384\n"
    "v_mfma_f32_16x16x32_bf16 v[164:167], a[120:123], a[164:167], v[164:167]\n"
    "v_mfma_f32_16x16x32_bf16 v[168:171], a[120:123], a[168:171], v[168:171]\n"
    "v_mfma_f32_16x16x32_bf16 v[180:183], a[124:127], a[168:171], v[180:183]\n"
    "v_mfma_f32_16x16x32_bf16 v[176:179], a[124:127], a[164:167], v[176:179]\n"
    "v_mfma_f32_16x16x32_bf16 v[172:175], a[124:127], a[160:163], v[172:175]\n"
    "s_waitcnt vmcnt(11) lgkmcnt(0)\n"
    "s_barrier\n"
    "v_mfma_f32_16x16x32_bf16 v[88:91], a[128:131], a[172:175], v[88:91]\n"
    "ds_read_b128 a[184:187], v16 offset:16384\n"
    "v_mfma_f32_16x16x32_bf16 v[92:95], a[128:131], a[176:179], v[92:95]\n"
    "ds_read_b64_tr_b16 a[96:97], v240 offset:0\n"
    "v_mfma_f32_16x16x32_bf16 v[96:99], a[128:131], a[180:183], v[96:99]\n"
    "s_add_u32 m0, s64, 81920\n"
    "s_add_u32 s71, s67, s68\n"
    "buffer_load_dwordx4 v18, s[44:47], s71 offen offset:0  lds\n"
    "ds_read_b64_tr_b16 a[98:99], v240 offset:1024\n"
    "v_mfma_f32_16x16x32_bf16 v[108:111], a[132:135], a[180:183], v[108:111]\n"
    "ds_read_b64_tr_b16 a[100:101], v241 offset:0\n"
    "v_mfma_f32_16x16x32_bf16 v[104:107], a[132:135], a[176:179], v[104:107]\n"
    "ds_read_b128 a[188:191], v16 offset:18432\n"
    "v_mfma_f32_16x16x32_bf16 v[100:103], a[132:135], a[172:175], v[100:103]\n"
    "ds_read_b64_tr_b16 a[102:103], v241 offset:1024\n"
    "ds_read_b64_tr_b16 a[104:105], v242 offset:0\n"
    "v_mfma_f32_16x16x32_bf16 v[112:115], a[136:139], a[172:175], v[112:115]\n"
    "s_add_u32 s72, s71, 31744\n"
    "buffer_load_dwordx4 v19, s[44:47], s71 offen offset:2048  lds\n"
    "ds_read_b64_tr_b16 a[106:107], v242 offset:1024\n"
    "v_mfma_f32_16x16x32_bf16 v[116:119], a[136:139], a[176:179], v[116:119]\n"
    "ds_read_b128 a[192:195], v16 offset:20480\n"
    "v_mfma_f32_16x16x32_bf16 v[120:123], a[136:139], a[180:183], v[120:123]\n"
    "ds_read_b64_tr_b16 a[108:109], v243 offset:0\n"
    "v_mfma_f32_16x16x32_bf16 v[132:135], a[140:143], a[180:183], v[132:135]\n"
    "ds_read_b64_tr_b16 a[110:111], v243 offset:1024\n"
    "v_mfma_f32_16x16x32_bf16 v[128:131], a[140:143], a[176:179], v[128:131]\n"
    "ds_read_b64_tr_b16 a[112:113], v244 offset:0\n"
    "ds_read_b128 a[196:199], v17 offset:16384\n"
    "v_mfma_f32_16x16x32_bf16 v[124:127], a[140:143], a[172:175], v[124:127]\n"
    "s_add_u32 s67, s67, 262144\n"
    "buffer_load_dwordx4 v18, s[44:47], s72 offen offset:1024  lds\n"
    "ds_read_b64_tr_b16 a[114:115], v244 offset:1024\n"
    "v_mfma_f32_16x16x32_bf16 v[136:139], a[144:147], a[172:175], v[136:139]\n"
    "ds_read_b64_tr_b16 a[116:117], v245 offset:0\n"
    "v_mfma_f32_16x16x32_bf16 v[140:143], a[144:147], a[176:179], v[140:143]\n"
    "ds_read_b64_tr_b16 a[118:119], v245 offset:1024\n"
    "v_mfma_f32_16x16x32_bf16 v[144:147], a[144:147], a[180:183], v[144:147]\n"
    "ds_read_b128 a[200:203], v17 offset:18432\n"
    "ds_read_b64_tr_b16 a[120:121], v246 offset:0\n"
    "v_mfma_f32_16x16x32_bf16 v[156:159], a[148:151], a[180:183], v[156:159]\n"
    "ds_read_b64_tr_b16 a[122:123], v246 offset:1024\n"
    "v_mfma_f32_16x16x32_bf16 v[152:155], a[148:151], a[176:179], v[152:155]\n"
    "ds_read_b64_tr_b16 a[124:125], v247 offset:0\n"
    "v_mfma_f32_16x16x32_bf16 v[148:151], a[148:151], a[172:175], v[148:151]\n"
    "ds_read_b128 a[204:207], v17 offset:20480\n"
    "ds_read_b64_tr_b16 a[126:127], v247 offset:1024\n"
    "v_mfma_f32_16x16x32_bf16 v[160:163], a[152:155], a[172:175], v[160:163]\n"
    "s_cmp_eq_u32 s67, 5242880\n"
    "s_cselect_b32 s67, 0, s67\n"
    "buffer_load_dwordx4 v19, s[44:47], s72 offen offset:3072  lds\n"
    "v_mfma_f32_16x16x32_bf16 v[164:167], a[152:155], a[176:179], v[164:167]\n"
    "v_mfma_f32_16x16x32_bf16 v[168:171], a[152:155], a[180:183], v[168:171]\n"
    "v_mfma_f32_16x16x32_bf16 v[180:183], a[156:159], a[180:183], v[180:183]\n"
    "v_mfma_f32_16x16x32_bf16 v[176:179], a[156:159], a[176:179], v[176:179]\n"
    "v_mfma_f32_16x16x32_bf16 v[172:175], a[156:159], a[172:175], v[172:175]\n"
    "s_waitcnt vmcnt(11) lgkmcnt(0)\n"
    "s_barrier\n"
    "v_mfma_f32_16x16x32_bf16 v[88:91], a[96:99], a[184:187], v[88:91]\n"
    "ds_read_b64_tr_b16 a[128:129], v240 offset:16384\n"
    "v_mfma_f32_16x16x32_bf16 v[92:95], a[96:99], a[188:191], v[92:95]\n"
    "ds_read_b64_tr_b16 a[130:131], v240 offset:17408\n"
    "v_mfma_f32_16x16x32_bf16 v[96:99], a[96:99], a[192:195], v[96:99]\n"
    "s_add_u32 m0, s64, 131072\n"
    "s_add_u32 s71, s67, s68\n"
    "buffer_load_dwordx4 v18, s[44:47], s71 offen offset:0  lds\n"
    "v_mfma_f32_16x16x32_bf16 v[108:111], a[100:103], a[192:195], v[108:111]\n"
    "s_add_u32 s72, s71, 31744\n"
    "buffer_load_dwordx4 v19, s[44:47], s71 offen offset:2048  lds\n"
    "ds_read_b64_tr_b16 a[132:133], v241 offset:16384\n"
    "v_mfma_f32_16x16x32_bf16 v[104:107], a[100:103], a[188:191], v[104:107]\n"
    "ds_read_b64_tr_b16 a[134:135], v241 offset:17408\n"
    "v_mfma_f32_16x16x32_bf16 v[100:103], a[100:103], a[184:187], v[100:103]\n"
    "ds_read_b64_tr_b16 a[136:137], v242 offset:16384\n"
    "v_mfma_f32_16x16x32_bf16 v[112:115], a[104:107], a[184:187], v[112:115]\n"
    "s_add_u32 s67, s67, 262144\n"
    "buffer_load_dwordx4 v18, s[44:47], s72 offen offset:1024  lds\n"
    "ds_read_b64_tr_b16 a[138:139], v242 offset:17408\n"
    "v_mfma_f32_16x16x32_bf16 v[116:119], a[104:107], a[188:191], v[116:119]\n"
    "ds_read_b64_tr_b16 a[140:141], v243 offset:16384\n"
    "v_mfma_f32_16x16x32_bf16 v[120:123], a[104:107], a[192:195], v[120:123]\n"
    "ds_read_b64_tr_b16 a[142:143], v243 offset:17408\n"
    "buffer_load_dwordx4 v19, s[44:47], s72 offen offset:3072  lds\n"
    "v_mfma_f32_16x16x32_bf16 v[132:135], a[108:111], a[192:195], v[132:135]\n"
    "ds_read_b64_tr_b16 a[144:145], v244 offset:16384\n"
    "v_mfma_f32_16x16x32_bf16 v[128:131], a[108:111], a[188:191], v[128:131]\n"
    "ds_read_b64_tr_b16 a[146:147], v244 offset:17408\n"
    "v_mfma_f32_16x16x32_bf16 v[124:127], a[108:111], a[184:187], v[124:127]\n"
    "ds_read_b64_tr_b16 a[148:149], v245 offset:16384\n"
    "v_mfma_f32_16x16x32_bf16 v[136:139], a[112:115], a[184:187], v[136:139]\n"
    "ds_read_b64_tr_b16 a[150:151], v245 offset:17408\n"
    "v_mfma_f32_16x16x32_bf16 v[140:143], a[112:115], a[188:191], v[140:143]\n"
    "s_mov_b32 m0, s70\n"
    "s_add_u32 s71, s69, 40960\n"
    "buffer_load_dwordx4 v20, s[40:43], s69 offen offset:0  lds\n"
    "v_mfma_f32_16x16x32_bf16 v[144:147], a[112:115], a[192:195], v[144:147]\n"
    "ds_read_b64_tr_b16 a[152:153], v246 offset:16384\n"
    "v_mfma_f32_16x16x32_bf16 v[156:159], a[116:119], a[192:195], v[156:159]\n"
    "ds_read_b64_tr_b16 a[154:155], v246 offset:17408\n"
    "v_mfma_f32_16x16x32_bf16 v[152:155], a[116:119], a[188:191], v[152:155]\n"
    "s_add_u32 m0, s70, 4096\n"
    "s_add_u32 s72, s69, 81920\n"
    "buffer_load_dwordx4 v20, s[40:43], s71 offen offset:0  lds\n"
    "ds_read_b64_tr_b16 a[156:157], v247 offset:16384\n"
    "v_mfma_f32_16x16x32_bf16 v[148:151], a[116:119], a[184:187], v[148:151]\n"
    "ds_read_b64_tr_b16 a[158:159], v247 offset:17408\n"
    "v_mfma_f32_16x16x32_bf16 v[160:163], a[120:123], a[184:187], v[160:163]\n"
    "s_add_u32 m0, s70, 8192\n"
    "s_add_u32 s69, s69, 128\n"
    "buffer_load_dwordx4 v20, s[40:43], s72 offen offset:0  lds\n"
    "s_cmp_eq_u32 s69, 1280\n"
    "s_cselect_b32 s69, 0, s69\n"
    "s_xor_b32 s70, s70, 16384\n"
    "v_mfma_f32_16x16x32_bf16 v[164:167], a[120:123], a[188:191], v[164:167]\n"
    "v_mfma_f32_16x16x32_bf16 v[168:171], a[120:123], a[192:195], v[168:171]\n"
    "v_mfma_f32_16x16x32_bf16 v[180:183], a[124:127], a[192:195], v[180:183]\n"
    "v_mfma_f32_16x16x32_bf16 v[176:179], a[124:127], a[188:191], v[176:179]\n"
    "v_mfma_f32_16x16x32_bf16 v[172:175], a[124:127], a[184:187], v[172:175]\n"
    "s_waitcnt vmcnt(11) lgkmcnt(0)\n"
    "s_barrier\n"
    "v_mfma_f32_16x16x32_bf16 v[88:91], a[128:131], a[196:199], v[88:91]\n"
    "ds_read_b128 a[160:163], v16 offset:0\n"
    "v_mfma_f32_16x16x32_bf16 v[92:95], a[128:131], a[200:203], v[92:95]\n"
    "ds_read_b64_tr_b16 a[96:97], v0 offset:32768\n"
    "v_mfma_f32_16x16x32_bf16 v[96:99], a[128:131], a[204:207], v[96:99]\n"
    "s_add_u32 m0, s64, 147456\n"
    "s_add_u32 s71, s67, s68\n"
    "buffer_load_dwordx4 v18, s[44:47], s71 offen offset:0  lds\n"
    "ds_read_b64_tr_b16 a[98:99], v0 offset:33792\n"
    "v_mfma_f32_16x16x32_bf16 v[108:111], a[132:135], a[204:207], v[108:111]\n"
    "ds_read_b64_tr_b16 a[100:101], v1 offset:32768\n"
    "v_mfma_f32_16x16x32_bf16 v[104:107], a[132:135], a[200:203], v[104:107]\n"
    "ds_read_b128 a[164:167], v16 offset:2048\n"
    "v_mfma_f32_16x16x32_bf16 v[100:103], a[132:135], a[196:199], v[100:103]\n"
    "ds_read_b64_tr_b16 a[102:103], v1 offset:33792\n"
    "ds_read_b64_tr_b16 a[104:105], v2 offset:32768\n"
    "v_mfma_f32_16x16x32_bf16 v[112:115], a[136:139], a[196:199], v[112:115]\n"
    "s_add_u32 s72, s71, 31744\n"
    "buffer_load_dwordx4 v19, s[44:47], s71 offen offset:2048  lds\n"
    "ds_read_b64_tr_b16 a[106:107], v2 offset:33792\n"
    "v_mfma_f32_16x16x32_bf16 v[116:119], a[136:139], a[200:203], v[116:119]\n"
    "ds_read_b128 a[168:171], v16 offset:4096\n"
    "v_mfma_f32_16x16x32_bf16 v[120:123], a[136:139], a[204:207], v[120:123]\n"
    "ds_read_b64_tr_b16 a[108:109], v3 offset:32768\n"
    "v_mfma_f32_16x16x32_bf16 v[132:135], a[140:143], a[204:207], v[132:135]\n"
    "ds_read_b64_tr_b16 a[110:111], v3 offset:33792\n"
    "v_mfma_f32_16x16x32_bf16 v[128:131], a[140:143], a[200:203], v[128:131]\n"
    "ds_read_b64_tr_b16 a[112:113], v4 offset:32768\n"
    "ds_read_b128 a[172:175], v17 offset:0\n"
    "v_mfma_f32_16x16x32_bf16 v[124:127], a[140:143], a[196:199], v[124:127]\n"
    "s_add_u32 s67, s67, 262144\n"
    "buffer_load_dwordx4 v18, s[44:47], s72 offen offset:1024  lds\n"
    "ds_read_b64_tr_b16 a[114:115], v4 offset:33792\n"
    "v_mfma_f32_16x16x32_bf16 v[136:139], a[144:147], a[196:199], v[136:139]\n"
    "ds_read_b64_tr_b16 a[116:117], v5 offset:32768\n"
    "v_mfma_f32_16x16x32_bf16 v[140:143], a[144:147], a[200:203], v[140:143]\n"
    "ds_read_b64_tr_b16 a[118:119], v5 offset:33792\n"
    "v_mfma_f32_16x16x32_bf16 v[144:147], a[144:147], a[204:207], v[144:147]\n"
    "ds_read_b128 a[176:179], v17 offset:2048\n"
    "ds_read_b64_tr_b16 a[120:121], v6 offset:32768\n"
    "v_mfma_f32_16x16x32_bf16 v[156:159], a[148:151], a[204:207], v[156:159]\n"
    "ds_read_b64_tr_b16 a[122:123], v6 offset:33792\n"
    "v_mfma_f32_16x16x32_bf16 v[152:155], a[148:151], a[200:203], v[152:155]\n"
    "ds_read_b64_tr_b16 a[124:125], v7 offset:32768\n"
    "v_mfma_f32_16x16x32_bf16 v[148:151], a[148:151], a[196:199], v[148:151]\n"
    "ds_read_b128 a[180:183], v17 offset:4096\n"
    "ds_read_b64_tr_b16 a[126:127], v7 offset:33792\n"
    "v_mfma_f32_16x16x32_bf16 v[160:163], a[152:155], a[196:199], v[160:163]\n"
    "s_cmp_eq_u32 s67, 5242880\n"
    "s_cselect_b32 s67, 0, s67\n"
    "buffer_load_dwordx4 v19, s[44:47], s72 offen offset:3072  lds\n"
    "v_mfma_f32_16x16x32_bf16 v[164:167], a[152:155], a[200:203], v[164:167]\n"
    "v_mfma_f32_16x16x32_bf16 v[168:171], a[152:155], a[204:207], v[168:171]\n"
    "v_mfma_f32_16x16x32_bf16 v[180:183], a[156:159], a[204:207], v[180:183]\n"
    "v_mfma_f32_16x16x32_bf16 v[176:179], a[156:159], a[200:203], v[176:179]\n"
    "v_mfma_f32_16x16x32_bf16 v[172:175], a[156:159], a[196:199], v[172:175]\n"
    "s_add_u32 s66, s66, 1\n"
    "s_cmp_lg_u32 s66, 5\n"
    "s_cbranch_scc1 L_r0_%=\n"
    "s_mov_b32 s37, 0\n"
    "v_accvgpr_read_b32 v36, a211\n"
    "v_accvgpr_read_b32 v37, a213\n"
    "v_add_u32 v36, 1, v36\n"
    "v_accvgpr_write_b32 a211, v36\n"
    "buffer_store_dword v36, v37, s[92:95], 0 offen offset:0 sc1\n"
    "s_waitcnt vmcnt(11) lgkmcnt(0)\n"
    "s_barrier\n"
    "v_mfma_f32_16x16x32_bf16 a[0:3], a[96:99], a[160:163], 0\n"
    "ds_read_b64_tr_b16 a[128:129], v0 offset:49152\n"
    "v_mfma_f32_16x16x32_bf16 a[4:7], a[96:99], a[164:167], 0\n"
    "ds_read_b64_tr_b16 a[130:131], v0 offset:50176\n"
    "v_mfma_f32_16x16x32_bf16 a[8:11], a[96:99], a[168:171], 0\n"
    "s_add_u32 m0, s64, 65536\n"
    "s_add_u32 s71, s67, s68\n"
    "buffer_load_dwordx4 v18, s[44:47], s71 offen offset:0  lds\n"
    "ds_read_b64_tr_b16 a[132:133], v1 offset:49152\n"
    "v_mfma_f32_16x16x32_bf16 a[20:23], a[100:103], a[168:171], 0\n"
    "s_add_u32 s72, s71, 31744\n"
    "buffer_load_dwordx4 v19, s[44:47], s71 offen offset:2048  lds\n"
    "ds_read_b64_tr_b16 a[134:135], v1 offset:50176\n"
    "v_mfma_f32_16x16x32_bf16 a[16:19], a[100:103], a[164:167], 0\n"
    "ds_read_b64_tr_b16 a[136:137], v2 offset:49152\n"
    "ds_read_b64_tr_b16 a[138:139], v2 offset:50176\n"
    "v_mfma_f32_16x16x32_bf16 a[12:15], a[100:103], a[160:163], 0\n"
    "ds_read_b64_tr_b16 a[140:141], v3 offset:49152\n"
    "v_mul_f32 v88, s76, v88\n"
    "v_mfma_f32_16x16x32_bf16 a[24:27], a[104:107], a[160:163], 0\n"
    "ds_read_b64_tr_b16 a[142:143], v3 offset:50176\n"
    "v_mul_f32 v89, s76, v89\n"
    "v_mfma_f32_16x16x32_bf16 a[28:31], a[104:107], a[164:167], 0\n"
    "ds_read_b64_tr_b16 a[144:145], v4 offset:49152\n"
    "ds_read_b64_tr_b16 a[146:147], v4 offset:50176\n"
    "v_mfma_f32_16x16x32_bf16 a[32:35], a[104:107], a[168:171], 0\n"
    "s_add_u32 s67, s67, 262144\n"
    "buffer_load_dwordx4 v18, s[44:47], s72 offen offset:1024  lds\n"
    "ds_read_b64_tr_b16 a[148:149], v5 offset:49152\n"
    "v_mul_f32 v90, s76, v90\n"
    "v_mul_f32 v91, s76, v91\n"
    "v_mfma_f32_16x16x32_bf16 a[44:47], a[108:111], a[168:171], 0\n"
    "ds_read_b64_tr_b16 a[150:151], v5 offset:50176\n"
    "v_mul_f32 v100, s76, v100\n"
    "v_mul_f32 v101, s76, v101\n"
    "v_mfma_f32_16x16x32_bf16 a[40:43], a[108:111], a[164:167], 0\n"
    "ds_read_b64_tr_b16 a[152:153], v6 offset:49152\n"
    "ds_read_b64_tr_b16 a[154:155], v6 offset:50176\n"
    "buffer_load_dwordx4 v19, s[44:47], s72 offen offset:3072  lds\n"
    "v_mul_f32 v102, s76, v102\n"
    "v_mul_f32 v103, s76, v103\n"
    "v_mfma_f32_16x16x32_bf16 a[36:39], a[108:111], a[160:163], 0\n"
    "ds_read_b64_tr_b16 a[156:157], v7 offset:49152\n"
    "v_cvt_pk_bf16_f32 v40, v88, v89\n"
    "v_cvt_pk_bf16_f32 v42, v100, v101\n"
    "v_mfma_f32_16x16x32_bf16 a[48:51], a[112:115], a[160:163], 0\n"
    "ds_read_b64_tr_b16 a[158:159], v7 offset:50176\n"
    "v_cvt_pk_bf16_f32 v41, v90, v91\n"
    "v_cvt_pk_bf16_f32 v43, v102, v103\n"
    "v_mfma_f32_16x16x32_bf16 a[52:55], a[112:115], a[164:167], 0\n"
    "s_mov_b32 m0, s70\n"
    "s_add_u32 s71, s69, 40960\n"
    "buffer_load_dwordx4 v20, s[40:43], s69 offen offset:0  lds\n"
    "ds_read_b128 v[208:211], v8 offset:16384\n"
    "ds_read_b128 v[212:215], v8 offset:17408\n"
    "v_mul_f32 v92, s76, v92\n"
    "v_mul_f32 v93, s76, v93\n"
    "v_mfma_f32_16x16x32_bf16 a[56:59], a[112:115], a[168:171], 0\n"
    "ds_read_b128 v[216:219], v9 offset:16384\n"
    "v_mul_f32 v94, s76, v94\n"
    "v_mfma_f32_16x16x32_bf16 a[68:71], a[116:119], a[168:171], 0\n"
    "ds_read_b128 v[220:223], v9 offset:17408\n"
    "v_mul_f32 v95, s76, v95\n"
    "v_mul_f32 v104, s76, v104\n"
    "v_mfma_f32_16x16x32_bf16 a[64:67], a[116:119], a[164:167], 0\n"
    "s_add_u32 m0, s70, 4096\n"
    "s_add_u32 s72, s69, 81920\n"
    "buffer_load_dwordx4 v20, s[40:43], s71 offen offset:0  lds\n"
    "ds_read_b128 v[224:227], v10 offset:16384\n"
    "ds_read_b128 v[228:231], v10 offset:17408\n"
    "v_mul_f32 v105, s76, v105\n"
    "v_mul_f32 v106, s76, v106\n"
    "v_mfma_f32_16x16x32_bf16 a[60:63], a[116:119], a[160:163], 0\n"
    "ds_read_b128 v[232:235], v11 offset:16384\n"
    "ds_read_b128 v[236:239], v11 offset:17408\n"
    "v_mul_f32 v107, s76, v107\n"
    "v_cvt_pk_bf16_f32 v44, v92, v93\n"
    "v_mfma_f32_16x16x32_bf16 a[72:75], a[120:123], a[160:163], 0\n"
    "v_cvt_pk_bf16_f32 v46, v104, v105\n"
    "v_cvt_pk_bf16_f32 v45, v94, v95\n"
    "v_mfma_f32_16x16x32_bf16 a[76:79], a[120:123], a[164:167], 0\n"
    "s_add_u32 m0, s70, 8192\n"
    "s_add_u32 s69, s69, 128\n"
    "buffer_load_dwordx4 v20, s[40:43], s72 offen offset:0  lds\n"
    "s_cmp_eq_u32 s69, 1280\n"
    "s_cselect_b32 s69, 0, s69\n"
    "s_xor_b32 s70, s70, 16384\n"
    "v_cvt_pk_bf16_f32 v47, v106, v107\n"
    "v_permlane16_swap_b32 v40, v42\n"
    "v_mfma_f32_16x16x32_bf16 a[80:83], a[120:123], a[168:171], 0\n"
    "v_permlane16_swap_b32 v41, v43\n"
    "v_mul_f32 v96, s76, v96\n"
    "v_mfma_f32_16x16x32_bf16 a[92:95], a[124:127], a[168:171], 0\n"
    "v_mul_f32 v97, s76, v97\n"
    "v_mul_f32 v98, s76, v98\n"
    "v_mfma_f32_16x16x32_bf16 a[88:91], a[124:127], a[164:167], 0\n"
    "v_mul_f32 v99, s76, v99\n"
    "v_mul_f32 v108, s76, v108\n"
    "v_mfma_f32_16x16x32_bf16 a[84:87], a[124:127], a[160:163], 0\n"
    "v_mul_f32 v109, s76, v109\n"
    "v_mul_f32 v110, s76, v110\n"
    "s_waitcnt vmcnt(11) lgkmcnt(0)\n"
    "s_barrier\n"
    "v_mfma_f32_16x16x32_bf16 a[0:3], a[128:131], a[172:175], a[0:3]\n"
    "v_mul_f32 v111, s76, v111\n"
    "ds_read_b128 a[184:187], v16 offset:16384\n"
    "v_mfma_f32_16x16x32_bf16 a[4:7], a[128:131], a[176:179], a[4:7]\n"
    "ds_read_b64_tr_b16 a[96:97], v240 offset:0\n"
    "v_cvt_pk_bf16_f32 v48, v96, v97\n"
    "v_cvt_pk_bf16_f32 v50, v108, v109\n"
    "v_mfma_f32_16x16x32_bf16 a[8:11], a[128:131], a[180:183], a[8:11]\n"
    "s_add_u32 m0, s64, 81920\n"
    "s_add_u32 s71, s67, s68\n"
    "buffer_load_dwordx4 v18, s[44:47], s71 offen offset:0  lds\n"
    "ds_read_b64_tr_b16 a[98:99], v240 offset:1024\n"
    "ds_read_b64_tr_b16 a[100:101], v241 offset:0\n"
    "v_cvt_pk_bf16_f32 v49, v98, v99\n"
    "v_cvt_pk_bf16_f32 v51, v110, v111\n"
    "v_mfma_f32_16x16x32_bf16 a[20:23], a[132:135], a[180:183], a[20:23]\n"
    "ds_read_b128 a[188:191], v16 offset:18432\n"
    "ds_read_b64_tr_b16 a[102:103], v241 offset:1024\n"
    "v_permlane16_swap_b32 v44, v46\n"
    "v_permlane16_swap_b32 v45, v47\n"
    "v_mfma_f32_16x16x32_bf16 a[16:19], a[132:135], a[176:179], a[16:19]\n"
    "ds_read_b64_tr_b16 a[104:105], v242 offset:0\n"
    "ds_read_b64_tr_b16 a[106:107], v242 offset:1024\n"
    "v_mul_f32 v112, s76, v112\n"
    "v_mul_f32 v113, s76, v113\n"
    "v_mfma_f32_16x16x32_bf16 a[12:15], a[132:135], a[172:175], a[12:15]\n"
    "s_add_u32 s72, s71, 31744\n"
    "buffer_load_dwordx4 v19, s[44:47], s71 offen offset:2048  lds\n"
    "ds_read_b128 a[192:195], v16 offset:20480\n"
    "ds_read_b64_tr_b16 a[108:109], v243 offset:0\n"
    "v_mul_f32 v114, s76, v114\n"
    "v_mul_f32 v115, s76, v115\n"
    "v_mfma_f32_16x16x32_bf16 a[24:27], a[136:139], a[172:175], a[24:27]\n"
    "ds_read_b64_tr_b16 a[110:111], v243 offset:1024\n"
    "ds_read_b64_tr_b16 a[112:113], v244 offset:0\n"
    "v_mul_f32 v124, s76, v124\n"
    "v_mfma_f32_16x16x32_bf16 a[28:31], a[136:139], a[176:179], a[28:31]\n"
    "ds_read_b128 a[196:199], v17 offset:16384\n"
    "ds_read_b64_tr_b16 a[114:115], v244 offset:1024\n"
    "v_mul_f32 v125, s76, v125\n"
    "v_mul_f32 v126, s76, v126\n"
    "v_mfma_f32_16x16x32_bf16 a[32:35], a[136:139], a[180:183], a[32:35]\n"
    "s_add_u32 s67, s67, 262144\n"
    "buffer_load_dwordx4 v18, s[44:47], s72 offen offset:1024  lds\n"
    "ds_read_b64_tr_b16 a[116:117], v245 offset:0\n"
    "ds_read_b64_tr_b16 a[118:119], v245 offset:1024\n"
    "v_mul_f32 v127, s76, v127\n"
    "v_cvt_pk_bf16_f32 v52, v112, v113\n"
    "v_mfma_f32_16x16x32_bf16 a[44:47], a[140:143], a[180:183], a[44:47]\n"
    "ds_read_b128 a[200:203], v17 offset:18432\n"
    "v_cvt_pk_bf16_f32 v54, v124, v125\n"
    "v_cvt_pk_bf16_f32 v53, v114, v115\n"
    "v_mfma_f32_16x16x32_bf16 a[40:43], a[140:143], a[176:179], a[40:43]\n"
    "ds_read_b64_tr_b16 a[120:121], v246 offset:0\n"
    "ds_read_b64_tr_b16 a[122:123], v246 offset:1024\n"
    "v_cvt_pk_bf16_f32 v55, v126, v127\n"
    "v_permlane16_swap_b32 v48, v50\n"
    "v_mfma_f32_16x16x32_bf16 a[36:39], a[140:143], a[172:175], a[36:39]\n"
    "s_cmp_eq_u32 s67, 5242880\n"
    "s_cselect_b32 s67, 0, s67\n"
    "buffer_load_dwordx4 v19, s[44:47], s72 offen offset:3072  lds\n"
    "ds_read_b64_tr_b16 a[124:125], v247 offset:0\n"
    "ds_read_b128 a[204:207], v17 offset:20480\n"
    "v_permlane16_swap_b32 v49, v51\n"
    "v_mul_f32 v116, s76, v116\n"
    "v_mfma_f32_16x16x32_bf16 a[48:51], a[144:147], a[172:175], a[48:51]\n"
    "ds_read_b64_tr_b16 a[126:127], v247 offset:1024\n"
    "ds_read_b64_tr_b16 a[216:217], v0 offset:16384\n"
    "v_mul_f32 v117, s76, v117\n"
    "v_mul_f32 v118, s76, v118\n"
    "v_mfma_f32_16x16x32_bf16 a[52:55], a[144:147], a[176:179], a[52:55]\n"
    "ds_read_b64_tr_b16 a[218:219], v0 offset:17408\n"
    "ds_read_b64_tr_b16 a[220:221], v1 offset:16384\n"
    "v_mul_f32 v119, s76, v119\n"
    "v_mul_f32 v128, s76, v128\n"
    "v_mfma_f32_16x16x32_bf16 a[56:59], a[144:147], a[180:183], a[56:59]\n"
    "s_add_u32 m0, s64, 16384\n"
    "s_add_u32 s75, s73, s74\n"
    "buffer_load_dwordx4 v12, s[48:51], s75 offen offset:0 nt lds\n"
    "ds_read_b64_tr_b16 a[222:223], v1 offset:17408\n"
    "ds_read_b64_tr_b16 a[224:225], v2 offset:16384\n"
    "v_mul_f32 v129, s76, v129\n"
    "v_mfma_f32_16x16x32_bf16 a[68:71], a[148:151], a[180:183], a[68:71]\n"
    "ds_read_b64_tr_b16 a[226:227], v2 offset:17408\n"
    "ds_read_b64_tr_b16 a[228:229], v3 offset:16384\n"
    "v_mul_f32 v130, s76, v130\n"
    "v_mul_f32 v131, s76, v131\n"
    "v_mfma_f32_16x16x32_bf16 a[64:67], a[148:151], a[176:179], a[64:67]\n"
    "ds_read_b64_tr_b16 a[230:231], v3 offset:17408\n"
    "ds_read_b64_tr_b16 a[232:233], v4 offset:16384\n"
    "v_cvt_pk_bf16_f32 v56, v116, v117\n"
    "v_cvt_pk_bf16_f32 v58, v128, v129\n"
    "v_mfma_f32_16x16x32_bf16 a[60:63], a[148:151], a[172:175], a[60:63]\n"
    "ds_read_b64_tr_b16 a[234:235], v4 offset:17408\n"
    "ds_read_b64_tr_b16 a[236:237], v5 offset:16384\n"
    "ds_read_b64_tr_b16 a[238:239], v5 offset:17408\n"
    "v_cvt_pk_bf16_f32 v57, v118, v119\n"
    "v_cvt_pk_bf16_f32 v59, v130, v131\n"
    "v_mfma_f32_16x16x32_bf16 a[72:75], a[152:155], a[172:175], a[72:75]\n"
    "buffer_load_dwordx4 v12, s[48:51], s75 offen offset:1024 nt lds\n"
    "v_permlane16_swap_b32 v52, v54\n"
    "v_permlane16_swap_b32 v53, v55\n"
    "v_mfma_f32_16x16x32_bf16 a[76:79], a[152:155], a[176:179], a[76:79]\n"
    "v_mul_f32 v120, s76, v120\n"
    "v_mul_f32 v121, s76, v121\n"
    "v_mfma_f32_16x16x32_bf16 a[80:83], a[152:155], a[180:183], a[80:83]\n"
    "buffer_load_dwordx4 v13, s[48:51], s75 offen offset:2048 nt lds\n"
    "v_mul_f32 v122, s76, v122\n"
    "v_mul_f32 v123, s76, v123\n"
    "v_mfma_f32_16x16x32_bf16 a[92:95], a[156:159], a[180:183], a[92:95]\n"
    "v_mul_f32 v132, s76, v132\n"
    "v_mul_f32 v133, s76, v133\n"
    "v_mfma_f32_16x16x32_bf16 a[88:91], a[156:159], a[176:179], a[88:91]\n"
    "buffer_load_dwordx4 v13, s[48:51], s75 offen offset:3072 nt lds\n"
    "v_mul_f32 v134, s76, v134\n"
    "v_mfma_f32_16x16x32_bf16 a[84:87], a[156:159], a[172:175], a[84:87]\n"
    "v_mul_f32 v135, s76, v135\n"
    "v_cvt_pk_bf16_f32 v60, v120, v121\n"
    "s_waitcnt vmcnt(15) lgkmcnt(0)\n"
    "s_barrier\n"
    "v_mfma_f32_16x16x32_bf16 a[0:3], a[96:99], a[184:187], a[0:3]\n"
    "v_cvt_pk_bf16_f32 v62, v132, v133\n"
    "v_cvt_pk_bf16_f32 v61, v122, v123\n"
    "v_mfma_f32_16x16x32_bf16 a[4:7], a[96:99], a[188:191], a[4:7]\n"
    "ds_read_b64_tr_b16 a[128:129], v240 offset:16384\n"
    "ds_read_b64_tr_b16 a[130:131], v240 offset:17408\n"
    "v_cvt_pk_bf16_f32 v63, v134, v135\n"
    "v_permlane16_swap_b32 v56, v58\n"
    "v_mfma_f32_16x16x32_bf16 a[8:11], a[96:99], a[192:195], a[8:11]\n"
    "ds_read_b64_tr_b16 a[132:133], v241 offset:16384\n"
    "v_permlane16_swap_b32 v57, v59\n"
    "v_mul_f32 v136, s76, v136\n"
    "v_mfma_f32_16x16x32_bf16 a[20:23], a[100:103], a[192:195], a[20:23]\n"
    "ds_read_b64_tr_b16 a[134:135], v241 offset:17408\n"
    "v_mul_f32 v137, s76, v137\n"
    "v_mul_f32 v138, s76, v138\n"
    "v_mfma_f32_16x16x32_bf16 a[16:19], a[100:103], a[188:191], a[16:19]\n"
    "ds_read_b64_tr_b16 a[136:137], v242 offset:16384\n"
    "v_mul_f32 v139, s76, v139\n"
    "v_mul_f32 v148, s76, v148\n"
    "v_mfma_f32_16x16x32_bf16 a[12:15], a[100:103], a[184:187], a[12:15]\n"
    "ds_read_b64_tr_b16 a[138:139], v242 offset:17408\n"
    "v_mul_f32 v149, s76, v149\n"
    "v_mfma_f32_16x16x32_bf16 a[24:27], a[104:107], a[184:187], a[24:27]\n"
    "ds_read_b64_tr_b16 a[140:141], v243 offset:16384\n"
    "v_mul_f32 v150, s76, v150\n"
    "v_mul_f32 v151, s76, v151\n"
    "v_mfma_f32_16x16x32_bf16 a[28:31], a[104:107], a[188:191], a[28:31]\n"
    "ds_read_b64_tr_b16 a[142:143], v243 offset:17408\n"
    "v_cvt_pk_bf16_f32 v64, v136, v137\n"
    "v_cvt_pk_bf16_f32 v66, v148, v149\n"
    "v_mfma_f32_16x16x32_bf16 a[32:35], a[104:107], a[192:195], a[32:35]\n"
    "ds_read_b64_tr_b16 a[144:145], v244 offset:16384\n"
    "v_cvt_pk_bf16_f32 v65, v138, v139\n"
    "v_cvt_pk_bf16_f32 v67, v150, v151\n"
    "v_mfma_f32_16x16x32_bf16 a[44:47], a[108:111], a[192:195], a[44:47]\n"
    "ds_read_b64_tr_b16 a[146:147], v244 offset:17408\n"
    "v_permlane16_swap_b32 v60, v62\n"
    "v_permlane16_swap_b32 v61, v63\n"
    "v_mfma_f32_16x16x32_bf16 a[40:43], a[108:111], a[188:191], a[40:43]\n"
    "ds_read_b64_tr_b16 a[148:149], v245 offset:16384\n"
    "ds_read_b64_tr_b16 a[150:151], v245 offset:17408\n"
    "v_mul_f32 v140, s76, v140\n"
    "v_mul_f32 v141, s76, v141\n"
    "v_mfma_f32_16x16x32_bf16 a[36:39], a[108:111], a[184:187], a[36:39]\n"
    "ds_read_b64_tr_b16 a[152:153], v246 offset:16384\n"
    "v_mul_f32 v142, s76, v142\n"
    "v_mul_f32 v143, s76, v143\n"
    "v_mfma_f32_16x16x32_bf16 a[48:51], a[112:115], a[184:187], a[48:51]\n"
    "ds_read_b64_tr_b16 a[154:155], v246 offset:17408\n"
    "v_mul_f32 v152, s76, v152\n"
    "v_mul_f32 v153, s76, v153\n"
    "v_mfma_f32_16x16x32_bf16 a[52:55], a[112:115], a[188:191], a[52:55]\n"
    "ds_read_b64_tr_b16 a[156:157], v247 offset:16384\n"
    "v_mul_f32 v154, s76, v154\n"
    "v_mfma_f32_16x16x32_bf16 a[56:59], a[112:115], a[192:195], a[56:59]\n"
    "ds_read_b64_tr_b16 a[158:159], v247 offset:17408\n"
    "v_mul_f32 v155, s76, v155\n"
    "v_cvt_pk_bf16_f32 v68, v140, v141\n"
    "v_mfma_f32_16x16x32_bf16 a[68:71], a[116:119], a[192:195], a[68:71]\n"
    "ds_read_b64_tr_b16 a[160:161], v6 offset:16384\n"
    "v_cvt_pk_bf16_f32 v70, v152, v153\n"
    "v_cvt_pk_bf16_f32 v69, v142, v143\n"
    "v_mfma_f32_16x16x32_bf16 a[64:67], a[116:119], a[188:191], a[64:67]\n"
    "ds_read_b64_tr_b16 a[162:163], v6 offset:17408\n"
    "v_cvt_pk_bf16_f32 v71, v154, v155\n"
    "v_permlane16_swap_b32 v64, v66\n"
    "v_mfma_f32_16x16x32_bf16 a[60:63], a[116:119], a[184:187], a[60:63]\n"
    "ds_read_b64_tr_b16 a[164:165], v7 offset:16384\n"
    "ds_read_b64_tr_b16 a[166:167], v7 offset:17408\n"
    "v_permlane16_swap_b32 v65, v67\n"
    "v_mul_f32 v144, s76, v144\n"
    "v_mfma_f32_16x16x32_bf16 a[72:75], a[120:123], a[184:187], a[72:75]\n"
    "v_mul_f32 v145, s76, v145\n"
    "v_mul_f32 v146, s76, v146\n"
    "v_mfma_f32_16x16x32_bf16 a[76:79], a[120:123], a[188:191], a[76:79]\n"
    "v_mul_f32 v147, s76, v147\n"
    "v_mul_f32 v156, s76, v156\n"
    "v_mfma_f32_16x16x32_bf16 a[80:83], a[120:123], a[192:195], a[80:83]\n"
    "v_mul_f32 v157, s76, v157\n"
    "v_mul_f32 v158, s76, v158\n"
    "v_mfma_f32_16x16x32_bf16 a[92:95], a[124:127], a[192:195], a[92:95]\n"
    "v_mul_f32 v159, s76, v159\n"
    "v_cvt_pk_bf16_f32 v72, v144, v145\n"
    "v_mfma_f32_16x16x32_bf16 a[88:91], a[124:127], a[188:191], a[88:91]\n"
    "v_cvt_pk_bf16_f32 v74, v156, v157\n"
    "v_cvt_pk_bf16_f32 v73, v146, v147\n"
    "v_mfma_f32_16x16x32_bf16 a[84:87], a[124:127], a[184:187], a[84:87]\n"
    "v_cvt_pk_bf16_f32 v75, v158, v159\n"
    "v_permlane16_swap_b32 v68, v70\n"
    "s_waitcnt lgkmcnt(0)\n"
    "s_barrier\n"
    "v_mfma_f32_16x16x32_bf16 a[0:3], a[128:131], a[196:199], a[0:3]\n"
    "v_permlane16_swap_b32 v69, v71\n"
    "v_mul_f32 v160, s76, v160\n"
    "v_mfma_f32_16x16x32_bf16 a[4:7], a[128:131], a[200:203], a[4:7]\n"
    "v_mul_f32 v161, s76, v161\n"
    "v_mul_f32 v162, s76, v162\n"
    "v_mfma_f32_16x16x32_bf16 a[8:11], a[128:131], a[204:207], a[8:11]\n"
    "s_add_u32 m0, s64, 49152\n"
    "s_add_u32 s73, s73, 8192\n"
    "buffer_load_dwordx4 v14, s[52:55], s75 offen offset:0 nt lds\n"
    "v_mul_f32 v163, s76, v163\n"
    "v_mfma_f32_16x16x32_bf16 a[20:23], a[132:135], a[204:207], a[20:23]\n"
    "v_mul_f32 v172, s76, v172\n"
    "v_mul_f32 v173, s76, v173\n"
    "v_mfma_f32_16x16x32_bf16 a[16:19], a[132:135], a[200:203], a[16:19]\n"
    "buffer_load_dwordx4 v14, s[52:55], s75 offen offset:1024 nt lds\n"
    "v_mul_f32 v174, s76, v174\n"
    "v_mul_f32 v175, s76, v175\n"
    "v_mfma_f32_16x16x32_bf16 a[12:15], a[132:135], a[196:199], a[12:15]\n"
    "v_cvt_pk_bf16_f32 v76, v160, v161\n"
    "buffer_load_dwordx4 v15, s[52:55], s75 offen offset:2048 nt lds\n"
    "v_mfma_f32_16x16x32_bf16 a[24:27], a[136:139], a[196:199], a[24:27]\n"
    "v_cvt_pk_bf16_f32 v78, v172, v173\n"
    "v_cvt_pk_bf16_f32 v77, v162, v163\n"
    "v_mfma_f32_16x16x32_bf16 a[28:31], a[136:139], a[200:203], a[28:31]\n"
    "buffer_load_dwordx4 v15, s[52:55], s75 offen offset:3072 nt lds\n"
    "v_cvt_pk_bf16_f32 v79, v174, v175\n"
    "v_permlane16_swap_b32 v72, v74\n"
    "v_mfma_f32_16x16x32_bf16 a[32:35], a[136:139], a[204:207], a[32:35]\n"
    "v_permlane16_swap_b32 v73, v75\n"
    "v_mul_f32 v164, s76, v164\n"
    "v_mfma_f32_16x16x32_bf16 a[44:47], a[140:143], a[204:207], a[44:47]\n"
    "s_and_b32 s73, s73, 0x1ffff\n"
    "s_add_u32 m0, s64, 131072\n"
    "s_add_u32 s75, s73, s74\n"
    "buffer_load_dwordx4 v12, s[48:51], s75 offen offset:0 nt lds\n"
    "v_mul_f32 v165, s76, v165\n"
    "v_mul_f32 v166, s76, v166\n"
    "v_mfma_f32_16x16x32_bf16 a[40:43], a[140:143], a[200:203], a[40:43]\n"
    "v_mul_f32 v167, s76, v167\n"
    "v_mul_f32 v176, s76, v176\n"
    "v_mfma_f32_16x16x32_bf16 a[36:39], a[140:143], a[196:199], a[36:39]\n"
    "v_mul_f32 v177, s76, v177\n"
    "v_mul_f32 v178, s76, v178\n"
    "v_mfma_f32_16x16x32_bf16 a[48:51], a[144:147], a[196:199], a[48:51]\n"
    "buffer_load_dwordx4 v12, s[48:51], s75 offen offset:1024 nt lds\n"
    "v_mul_f32 v179, s76, v179\n"
    "v_cvt_pk_bf16_f32 v80, v164, v165\n"
    "v_mfma_f32_16x16x32_bf16 a[52:55], a[144:147], a[200:203], a[52:55]\n"
    "buffer_load_dwordx4 v13, s[48:51], s75 offen offset:2048 nt lds\n"
    "v_cvt_pk_bf16_f32 v82, v176, v177\n"
    "v_mfma_f32_16x16x32_bf16 a[56:59], a[144:147], a[204:207], a[56:59]\n"
    "v_cvt_pk_bf16_f32 v81, v166, v167\n"
    "v_cvt_pk_bf16_f32 v83, v178, v179\n"
    "v_mfma_f32_16x16x32_bf16 a[68:71], a[148:151], a[204:207], a[68:71]\n"
    "buffer_load_dwordx4 v13, s[48:51], s75 offen offset:3072 nt lds\n"
    "v_permlane16_swap_b32 v76, v78\n"
    "v_permlane16_swap_b32 v77, v79\n"
    "v_mfma_f32_16x16x32_bf16 a[64:67], a[148:151], a[200:203], a[64:67]\n"
    "v_mul_f32 v168, s76, v168\n"
    "v_mul_f32 v169, s76, v169\n"
    "v_mfma_f32_16x16x32_bf16 a[60:63], a[148:151], a[196:199], a[60:63]\n"
    "s_add_u32 m0, s64, 147456\n"
    "s_add_u32 s73, s73, 8192\n"
    "buffer_load_dwordx4 v14, s[52:55], s75 offen offset:0 nt lds\n"
    "v_mul_f32 v170, s76, v170\n"
    "v_mul_f32 v171, s76, v171\n"
    "v_mfma_f32_16x16x32_bf16 a[72:75], a[152:155], a[196:199], a[72:75]\n"
    "v_mul_f32 v180, s76, v180\n"
    "v_mul_f32 v181, s76, v181\n"
    "v_mfma_f32_16x16x32_bf16 a[76:79], a[152:155], a[200:203], a[76:79]\n"
    "buffer_load_dwordx4 v14, s[52:55], s75 offen offset:1024 nt lds\n"
    "v_mul_f32 v182, s76, v182\n"
    "v_mul_f32 v183, s76, v183\n"
    "v_mfma_f32_16x16x32_bf16 a[80:83], a[152:155], a[204:207], a[80:83]\n"
    "v_cvt_pk_bf16_f32 v84, v168, v169\n"
    "v_cvt_pk_bf16_f32 v86, v180, v181\n"
    "v_mfma_f32_16x16x32_bf16 a[92:95], a[156:159], a[204:207], a[92:95]\n"
    "buffer_load_dwordx4 v15, s[52:55], s75 offen offset:2048 nt lds\n"
    "v_cvt_pk_bf16_f32 v85, v170, v171\n"
    "v_mfma_f32_16x16x32_bf16 a[88:91], a[156:159], a[200:203], a[88:91]\n"
    "buffer_load_dwordx4 v15, s[52:55], s75 offen offset:3072 nt lds\n"
    "s_and_b32 s73, s73, 0x1ffff\n"
    "v_cvt_pk_bf16_f32 v87, v182, v183\n"
    "v_permlane16_swap_b32 v80, v82\n"
    "v_permlane16_swap_b32 v81, v83\n"
    "s_nop 1\n"
    "v_permlane16_swap_b32 v84, v86\n"
    "v_permlane16_swap_b32 v85, v87\n"
    "v_mfma_f32_16x16x32_bf16 a[84:87], a[156:159], a[196:199], a[84:87]\n"
    "s_waitcnt vmcnt(20) lgkmcnt(0)\n"
    "s_barrier\n"
    "v_mfma_f32_16x16x32_bf16 v[184:187], v[208:211], v[40:43], 0\n"
    "ds_read_b128 a[96:99], v16 offset:0\n"
    "v_mfma_f32_16x16x32_bf16 v[188:191], v[212:215], v[40:43], 0\n"
    "s_mov_b32 m0, s70\n"
    "s_add_u32 s71, s69, 40960\n"
    "buffer_load_dwordx4 v20, s[40:43], s69 offen offset:0  lds\n"
    "v_mfma_f32_16x16x32_bf16 v[184:187], v[216:219], v[52:55], v[184:187]\n"
    "ds_read_b64_tr_b16 v[240:241], v0 offset:32768\n"
    "v_mfma_f32_16x16x32_bf16 v[188:191], v[220:223], v[52:55], v[188:191]\n"
    "ds_read_b64_tr_b16 v[242:243], v0 offset:33792\n"
    "v_mfma_f32_16x16x32_bf16 v[184:187], v[224:227], v[64:67], v[184:187]\n"
    "ds_read_b128 a[100:103], v16 offset:2048\n"
    "v_mfma_f32_16x16x32_bf16 v[188:191], v[228:231], v[64:67], v[188:191]\n"
    "ds_read_b64_tr_b16 v[244:245], v1 offset:32768\n"
    "v_mfma_f32_16x16x32_bf16 v[184:187], v[232:235], v[76:79], v[184:187]\n"
    "ds_read_b64_tr_b16 v[246:247], v1 offset:33792\n"
    "v_mfma_f32_16x16x32_bf16 v[188:191], v[236:239], v[76:79], v[188:191]\n"
    "s_add_u32 m0, s70, 4096\n"
    "s_add_u32 s72, s69, 81920\n"
    "buffer_load_dwordx4 v20, s[40:43], s71 offen offset:0  lds\n"
    "v_mfma_f32_16x16x32_bf16 v[192:195], v[208:211], v[44:47], 0\n"
    "s_nop 1\n"
    "ds_read_b128 a[104:107], v16 offset:4096\n"
    "v_mfma_f32_16x16x32_bf16 v[196:199], v[212:215], v[44:47], 0\n"
    "s_nop 1\n"
    "v_xor_b32 v16, 16384, v16\n"
    "v_mfma_f32_16x16x32_bf16 v[192:195], v[216:219], v[56:59], v[192:195]\n"
    "v_mfma_f32_16x16x32_bf16 v[196:199], v[220:223], v[56:59], v[196:199]\n"
    "v_max3_f32 v37, v184, v185, v186\n"
    "v_max3_f32 v36, v187, v188, v189\n"
    "v_mfma_f32_16x16x32_bf16 v[192:195], v[224:227], v[68:71], v[192:195]\n"
    "v_max3_f32 v37, v37, v190, v191\n"
    "v_max_f32 v37, v37, v36\n"
    "v_mfma_f32_16x16x32_bf16 v[196:199], v[228:231], v[68:71], v[196:199]\n"
    "v_mov_b32 v36, v37\n"
    "s_nop 1\n"
    "v_permlane32_swap_b32 v37, v36\n"
    "v_mfma_f32_16x16x32_bf16 v[192:195], v[232:235], v[80:83], v[192:195]\n"
    "ds_read_b64_tr_b16 v[248:249], v2 offset:32768\n"
    "v_max_f32 v37, v37, v36\n"
    "v_mfma_f32_16x16x32_bf16 v[196:199], v[236:239], v[80:83], v[196:199]\n"
    "v_mov_b32 v36, v37\n"
    "s_nop 1\n"
    "v_permlane16_swap_b32 v37, v36\n"
    "v_mfma_f32_16x16x32_bf16 v[200:203], v[208:211], v[48:51], 0\n"
    "s_add_u32 m0, s70, 8192\n"
    "s_add_u32 s69, s69, 128\n"
    "buffer_load_dwordx4 v20, s[40:43], s72 offen offset:0  lds\n"
    "s_cmp_eq_u32 s69, 1280\n"
    "s_cselect_b32 s69, 0, s69\n"
    "s_xor_b32 s70, s70, 16384\n"
    "ds_read_b64_tr_b16 v[250:251], v2 offset:33792\n"
    "v_max_f32 v37, v37, v36\n"
    "v_mfma_f32_16x16x32_bf16 v[204:207], v[212:215], v[48:51], 0\n"
    "v_sub_f32 v184, v184, v37\n"
    "v_sub_f32 v185, v185, v37\n"
    "v_mfma_f32_16x16x32_bf16 v[200:203], v[216:219], v[60:63], v[200:203]\n"
    "ds_read_b64_tr_b16 v[252:253], v3 offset:32768\n"
    "v_sub_f32 v186, v186, v37\n"
    "v_sub_f32 v187, v187, v37\n"
    "v_sub_f32 v188, v188, v37\n"
    "v_mfma_f32_16x16x32_bf16 v[204:207], v[220:223], v[60:63], v[204:207]\n"
    "v_sub_f32 v189, v189, v37\n"
    "v_sub_f32 v190, v190, v37\n"
    "v_sub_f32 v191, v191, v37\n"
    "v_mfma_f32_16x16x32_bf16 v[200:203], v[224:227], v[72:75], v[200:203]\n"
    "ds_read_b64_tr_b16 v[254:255], v3 offset:33792\n"
    "v_sub_f32 v24, 0, v37\n"
    "v_sub_f32 v25, 0, v37\n"
    "v_mfma_f32_16x16x32_bf16 v[204:207], v[228:231], v[72:75], v[204:207]\n"
    "ds_read_b64_tr_b16 a[240:241], v4 offset:32768\n"
    "v_sub_f32 v26, 0, v37\n"
    "v_sub_f32 v27, 0, v37\n"
    "v_exp_f32 v184, v184\n"
    "v_mfma_f32_16x16x32_bf16 v[200:203], v[232:235], v[84:87], v[200:203]\n"
    "v_exp_f32 v185, v185\n"
    "v_mfma_f32_16x16x32_bf16 v[204:207], v[236:239], v[84:87], v[204:207]\n"
    "s_waitcnt lgkmcnt(3)\n"
    "ds_read_b64_tr_b16 a[242:243], v4 offset:33792\n"
    "v_exp_f32 v186, v186\n"
    "v_mfma_f32_16x16x32_bf16 a[0:3], v[240:243], a[96:99], a[0:3]\n"
    "v_add_f32 v21, v21, v184\n"
    "v_exp_f32 v187, v187\n"
    "v_mfma_f32_16x16x32_bf16 a[4:7], v[240:243], a[100:103], a[4:7]\n"
    "ds_read_b64_tr_b16 a[244:245], v5 offset:32768\n"
    "v_add_f32 v21, v21, v185\n"
    "v_cvt_pk_bf16_f32 v184, v184, v185\n"
    "v_mfma_f32_16x16x32_bf16 a[8:11], v[240:243], a[104:107], a[8:11]\n"
    "ds_read_b64_tr_b16 a[246:247], v5 offset:33792\n"
    "v_exp_f32 v188, v188\n"
    "v_add_f32 v21, v21, v186\n"
    "v_mfma_f32_16x16x32_bf16 a[20:23], v[244:247], a[104:107], a[20:23]\n"
    "v_exp_f32 v189, v189\n"
    "v_mfma_f32_16x16x32_bf16 a[16:19], v[244:247], a[100:103], a[16:19]\n"
    "ds_read_b64_tr_b16 a[248:249], v6 offset:32768\n"
    "v_add_f32 v21, v21, v187\n"
    "v_cvt_pk_bf16_f32 v185, v186, v187\n"
    "v_exp_f32 v190, v190\n"
    "v_mfma_f32_16x16x32_bf16 a[12:15], v[244:247], a[96:99], a[12:15]\n"
    "v_add_f32 v21, v21, v188\n"
    "v_exp_f32 v191, v191\n"
    "v_mfma_f32_16x16x32_bf16 a[24:27], v[248:251], a[96:99], a[24:27]\n"
    "ds_read_b64_tr_b16 a[250:251], v6 offset:33792\n"
    "v_add_f32 v21, v21, v189\n"
    "v_mfma_f32_16x16x32_bf16 a[28:31], v[248:251], a[100:103], a[28:31]\n"
    "ds_read_b64_tr_b16 a[252:253], v7 offset:32768\n"
    "v_cvt_pk_bf16_f32 v186, v188, v189\n"
    "v_add_f32 v21, v21, v190\n"
    "v_add_f32 v21, v21, v191\n"
    "v_mfma_f32_16x16x32_bf16 a[32:35], v[248:251], a[104:107], a[32:35]\n"
    "s_waitcnt lgkmcnt(4)\n"
    "ds_read_b64_tr_b16 a[254:255], v7 offset:33792\n"
    "v_cvt_pk_bf16_f32 v187, v190, v191\n"
    "v_max3_f32 v37, v192, v193, v194\n"
    "v_mfma_f32_16x16x32_bf16 a[44:47], v[252:255], a[104:107], a[44:47]\n"
    "v_max3_f32 v36, v195, v196, v197\n"
    "v_max3_f32 v37, v37, v198, v199\n"
    "v_max_f32 v37, v37, v36\n"
    "v_mfma_f32_16x16x32_bf16 a[40:43], v[252:255], a[100:103], a[40:43]\n"
    "v_mov_b32 v36, v37\n"
    "s_nop 1\n"
    "v_permlane32_swap_b32 v37, v36\n"
    "v_mfma_f32_16x16x32_bf16 a[36:39], v[252:255], a[96:99], a[36:39]\n"
    "v_max_f32 v37, v37, v36\n"
    "v_mov_b32 v36, v37\n"
    "s_nop 1\n"
    "v_permlane16_swap_b32 v37, v36\n"
    "v_mfma_f32_16x16x32_bf16 a[48:51], a[240:243], a[96:99], a[48:51]\n"
    "v_max_f32 v37, v37, v36\n"
    "v_sub_f32 v192, v192, v37\n"
    "v_mfma_f32_16x16x32_bf16 a[52:55], a[240:243], a[100:103], a[52:55]\n"
    "v_sub_f32 v193, v193, v37\n"
    "v_sub_f32 v194, v194, v37\n"
    "v_mfma_f32_16x16x32_bf16 a[56:59], a[240:243], a[104:107], a[56:59]\n"
    "s_waitcnt lgkmcnt(1)\n"
    "v_sub_f32 v195, v195, v37\n"
    "v_sub_f32 v196, v196, v37\n"
    "v_sub_f32 v197, v197, v37\n"
    "v_mfma_f32_16x16x32_bf16 a[68:71], a[244:247], a[104:107], a[68:71]\n"
    "v_sub_f32 v198, v198, v37\n"
    "v_sub_f32 v199, v199, v37\n"
    "v_sub_f32 v28, 0, v37\n"
    "v_mfma_f32_16x16x32_bf16 a[64:67], a[244:247], a[100:103], a[64:67]\n"
    "v_sub_f32 v29, 0, v37\n"
    "v_sub_f32 v30, 0, v37\n"
    "v_mfma_f32_16x16x32_bf16 a[60:63], a[244:247], a[96:99], a[60:63]\n"
    "v_sub_f32 v31, 0, v37\n"
    "v_exp_f32 v192, v192\n"
    "v_mfma_f32_16x16x32_bf16 a[72:75], a[248:251], a[96:99], a[72:75]\n"
    "v_exp_f32 v193, v193\n"
    "v_mfma_f32_16x16x32_bf16 a[76:79], a[248:251], a[100:103], a[76:79]\n"
    "v_exp_f32 v194, v194\n"
    "v_add_f32 v22, v22, v192\n"
    "v_mfma_f32_16x16x32_bf16 a[80:83], a[248:251], a[104:107], a[80:83]\n"
    "s_waitcnt lgkmcnt(0)\n"
    "v_exp_f32 v195, v195\n"
    "v_mfma_f32_16x16x32_bf16 a[92:95], a[252:255], a[104:107], a[92:95]\n"
    "v_add_f32 v22, v22, v193\n"
    "v_cvt_pk_bf16_f32 v192, v192, v193\n"
    "v_exp_f32 v196, v196\n"
    "v_mfma_f32_16x16x32_bf16 a[88:91], a[252:255], a[100:103], a[88:91]\n"
    "v_add_f32 v22, v22, v194\n"
    "v_exp_f32 v197, v197\n"
    "v_mfma_f32_16x16x32_bf16 a[84:87], a[252:255], a[96:99], a[84:87]\n"
    "v_add_f32 v22, v22, v195\n"
    "v_cvt_pk_bf16_f32 v193, v194, v195\n"
    "s_waitcnt vmcnt(56) lgkmcnt(0)\n"
    "s_barrier\n"
    "v_mfma_f32_16x16x32_bf16 v[88:91], a[216:219], v[184:187], 0\n"
    "v_exp_f32 v198, v198\n"
    "v_mfma_f32_16x16x32_bf16 v[100:103], a[220:223], v[184:187], 0\n"
    "ds_read_b128 v[240:243], v8 offset:0\n"
    "v_add_f32 v22, v22, v196\n"
    "v_exp_f32 v199, v199\n"
    "v_mfma_f32_16x16x32_bf16 v[112:115], a[224:227], v[184:187], 0\n"
    "s_add_u32 m0, s64, 65536\n"
    "s_add_u32 s71, s67, s68\n"
    "buffer_load_dwordx4 v18, s[44:47], s71 offen offset:0  lds\n"
    "ds_read_b128 v[244:247], v8 offset:1024\n"
    "v_add_f32 v22, v22, v197\n"
    "v_cvt_pk_bf16_f32 v194, v196, v197\n"
    "v_mfma_f32_16x16x32_bf16 v[124:127], a[228:231], v[184:187], 0\n"
    "ds_read_b128 v[248:251], v9 offset:0\n"
    "v_add_f32 v22, v22, v198\n"
    "v_add_f32 v22, v22, v199\n"
    "v_cvt_pk_bf16_f32 v195, v198, v199\n"
    "v_mfma_f32_16x16x32_bf16 v[136:139], a[232:235], v[184:187], 0\n"
    "ds_read_b128 v[252:255], v9 offset:1024\n"
    "v_max3_f32 v37, v200, v201, v202\n"
    "v_max3_f32 v36, v203, v204, v205\n"
    "v_mfma_f32_16x16x32_bf16 v[148:151], a[236:239], v[184:187], 0\n"
    "v_max3_f32 v37, v37, v206, v207\n"
    "v_max_f32 v37, v37, v36\n"
    "v_mov_b32 v36, v37\n"
    "s_nop 1\n"
    "v_permlane32_swap_b32 v37, v36\n"
    "v_mfma_f32_16x16x32_bf16 v[160:163], a[160:163], v[184:187], 0\n"
    "ds_read_b128 a[240:243], v10 offset:0\n"
    "v_max_f32 v37, v37, v36\n"
    "v_mfma_f32_16x16x32_bf16 v[172:175], a[164:167], v[184:187], 0\n"
    "ds_read_b128 a[244:247], v10 offset:1024\n"
    "v_mov_b32 v36, v37\n"
    "s_nop 1\n"
    "v_permlane16_swap_b32 v37, v36\n"
    "v_mfma_f32_16x16x32_bf16 v[176:179], a[164:167], v[192:195], 0\n"
    "ds_read_b128 a[248:251], v11 offset:0\n"
    "v_max_f32 v37, v37, v36\n"
    "v_sub_f32 v200, v200, v37\n"
    "v_sub_f32 v201, v201, v37\n"
    "v_mfma_f32_16x16x32_bf16 v[164:167], a[160:163], v[192:195], 0\n"
    "ds_read_b128 a[252:255], v11 offset:1024\n"
    "v_sub_f32 v202, v202, v37\n"
    "v_sub_f32 v203, v203, v37\n"
    "v_mfma_f32_16x16x32_bf16 v[152:155], a[236:239], v[192:195], 0\n"
    "v_sub_f32 v204, v204, v37\n"
    "v_sub_f32 v205, v205, v37\n"
    "v_sub_f32 v206, v206, v37\n"
    "v_mfma_f32_16x16x32_bf16 v[140:143], a[232:235], v[192:195], 0\n"
    "s_add_u32 s72, s71, 31744\n"
    "buffer_load_dwordx4 v19, s[44:47], s71 offen offset:2048  lds\n"
    "v_sub_f32 v207, v207, v37\n"
    "v_sub_f32 v32, 0, v37\n"
    "v_mfma_f32_16x16x32_bf16 v[128:131], a[228:231], v[192:195], 0\n"
    "v_sub_f32 v33, 0, v37\n"
    "v_sub_f32 v34, 0, v37\n"
    "v_sub_f32 v35, 0, v37\n"
    "v_mfma_f32_16x16x32_bf16 v[116:119], a[224:227], v[192:195], 0\n"
    "v_exp_f32 v200, v200\n"
    "v_exp_f32 v201, v201\n"
    "v_mfma_f32_16x16x32_bf16 v[104:107], a[220:223], v[192:195], 0\n"
    "v_exp_f32 v202, v202\n"
    "v_mfma_f32_16x16x32_bf16 v[92:95], a[216:219], v[192:195], 0\n"
    "s_waitcnt lgkmcnt(1)\n"
    "v_add_f32 v23, v23, v200\n"
    "v_exp_f32 v203, v203\n"
    "v_mfma_f32_16x16x32_bf16 v[184:187], v[240:243], v[40:43], v[24:27]\n"
    "v_add_f32 v23, v23, v201\n"
    "v_cvt_pk_bf16_f32 v200, v200, v201\n"
    "v_mfma_f32_16x16x32_bf16 v[188:191], v[244:247], v[40:43], v[24:27]\n"
    "v_exp_f32 v204, v204\n"
    "v_mfma_f32_16x16x32_bf16 v[184:187], v[248:251], v[52:55], v[184:187]\n"
    "v_add_f32 v23, v23, v202\n"
    "v_exp_f32 v205, v205\n"
    "v_mfma_f32_16x16x32_bf16 v[188:191], v[252:255], v[52:55], v[188:191]\n"
    "s_add_u32 s67, s67, 262144\n"
    "buffer_load_dwordx4 v18, s[44:47], s72 offen offset:1024  lds\n"
    "v_add_f32 v23, v23, v203\n"
    "v_cvt_pk_bf16_f32 v201, v202, v203\n"
    "v_mfma_f32_16x16x32_bf16 v[184:187], a[240:243], v[64:67], v[184:187]\n"
    "v_exp_f32 v206, v206\n"
    "v_add_f32 v23, v23, v204\n"
    "v_mfma_f32_16x16x32_bf16 v[188:191], a[244:247], v[64:67], v[188:191]\n"
    "v_exp_f32 v207, v207\n"
    "v_add_f32 v23, v23, v205\n"
    "v_cvt_pk_bf16_f32 v202, v204, v205\n"
    "v_add_f32 v23, v23, v206\n"
    "v_add_f32 v23, v23, v207\n"
    "v_cvt_pk_bf16_f32 v203, v206, v207\n"
    "v_mfma_f32_16x16x32_bf16 v[184:187], a[248:251], v[76:79], v[184:187]\n"
    "s_waitcnt lgkmcnt(0)\n"
    "v_mfma_f32_16x16x32_bf16 v[188:191], a[252:255], v[76:79], v[188:191]\n"
    "s_nop 1\n"
    "v_mfma_f32_16x16x32_bf16 v[96:99], a[216:219], v[200:203], 0\n"
    "s_nop 1\n"
    "buffer_load_dwordx4 v19, s[44:47], s72 offen offset:3072  lds\n"
    "v_mfma_f32_16x16x32_bf16 v[108:111], a[220:223], v[200:203], 0\n"
    "s_nop 0\n"
    "v_mfma_f32_16x16x32_bf16 v[120:123], a[224:227], v[200:203], 0\n"
    "v_max3_f32 v37, v184, v185, v186\n"
    "v_max3_f32 v36, v187, v188, v189\n"
    "v_max3_f32 v37, v37, v190, v191\n"
    "v_mfma_f32_16x16x32_bf16 v[132:135], a[228:231], v[200:203], 0\n"
    "v_max_f32 v37, v37, v36\n"
    "v_cmp_nge_f32 vcc, s77, v37\n"
    "v_mfma_f32_16x16x32_bf16 v[144:147], a[232:235], v[200:203], 0\n"
    "s_cbranch_vccnz L_stub110a_%=\n"
    "L_back110a_%=:\n"
    "v_mfma_f32_16x16x32_bf16 v[156:159], a[236:239], v[200:203], 0\n"
    "v_exp_f32 v184, v184\n"
    "v_mfma_f32_16x16x32_bf16 v[168:171], a[160:163], v[200:203], 0\n"
    "v_exp_f32 v185, v185\n"
    "v_mfma_f32_16x16x32_bf16 v[180:183], a[164:167], v[200:203], 0\n"
    "v_exp_f32 v186, v186\n"
    "s_waitcnt vmcnt(23)\n"
    "s_barrier\n"
    "v_mfma_f32_16x16x32_bf16 v[192:195], v[240:243], v[44:47], v[28:31]\n"
    "ds_read_b128 a[96:99], v17 offset:0\n"
    "v_add_f32 v21, v21, v184\n"
    "v_mfma_f32_16x16x32_bf16 v[196:199], v[244:247], v[44:47], v[28:31]\n"
    "ds_read_b64_tr_b16 v[208:209], v0 offset:49152\n"
    "v_mfma_f32_16x16x32_bf16 v[192:195], v[248:251], v[56:59], v[192:195]\n"
    "s_add_u32 m0, s64, 0\n"
    "s_add_u32 s75, s73, s74\n"
    "buffer_load_dwordx4 v12, s[48:51], s75 offen offset:0 nt lds\n"
    "v_exp_f32 v187, v187\n"
    "v_mfma_f32_16x16x32_bf16 v[196:199], v[252:255], v[56:59], v[196:199]\n"
    "ds_read_b64_tr_b16 v[210:211], v0 offset:50176\n"
    "v_add_f32 v21, v21, v185\n"
    "v_mfma_f32_16x16x32_bf16 v[192:195], a[240:243], v[68:71], v[192:195]\n"
    "ds_read_b128 a[100:103], v17 offset:2048\n"
    "v_cvt_pk_bf16_f32 v184, v184, v185\n"
    "v_mfma_f32_16x16x32_bf16 v[196:199], a[244:247], v[68:71], v[196:199]\n"
    "ds_read_b64_tr_b16 v[212:213], v1 offset:49152\n"
    "v_exp_f32 v188, v188\n"
    "v_mfma_f32_16x16x32_bf16 v[192:195], a[248:251], v[80:83], v[192:195]\n"
    "ds_read_b64_tr_b16 v[214:215], v1 offset:50176\n"
    "v_add_f32 v21, v21, v186\n"
    "v_mfma_f32_16x16x32_bf16 v[196:199], a[252:255], v[80:83], v[196:199]\n"
    "ds_read_b128 a[104:107], v17 offset:4096\n"
    "v_exp_f32 v189, v189\n"
    "v_mfma_f32_16x16x32_bf16 v[200:203], v[240:243], v[48:51], v[32:35]\n"
    "v_xor_b32 v17, 16384, v17\n"
    "v_add_f32 v21, v21, v187\n"
    "v_mfma_f32_16x16x32_bf16 v[204:207], v[244:247], v[48:51], v[32:35]\n"
    "ds_read_b64_tr_b16 v[216:217], v2 offset:49152\n"
    "v_cvt_pk_bf16_f32 v185, v186, v187\n"
    "v_mfma_f32_16x16x32_bf16 v[200:203], v[248:251], v[60:63], v[200:203]\n"
    "ds_read_b64_tr_b16 v[218:219], v2 offset:50176\n"
    "buffer_load_dwordx4 v12, s[48:51], s75 offen offset:1024 nt lds\n"
    "v_exp_f32 v190, v190\n"
    "v_mfma_f32_16x16x32_bf16 v[204:207], v[252:255], v[60:63], v[204:207]\n"
    "ds_read_b64_tr_b16 v[220:221], v3 offset:49152\n"
    "v_add_f32 v21, v21, v188\n"
    "v_mfma_f32_16x16x32_bf16 v[200:203], a[240:243], v[72:75], v[200:203]\n"
    "ds_read_b64_tr_b16 v[222:223], v3 offset:50176\n"
    "v_exp_f32 v191, v191\n"
    "v_mfma_f32_16x16x32_bf16 v[204:207], a[244:247], v[72:75], v[204:207]\n"
    "ds_read_b64_tr_b16 v[224:225], v4 offset:49152\n"
    "v_add_f32 v21, v21, v189\n"
    "v_mfma_f32_16x16x32_bf16 v[200:203], a[248:251], v[84:87], v[200:203]\n"
    "ds_read_b64_tr_b16 v[226:227], v4 offset:50176\n"
    "v_cvt_pk_bf16_f32 v186, v188, v189\n"
    "v_mfma_f32_16x16x32_bf16 v[204:207], a[252:255], v[84:87], v[204:207]\n"
    "s_waitcnt lgkmcnt(6)\n"
    "ds_read_b64_tr_b16 v[228:229], v5 offset:49152\n"
    "v_add_f32 v21, v21, v190\n"
    "v_add_f32 v21, v21, v191\n"
    "v_mfma_f32_16x16x32_bf16 a[0:3], v[208:211], a[96:99], a[0:3]\n"
    "ds_read_b64_tr_b16 v[230:231], v5 offset:50176\n"
    "buffer_load_dwordx4 v13, s[48:51], s75 offen offset:2048 nt lds\n"
    "v_cvt_pk_bf16_f32 v187, v190, v191\n"
    "v_mfma_f32_16x16x32_bf16 a[4:7], v[208:211], a[100:103], a[4:7]\n"
    "ds_read_b64_tr_b16 v[232:233], v6 offset:49152\n"
    "v_max3_f32 v37, v192, v193, v194\n"
    "v_max3_f32 v36, v195, v196, v197\n"
    "v_mfma_f32_16x16x32_bf16 a[8:11], v[208:211], a[104:107], a[8:11]\n"
    "ds_read_b64_tr_b16 v[234:235], v6 offset:50176\n"
    "v_max3_f32 v37, v37, v198, v199\n"
    "v_mfma_f32_16x16x32_bf16 a[20:23], v[212:215], a[104:107], a[20:23]\n"
    "ds_read_b64_tr_b16 v[236:237], v7 offset:49152\n"
    "v_max_f32 v37, v37, v36\n"
    "v_cmp_nge_f32 vcc, s77, v37\n"
    "v_mfma_f32_16x16x32_bf16 a[16:19], v[212:215], a[100:103], a[16:19]\n"
    "s_cbranch_vccnz L_stub111a_%=\n"
    "L_back111a_%=:\n"
    "ds_read_b64_tr_b16 v[238:239], v7 offset:50176\n"
    "v_mfma_f32_16x16x32_bf16 a[12:15], v[212:215], a[96:99], a[12:15]\n"
    "s_waitcnt lgkmcnt(6)\n"
    "ds_read_b64_tr_b16 v[240:241], v0 offset:0\n"
    "v_exp_f32 v192, v192\n"
    "v_mfma_f32_16x16x32_bf16 a[24:27], v[216:219], a[96:99], a[24:27]\n"
    "ds_read_b64_tr_b16 v[242:243], v0 offset:1024\n"
    "buffer_load_dwordx4 v13, s[48:51], s75 offen offset:3072 nt lds\n"
    "v_exp_f32 v193, v193\n"
    "v_mfma_f32_16x16x32_bf16 a[28:31], v[216:219], a[100:103], a[28:31]\n"
    "ds_read_b64_tr_b16 v[244:245], v1 offset:0\n"
    "v_mfma_f32_16x16x32_bf16 a[32:35], v[216:219], a[104:107], a[32:35]\n"
    "ds_read_b64_tr_b16 v[246:247], v1 offset:1024\n"
    "v_exp_f32 v194, v194\n"
    "v_mfma_f32_16x16x32_bf16 a[44:47], v[220:223], a[104:107], a[44:47]\n"
    "ds_read_b64_tr_b16 v[248:249], v2 offset:0\n"
    "v_add_f32 v22, v22, v192\n"
    "v_mfma_f32_16x16x32_bf16 a[40:43], v[220:223], a[100:103], a[40:43]\n"
    "ds_read_b64_tr_b16 v[250:251], v2 offset:1024\n"
    "v_exp_f32 v195, v195\n"
    "v_mfma_f32_16x16x32_bf16 a[36:39], v[220:223], a[96:99], a[36:39]\n"
    "ds_read_b64_tr_b16 v[252:253], v3 offset:0\n"
    "v_add_f32 v22, v22, v193\n"
    "v_mfma_f32_16x16x32_bf16 a[48:51], v[224:227], a[96:99], a[48:51]\n"
    "ds_read_b64_tr_b16 v[254:255], v3 offset:1024\n"
    "v_cvt_pk_bf16_f32 v192, v192, v193\n"
    "v_exp_f32 v196, v196\n"
    "v_mfma_f32_16x16x32_bf16 a[52:55], v[224:227], a[100:103], a[52:55]\n"
    "ds_read_b64_tr_b16 a[240:241], v4 offset:0\n"
    "v_add_f32 v22, v22, v194\n"
    "v_mfma_f32_16x16x32_bf16 a[56:59], v[224:227], a[104:107], a[56:59]\n"
    "s_waitcnt lgkmcnt(6)\n"
    "ds_read_b64_tr_b16 a[242:243], v4 offset:1024\n"
    "v_exp_f32 v197, v197\n"
    "v_mfma_f32_16x16x32_bf16 a[68:71], v[228:231], a[104:107], a[68:71]\n"
    "ds_read_b64_tr_b16 a[244:245], v5 offset:0\n"
    "v_add_f32 v22, v22, v195\n"
    "v_mfma_f32_16x16x32_bf16 a[64:67], v[228:231], a[100:103], a[64:67]\n"
    "ds_read_b64_tr_b16 a[246:247], v5 offset:1024\n"
    "v_cvt_pk_bf16_f32 v193, v194, v195\n"
    "v_mfma_f32_16x16x32_bf16 a[60:63], v[228:231], a[96:99], a[60:63]\n"
    "ds_read_b64_tr_b16 a[248:249], v6 offset:0\n"
    "v_exp_f32 v198, v198\n"
    "v_mfma_f32_16x16x32_bf16 a[72:75], v[232:235], a[96:99], a[72:75]\n"
    "ds_read_b64_tr_b16 a[250:251], v6 offset:1024\n"
    "ds_read_b64_tr_b16 a[252:253], v7 offset:0\n"
    "ds_read_b64_tr_b16 a[254:255], v7 offset:1024\n"
    "v_mfma_f32_16x16x32_bf16 a[76:79], v[232:235], a[100:103], a[76:79]\n"
    "v_add_f32 v22, v22, v196\n"
    "v_exp_f32 v199, v199\n"
    "v_mfma_f32_16x16x32_bf16 a[80:83], v[232:235], a[104:107], a[80:83]\n"
    "v_add_f32 v22, v22, v197\n"
    "v_cvt_pk_bf16_f32 v194, v196, v197\n"
    "v_mfma_f32_16x16x32_bf16 a[92:95], v[236:239], a[104:107], a[92:95]\n"
    "v_add_f32 v22, v22, v198\n"
    "v_add_f32 v22, v22, v199\n"
    "v_mfma_f32_16x16x32_bf16 a[88:91], v[236:239], a[100:103], a[88:91]\n"
    "v_cvt_pk_bf16_f32 v195, v198, v199\n"
    "v_max3_f32 v37, v200, v201, v202\n"
    "v_mfma_f32_16x16x32_bf16 a[84:87], v[236:239], a[96:99], a[84:87]\n"
    "v_max3_f32 v36, v203, v204, v205\n"
    "v_max3_f32 v37, v37, v206, v207\n"
    "s_waitcnt vmcnt(23) lgkmcnt(0)\n"
    "s_barrier\n"
    "v_mfma_f32_16x16x32_bf16 v[88:91], v[240:243], v[184:187], v[88:91]\n"
    "ds_read_b128 v[208:211], v8 offset:16384\n"
    "v_max_f32 v37, v37, v36\n"
    "v_mfma_f32_16x16x32_bf16 v[100:103], v[244:247], v[184:187], v[100:103]\n"
    "ds_read_b128 v[212:215], v8 offset:17408\n"
    "v_cmp_nge_f32 vcc, s77, v37\n"
    "v_mfma_f32_16x16x32_bf16 v[112:115], v[248:251], v[184:187], v[112:115]\n"
    "s_add_u32 m0, s64, 32768\n"
    "s_add_u32 s73, s73, 8192\n"
    "buffer_load_dwordx4 v14, s[52:55], s75 offen offset:0 nt lds\n"
    "v_mfma_f32_16x16x32_bf16 v[124:127], v[252:255], v[184:187], v[124:127]\n"
    "s_cbranch_vccnz L_stub112a_%=\n"
    "L_back112a_%=:\n"
    "ds_read_b128 v[216:219], v9 offset:16384\n"
    "v_mfma_f32_16x16x32_bf16 v[136:139], a[240:243], v[184:187], v[136:139]\n"
    "ds_read_b128 v[220:223], v9 offset:17408\n"
    "v_exp_f32 v200, v200\n"
    "v_mfma_f32_16x16x32_bf16 v[148:151], a[244:247], v[184:187], v[148:151]\n"
    "ds_read_b128 v[224:227], v10 offset:16384\n"
    "buffer_load_dwordx4 v14, s[52:55], s75 offen offset:1024 nt lds\n"
    "v_mfma_f32_16x16x32_bf16 v[160:163], a[248:251], v[184:187], v[160:163]\n"
    "v_exp_f32 v201, v201\n"
    "v_mfma_f32_16x16x32_bf16 v[172:175], a[252:255], v[184:187], v[172:175]\n"
    "ds_read_b128 v[228:231], v10 offset:17408\n"
    "v_exp_f32 v202, v202\n"
    "v_mfma_f32_16x16x32_bf16 v[176:179], a[252:255], v[192:195], v[176:179]\n"
    "ds_read_b128 v[232:235], v11 offset:16384\n"
    "v_add_f32 v23, v23, v200\n"
    "v_mfma_f32_16x16x32_bf16 v[164:167], a[248:251], v[192:195], v[164:167]\n"
    "ds_read_b128 v[236:239], v11 offset:17408\n"
    "buffer_load_dwordx4 v15, s[52:55], s75 offen offset:2048 nt lds\n"
    "v_exp_f32 v203, v203\n"
    "v_mfma_f32_16x16x32_bf16 v[152:155], a[244:247], v[192:195], v[152:155]\n"
    "v_add_f32 v23, v23, v201\n"
    "v_cvt_pk_bf16_f32 v200, v200, v201\n"
    "v_mfma_f32_16x16x32_bf16 v[140:143], a[240:243], v[192:195], v[140:143]\n"
    "buffer_load_dwordx4 v15, s[52:55], s75 offen offset:3072 nt lds\n"
    "v_mfma_f32_16x16x32_bf16 v[128:131], v[252:255], v[192:195], v[128:131]\n"
    "v_exp_f32 v204, v204\n"
    "v_mfma_f32_16x16x32_bf16 v[116:119], v[248:251], v[192:195], v[116:119]\n"
    "v_add_f32 v23, v23, v202\n"
    "v_mfma_f32_16x16x32_bf16 v[104:107], v[244:247], v[192:195], v[104:107]\n"
    "v_exp_f32 v205, v205\n"
    "v_mfma_f32_16x16x32_bf16 v[92:95], v[240:243], v[192:195], v[92:95]\n"
    "s_waitcnt lgkmcnt(1)\n"
    "s_and_b32 s73, s73, 0x1ffff\n"
    "s_add_u32 m0, s64, 81920\n"
    "s_add_u32 s71, s67, s68\n"
    "buffer_load_dwordx4 v18, s[44:47], s71 offen offset:0  lds\n"
    "v_add_f32 v23, v23, v203\n"
    "v_mfma_f32_16x16x32_bf16 v[184:187], v[208:211], v[40:43], v[24:27]\n"
    "v_cvt_pk_bf16_f32 v201, v202, v203\n"
    "v_mfma_f32_16x16x32_bf16 v[188:191], v[212:215], v[40:43], v[24:27]\n"
    "v_exp_f32 v206, v206\n"
    "v_mfma_f32_16x16x32_bf16 v[184:187], v[216:219], v[52:55], v[184:187]\n"
    "s_add_u32 s72, s71, 31744\n"
    "buffer_load_dwordx4 v19, s[44:47], s71 offen offset:2048  lds\n"
    "v_add_f32 v23, v23, v204\n"
    "v_mfma_f32_16x16x32_bf16 v[188:191], v[220:223], v[52:55], v[188:191]\n"
    "v_exp_f32 v207, v207\n"
    "v_mfma_f32_16x16x32_bf16 v[184:187], v[224:227], v[64:67], v[184:187]\n"
    "v_add_f32 v23, v23, v205\n"
    "v_cvt_pk_bf16_f32 v202, v204, v205\n"
    "v_mfma_f32_16x16x32_bf16 v[188:191], v[228:231], v[64:67], v[188:191]\n"
    "v_add_f32 v23, v23, v206\n"
    "v_add_f32 v23, v23, v207\n"
    "v_cvt_pk_bf16_f32 v203, v206, v207\n"
    "v_mfma_f32_16x16x32_bf16 v[184:187], v[232:235], v[76:79], v[184:187]\n"
    "s_waitcnt lgkmcnt(0)\n"
    "s_add_u32 s67, s67, 262144\n"
    "buffer_load_dwordx4 v18, s[44:47], s72 offen offset:1024  lds\n"
    "v_mfma_f32_16x16x32_bf16 v[188:191], v[236:239], v[76:79], v[188:191]\n"
    "s_nop 1\n"
    "v_mfma_f32_16x16x32_bf16 v[96:99], v[240:243], v[200:203], v[96:99]\n"
    "s_nop 1\n"
    "v_mfma_f32_16x16x32_bf16 v[108:111], v[244:247], v[200:203], v[108:111]\n"
    "s_nop 0\n"
    "v_mfma_f32_16x16x32_bf16 v[120:123], v[248:251], v[200:203], v[120:123]\n"
    "v_max3_f32 v37, v184, v185, v186\n"
    "v_max3_f32 v36, v187, v188, v189\n"
    "v_max3_f32 v37, v37, v190, v191\n"
    "v_mfma_f32_16x16x32_bf16 v[132:135], v[252:255], v[200:203], v[132:135]\n"
    "v_max_f32 v37, v37, v36\n"
    "v_cmp_nge_f32 vcc, s77, v37\n"
    "v_mfma_f32_16x16x32_bf16 v[144:147], a[240:243], v[200:203], v[144:147]\n"
    "s_cmp_eq_u32 s67, 5242880\n"
    "s_cselect_b32 s67, 0, s67\n"
    "buffer_load_dwordx4 v19, s[44:47], s72 offen offset:3072  lds\n"
    "s_cbranch_vccnz L_stub100a_%=\n"
    "L_back100a_%=:\n"
    "v_mfma_f32_16x16x32_bf16 v[156:159], a[244:247], v[200:203], v[156:159]\n"
    "v_exp_f32 v184, v184\n"
    "v_mfma_f32_16x16x32_bf16 v[168:171], a[248:251], v[200:203], v[168:171]\n"
    "v_exp_f32 v185, v185\n"
    "v_mfma_f32_16x16x32_bf16 v[180:183], a[252:255], v[200:203], v[180:183]\n"
    "v_exp_f32 v186, v186\n"
    "s_waitcnt vmcnt(12)\n"
    "s_barrier\n"
    "v_mfma_f32_16x16x32_bf16 v[192:195], v[208:211], v[44:47], v[28:31]\n"
    "ds_read_b128 a[96:99], v16 offset:0\n"
    "v_add_f32 v21, v21, v184\n"
    "v_mfma_f32_16x16x32_bf16 v[196:199], v[212:215], v[44:47], v[28:31]\n"
    "ds_read_b64_tr_b16 v[240:241], v0 offset:32768\n"
    "v_mfma_f32_16x16x32_bf16 v[192:195], v[216:219], v[56:59], v[192:195]\n"
    "s_mov_b32 m0, s70\n"
    "s_add_u32 s71, s69, 40960\n"
    "buffer_load_dwordx4 v20, s[40:43], s69 offen offset:0  lds\n"
    "v_exp_f32 v187, v187\n"
    "v_mfma_f32_16x16x32_bf16 v[196:199], v[220:223], v[56:59], v[196:199]\n"
    "ds_read_b64_tr_b16 v[242:243], v0 offset:33792\n"
    "v_add_f32 v21, v21, v185\n"
    "v_mfma_f32_16x16x32_bf16 v[192:195], v[224:227], v[68:71], v[192:195]\n"
    "ds_read_b128 a[100:103], v16 offset:2048\n"
    "v_cvt_pk_bf16_f32 v184, v184, v185\n"
    "v_mfma_f32_16x16x32_bf16 v[196:199], v[228:231], v[68:71], v[196:199]\n"
    "ds_read_b64_tr_b16 v[244:245], v1 offset:32768\n"
    "v_exp_f32 v188, v188\n"
    "v_mfma_f32_16x16x32_bf16 v[192:195], v[232:235], v[80:83], v[192:195]\n"
    "s_add_u32 m0, s70, 4096\n"
    "s_add_u32 s72, s69, 81920\n"
    "buffer_load_dwordx4 v20, s[40:43], s71 offen offset:0  lds\n"
    "ds_read_b64_tr_b16 v[246:247], v1 offset:33792\n"
    "v_add_f32 v21, v21, v186\n"
    "v_mfma_f32_16x16x32_bf16 v[196:199], v[236:239], v[80:83], v[196:199]\n"
    "ds_read_b128 a[104:107], v16 offset:4096\n"
    "v_exp_f32 v189, v189\n"
    "v_mfma_f32_16x16x32_bf16 v[200:203], v[208:211], v[48:51], v[32:35]\n"
    "v_xor_b32 v16, 16384, v16\n"
    "v_add_f32 v21, v21, v187\n"
    "v_mfma_f32_16x16x32_bf16 v[204:207], v[212:215], v[48:51], v[32:35]\n"
    "ds_read_b64_tr_b16 v[248:249], v2 offset:32768\n"
    "v_cvt_pk_bf16_f32 v185, v186, v187\n"
    "v_mfma_f32_16x16x32_bf16 v[200:203], v[216:219], v[60:63], v[200:203]\n"
    "s_add_u32 m0, s70, 8192\n"
    "s_add_u32 s69, s69, 128\n"
    "buffer_load_dwordx4 v20, s[40:43], s72 offen offset:0  lds\n"
    "ds_read_b64_tr_b16 v[250:251], v2 offset:33792\n"
    "v_exp_f32 v190, v190\n"
    "v_mfma_f32_16x16x32_bf16 v[204:207], v[220:223], v[60:63], v[204:207]\n"
    "ds_read_b64_tr_b16 v[252:253], v3 offset:32768\n"
    "v_add_f32 v21, v21, v188\n"
    "v_mfma_f32_16x16x32_bf16 v[200:203], v[224:227], v[72:75], v[200:203]\n"
    "ds_read_b64_tr_b16 v[254:255], v3 offset:33792\n"
    "v_exp_f32 v191, v191\n"
    "v_mfma_f32_16x16x32_bf16 v[204:207], v[228:231], v[72:75], v[204:207]\n"
    "ds_read_b64_tr_b16 a[240:241], v4 offset:32768\n"
    "v_add_f32 v21, v21, v189\n"
    "v_mfma_f32_16x16x32_bf16 v[200:203], v[232:235], v[84:87], v[200:203]\n"
    "s_cmp_eq_u32 s69, 1280\n"
    "s_cselect_b32 s69, 0, s69\n"
    "s_xor_b32 s70, s70, 16384\n"
    "s_add_u32 m0, s64, 16384\n"
    "s_add_u32 s75, s73, s74\n"
    "buffer_load_dwordx4 v12, s[48:51], s75 offen offset:0 nt lds\n"
    "ds_read_b64_tr_b16 a[242:243], v4 offset:33792\n"
    "v_cvt_pk_bf16_f32 v186, v188, v189\n"
    "v_mfma_f32_16x16x32_bf16 v[204:207], v[236:239], v[84:87], v[204:207]\n"
    "s_waitcnt lgkmcnt(6)\n"
    "ds_read_b64_tr_b16 a[244:245], v5 offset:32768\n"
    "v_add_f32 v21, v21, v190\n"
    "v_add_f32 v21, v21, v191\n"
    "v_mfma_f32_16x16x32_bf16 a[0:3], v[240:243], a[96:99], a[0:3]\n"
    "ds_read_b64_tr_b16 a[246:247], v5 offset:33792\n"
    "v_cvt_pk_bf16_f32 v187, v190, v191\n"
    "v_mfma_f32_16x16x32_bf16 a[4:7], v[240:243], a[100:103], a[4:7]\n"
    "ds_read_b64_tr_b16 a[248:249], v6 offset:32768\n"
    "v_max3_f32 v37, v192, v193, v194\n"
    "v_max3_f32 v36, v195, v196, v197\n"
    "v_mfma_f32_16x16x32_bf16 a[8:11], v[240:243], a[104:107], a[8:11]\n"
    "ds_read_b64_tr_b16 a[250:251], v6 offset:33792\n"
    "v_max3_f32 v37, v37, v198, v199\n"
    "v_mfma_f32_16x16x32_bf16 a[20:23], v[244:247], a[104:107], a[20:23]\n"
    "ds_read_b64_tr_b16 a[252:253], v7 offset:32768\n"
    "v_max_f32 v37, v37, v36\n"
    "v_cmp_nge_f32 vcc, s77, v37\n"
    "v_mfma_f32_16x16x32_bf16 a[16:19], v[244:247], a[100:103], a[16:19]\n"
    "s_cbranch_vccnz L_stub101d_%=\n"
    "L_back101d_%=:\n"
    "ds_read_b64_tr_b16 a[254:255], v7 offset:33792\n"
    "buffer_load_dwordx4 v12, s[48:51], s75 offen offset:1024 nt lds\n"
    "v_mfma_f32_16x16x32_bf16 a[12:15], v[244:247], a[96:99], a[12:15]\n"
    "s_waitcnt lgkmcnt(6)\n"
    "ds_read_b64_tr_b16 v[208:209], v0 offset:16384\n"
    "v_exp_f32 v192, v192\n"
    "v_mfma_f32_16x16x32_bf16 a[24:27], v[248:251], a[96:99], a[24:27]\n"
    "ds_read_b64_tr_b16 v[210:211], v0 offset:17408\n"
    "buffer_load_dwordx4 v13, s[48:51], s75 offen offset:2048 nt lds\n"
    "v_exp_f32 v193, v193\n"
    "v_mfma_f32_16x16x32_bf16 a[28:31], v[248:251], a[100:103], a[28:31]\n"
    "ds_read_b64_tr_b16 v[212:213], v1 offset:16384\n"
    "buffer_load_dwordx4 v13, s[48:51], s75 offen offset:3072 nt lds\n"
    "v_mfma_f32_16x16x32_bf16 a[32:35], v[248:251], a[104:107], a[32:35]\n"
    "ds_read_b64_tr_b16 v[214:215], v1 offset:17408\n"
    "v_exp_f32 v194, v194\n"
    "v_mfma_f32_16x16x32_bf16 a[44:47], v[252:255], a[104:107], a[44:47]\n"
    "ds_read_b64_tr_b16 v[216:217], v2 offset:16384\n"
    "v_add_f32 v22, v22, v192\n"
    "v_mfma_f32_16x16x32_bf16 a[40:43], v[252:255], a[100:103], a[40:43]\n"
    "ds_read_b64_tr_b16 v[218:219], v2 offset:17408\n"
    "v_exp_f32 v195, v195\n"
    "v_mfma_f32_16x16x32_bf16 a[36:39], v[252:255], a[96:99], a[36:39]\n"
    "ds_read_b64_tr_b16 v[220:221], v3 offset:16384\n"
    "v_add_f32 v22, v22, v193\n"
    "v_mfma_f32_16x16x32_bf16 a[48:51], a[240:243], a[96:99], a[48:51]\n"
    "ds_read_b64_tr_b16 v[222:223], v3 offset:17408\n"
    "v_cvt_pk_bf16_f32 v192, v192, v193\n"
    "v_exp_f32 v196, v196\n"
    "v_mfma_f32_16x16x32_bf16 a[52:55], a[240:243], a[100:103], a[52:55]\n"
    "ds_read_b64_tr_b16 v[224:225], v4 offset:16384\n"
    "v_add_f32 v22, v22, v194\n"
    "v_mfma_f32_16x16x32_bf16 a[56:59], a[240:243], a[104:107], a[56:59]\n"
    "s_waitcnt lgkmcnt(6)\n"
    "ds_read_b64_tr_b16 v[226:227], v4 offset:17408\n"
    "v_exp_f32 v197, v197\n"
    "v_mfma_f32_16x16x32_bf16 a[68:71], a[244:247], a[104:107], a[68:71]\n"
    "ds_read_b64_tr_b16 v[228:229], v5 offset:16384\n"
    "v_add_f32 v22, v22, v195\n"
    "v_mfma_f32_16x16x32_bf16 a[64:67], a[244:247], a[100:103], a[64:67]\n"
    "ds_read_b64_tr_b16 v[230:231], v5 offset:17408\n"
    "v_cvt_pk_bf16_f32 v193, v194, v195\n"
    "v_mfma_f32_16x16x32_bf16 a[60:63], a[244:247], a[96:99], a[60:63]\n"
    "ds_read_b64_tr_b16 v[232:233], v6 offset:16384\n"
    "v_exp_f32 v198, v198\n"
    "v_mfma_f32_16x16x32_bf16 a[72:75], a[248:251], a[96:99], a[72:75]\n"
    "ds_read_b64_tr_b16 v[234:235], v6 offset:17408\n"
    "ds_read_b64_tr_b16 v[236:237], v7 offset:16384\n"
    "ds_read_b64_tr_b16 v[238:239], v7 offset:17408\n"
    "v_mfma_f32_16x16x32_bf16 a[76:79], a[248:251], a[100:103], a[76:79]\n"
    "v_add_f32 v22, v22, v196\n"
    "v_exp_f32 v199, v199\n"
    "v_mfma_f32_16x16x32_bf16 a[80:83], a[248:251], a[104:107], a[80:83]\n"
    "v_add_f32 v22, v22, v197\n"
    "v_cvt_pk_bf16_f32 v194, v196, v197\n"
    "v_mfma_f32_16x16x32_bf16 a[92:95], a[252:255], a[104:107], a[92:95]\n"
    "v_add_f32 v22, v22, v198\n"
    "v_add_f32 v22, v22, v199\n"
    "v_mfma_f32_16x16x32_bf16 a[88:91], a[252:255], a[100:103], a[88:91]\n"
    "v_cvt_pk_bf16_f32 v195, v198, v199\n"
    "v_max3_f32 v37, v200, v201, v202\n"
    "v_mfma_f32_16x16x32_bf16 a[84:87], a[252:255], a[96:99], a[84:87]\n"
    "v_max3_f32 v36, v203, v204, v205\n"
    "v_max3_f32 v37, v37, v206, v207\n"
    "s_waitcnt vmcnt(30) lgkmcnt(0)\n"
    "s_barrier\n"
    "v_mfma_f32_16x16x32_bf16 v[88:91], v[208:211], v[184:187], v[88:91]\n"
    "v_add_u32 v8, 131072, v8\n"
    "ds_read_b128 v[240:243], v8 offset:0\n"
    "v_mfma_f32_16x16x32_bf16 v[100:103], v[212:215], v[184:187], v[100:103]\n"
    "ds_read_b128 v[244:247], v8 offset:1024\n"
    "v_add_u32 v8, -131072, v8\n"
    "v_mfma_f32_16x16x32_bf16 v[112:115], v[216:219], v[184:187], v[112:115]\n"
    "s_add_u32 m0, s64, 65536\n"
    "s_add_u32 s71, s67, s68\n"
    "buffer_load_dwordx4 v18, s[44:47], s71 offen offset:0  lds\n"
    "v_max_f32 v37, v37, v36\n"
    "v_mfma_f32_16x16x32_bf16 v[124:127], v[220:223], v[184:187], v[124:127]\n"
    "v_add_u32 v9, 131072, v9\n"
    "v_cmp_nge_f32 vcc, s77, v37\n"
    "s_cbranch_vccnz L_stub102d_%=\n"
    "L_back102d_%=:\n"
    "v_mfma_f32_16x16x32_bf16 v[136:139], v[224:227], v[184:187], v[136:139]\n"
    "ds_read_b128 v[248:251], v9 offset:0\n"
    "ds_read_b128 v[252:255], v9 offset:1024\n"
    "v_exp_f32 v200, v200\n"
    "v_mfma_f32_16x16x32_bf16 v[148:151], v[228:231], v[184:187], v[148:151]\n"
    "v_add_u32 v9, -131072, v9\n"
    "v_add_u32 v10, 131072, v10\n"
    "v_mfma_f32_16x16x32_bf16 v[160:163], v[232:235], v[184:187], v[160:163]\n"
    "s_add_u32 s72, s71, 31744\n"
    "buffer_load_dwordx4 v19, s[44:47], s71 offen offset:2048  lds\n"
    "ds_read_b128 a[240:243], v10 offset:0\n"
    "v_exp_f32 v201, v201\n"
    "v_mfma_f32_16x16x32_bf16 v[172:175], v[236:239], v[184:187], v[172:175]\n"
    "ds_read_b128 a[244:247], v10 offset:1024\n"
    "v_add_u32 v10, -131072, v10\n"
    "v_exp_f32 v202, v202\n"
    "v_mfma_f32_16x16x32_bf16 v[176:179], v[236:239], v[192:195], v[176:179]\n"
    "v_add_u32 v11, 131072, v11\n"
    "v_add_f32 v23, v23, v200\n"
    "v_mfma_f32_16x16x32_bf16 v[164:167], v[232:235], v[192:195], v[164:167]\n"
    "ds_read_b128 a[248:251], v11 offset:0\n"
    "ds_read_b128 a[252:255], v11 offset:1024\n"
    "v_add_u32 v11, -131072, v11\n"
    "v_exp_f32 v203, v203\n"
    "v_mfma_f32_16x16x32_bf16 v[152:155], v[228:231], v[192:195], v[152:155]\n"
    "s_add_u32 s67, s67, 262144\n"
    "buffer_load_dwordx4 v18, s[44:47], s72 offen offset:1024  lds\n"
    "v_add_f32 v23, v23, v201\n"
    "v_mfma_f32_16x16x32_bf16 v[140:143], v[224:227], v[192:195], v[140:143]\n"
    "v_cvt_pk_bf16_f32 v200, v200, v201\n"
    "v_mfma_f32_16x16x32_bf16 v[128:131], v[220:223], v[192:195], v[128:131]\n"
    "v_exp_f32 v204, v204\n"
    "v_mfma_f32_16x16x32_bf16 v[116:119], v[216:219], v[192:195], v[116:119]\n"
    "v_add_f32 v23, v23, v202\n"
    "buffer_load_dwordx4 v19, s[44:47], s72 offen offset:3072  lds\n"
    "v_mfma_f32_16x16x32_bf16 v[104:107], v[212:215], v[192:195], v[104:107]\n"
    "v_exp_f32 v205, v205\n"
    "v_mfma_f32_16x16x32_bf16 v[92:95], v[208:211], v[192:195], v[92:95]\n"
    "s_waitcnt lgkmcnt(2)\n"
    "v_add_f32 v23, v23, v203\n"
    "v_cvt_pk_bf16_f32 v201, v202, v203\n"
    "v_mfma_f32_16x16x32_bf16 v[184:187], v[240:243], v[40:43], v[24:27]\n"
    "s_add_u32 m0, s64, 49152\n"
    "s_add_u32 s73, s73, 8192\n"
    "buffer_load_dwordx4 v14, s[52:55], s75 offen offset:0 nt lds\n"
    "v_mfma_f32_16x16x32_bf16 v[188:191], v[244:247], v[40:43], v[24:27]\n"
    "v_exp_f32 v206, v206\n"
    "v_mfma_f32_16x16x32_bf16 v[184:187], v[248:251], v[52:55], v[184:187]\n"
    "v_add_f32 v23, v23, v204\n"
    "v_mfma_f32_16x16x32_bf16 v[188:191], v[252:255], v[52:55], v[188:191]\n"
    "v_exp_f32 v207, v207\n"
    "v_mfma_f32_16x16x32_bf16 v[184:187], a[240:243], v[64:67], v[184:187]\n"
    "v_add_f32 v23, v23, v205\n"
    "v_cvt_pk_bf16_f32 v202, v204, v205\n"
    "v_mfma_f32_16x16x32_bf16 v[188:191], a[244:247], v[64:67], v[188:191]\n"
    "s_waitcnt lgkmcnt(0)\n"
    "v_add_f32 v23, v23, v206\n"
    "v_add_f32 v23, v23, v207\n"
    "v_cvt_pk_bf16_f32 v203, v206, v207\n"
    "v_mfma_f32_16x16x32_bf16 v[184:187], a[248:251], v[76:79], v[184:187]\n"
    "buffer_load_dwordx4 v14, s[52:55], s75 offen offset:1024 nt lds\n"
    "v_mfma_f32_16x16x32_bf16 v[188:191], a[252:255], v[76:79], v[188:191]\n"
    "s_nop 1\n"
    "buffer_load_dwordx4 v15, s[52:55], s75 offen offset:2048 nt lds\n"
    "v_mfma_f32_16x16x32_bf16 v[96:99], v[208:211], v[200:203], v[96:99]\n"
    "s_nop 1\n"
    "v_mfma_f32_16x16x32_bf16 v[108:111], v[212:215], v[200:203], v[108:111]\n"
    "s_nop 0\n"
    "v_mfma_f32_16x16x32_bf16 v[120:123], v[216:219], v[200:203], v[120:123]\n"
    "v_max3_f32 v37, v184, v185, v186\n"
    "v_max3_f32 v36, v187, v188, v189\n"
    "v_max3_f32 v37, v37, v190, v191\n"
    "v_mfma_f32_16x16x32_bf16 v[132:135], v[220:223], v[200:203], v[132:135]\n"
    "v_max_f32 v37, v37, v36\n"
    "v_cmp_nge_f32 vcc, s77, v37\n"
    "v_mfma_f32_16x16x32_bf16 v[144:147], v[224:227], v[200:203], v[144:147]\n"
    "s_cbranch_vccnz L_stub110d_%=\n"
    "L_back110d_%=:\n"
    "v_mfma_f32_16x16x32_bf16 v[156:159], v[228:231], v[200:203], v[156:159]\n"
    "buffer_load_dwordx4 v15, s[52:55], s75 offen offset:3072 nt lds\n"
    "s_and_b32 s73, s73, 0x1ffff\n"
    "v_exp_f32 v184, v184\n"
    "v_mfma_f32_16x16x32_bf16 v[168:171], v[232:235], v[200:203], v[168:171]\n"
    "v_exp_f32 v185, v185\n"
    "v_mfma_f32_16x16x32_bf16 v[180:183], v[236:239], v[200:203], v[180:183]\n"
    "v_exp_f32 v186, v186\n"
    "v_accvgpr_read_b32 v37, a214\n"
    "buffer_load_dword a212, v37, s[92:95], 0 offen offset:0 sc1\n"
    "s_waitcnt vmcnt(15)\n"
    "s_barrier\n"
    "v_mfma_f32_16x16x32_bf16 v[192:195], v[240:243], v[44:47], v[28:31]\n"
    "ds_read_b128 a[96:99], v17 offset:0\n"
    "v_add_f32 v21, v21, v184\n"
    "v_mfma_f32_16x16x32_bf16 v[196:199], v[244:247], v[44:47], v[28:31]\n"
    "ds_read_b64_tr_b16 v[208:209], v0 offset:49152\n"
    "v_mfma_f32_16x16x32_bf16 v[192:195], v[248:251], v[56:59], v[192:195]\n"
    "v_exp_f32 v187, v187\n"
    "v_mfma_f32_16x16x32_bf16 v[196:199], v[252:255], v[56:59], v[196:199]\n"
    "ds_read_b64_tr_b16 v[210:211], v0 offset:50176\n"
    "v_add_f32 v21, v21, v185\n"
    "v_mfma_f32_16x16x32_bf16 v[192:195], a[240:243], v[68:71], v[192:195]\n"
    "ds_read_b128 a[100:103], v17 offset:2048\n"
    "ds_read_b64_tr_b16 v[212:213], v1 offset:49152\n"
    "v_cvt_pk_bf16_f32 v184, v184, v185\n"
    "v_mfma_f32_16x16x32_bf16 v[196:199], a[244:247], v[68:71], v[196:199]\n"
    "ds_read_b64_tr_b16 v[214:215], v1 offset:50176\n"
    "v_exp_f32 v188, v188\n"
    "v_mfma_f32_16x16x32_bf16 v[192:195], a[248:251], v[80:83], v[192:195]\n"
    "ds_read_b128 a[104:107], v17 offset:4096\n"
    "v_xor_b32 v17, 16384, v17\n"
    "v_add_f32 v21, v21, v186\n"
    "v_mfma_f32_16x16x32_bf16 v[196:199], a[252:255], v[80:83], v[196:199]\n"
    "ds_read_b64_tr_b16 v[216:217], v2 offset:49152\n"
    "v_exp_f32 v189, v189\n"
    "v_mfma_f32_16x16x32_bf16 v[200:203], v[240:243], v[48:51], v[32:35]\n"
    "ds_read_b64_tr_b16 v[218:219], v2 offset:50176\n"
    "ds_read_b64_tr_b16 v[220:221], v3 offset:49152\n"
    "v_add_f32 v21, v21, v187\n"
    "v_mfma_f32_16x16x32_bf16 v[204:207], v[244:247], v[48:51], v[32:35]\n"
    "ds_read_b64_tr_b16 v[222:223], v3 offset:50176\n"
    "v_cvt_pk_bf16_f32 v185, v186, v187\n"
    "v_mfma_f32_16x16x32_bf16 v[200:203], v[248:251], v[60:63], v[200:203]\n"
    "ds_read_b64_tr_b16 v[224:225], v4 offset:49152\n"
    "ds_read_b64_tr_b16 v[226:227], v4 offset:50176\n"
    "v_exp_f32 v190, v190\n"
    "v_mfma_f32_16x16x32_bf16 v[204:207], v[252:255], v[60:63], v[204:207]\n"
    "ds_read_b64_tr_b16 v[228:229], v5 offset:49152\n"
    "v_add_f32 v21, v21, v188\n"
    "v_mfma_f32_16x16x32_bf16 v[200:203], a[240:243], v[72:75], v[200:203]\n"
    "ds_read_b64_tr_b16 v[230:231], v5 offset:50176\n"
    "ds_read_b64_tr_b16 v[232:233], v6 offset:49152\n"
    "v_exp_f32 v191, v191\n"
    "v_mfma_f32_16x16x32_bf16 v[204:207], a[244:247], v[72:75], v[204:207]\n"
    "ds_read_b64_tr_b16 v[234:235], v6 offset:50176\n"
    "v_add_f32 v21, v21, v189\n"
    "v_mfma_f32_16x16x32_bf16 v[200:203], a[248:251], v[84:87], v[200:203]\n"
    "ds_read_b64_tr_b16 v[236:237], v7 offset:49152\n"
    "ds_read_b64_tr_b16 v[238:239], v7 offset:50176\n"
    "v_cvt_pk_bf16_f32 v186, v188, v189\n"
    "v_mfma_f32_16x16x32_bf16 v[204:207], a[252:255], v[84:87], v[204:207]\n"
    "s_waitcnt lgkmcnt(9)\n"
    "v_add_u32 v0, 114688, v0\n"
    "v_add_f32 v21, v21, v190\n"
    "v_add_f32 v21, v21, v191\n"
    "v_mfma_f32_16x16x32_bf16 a[0:3], v[208:211], a[96:99], a[0:3]\n"
    "ds_read_b64_tr_b16 v[240:241], v0 offset:0\n"
    "ds_read_b64_tr_b16 v[242:243], v0 offset:1024\n"
    "v_cvt_pk_bf16_f32 v187, v190, v191\n"
    "v_mfma_f32_16x16x32_bf16 a[4:7], v[208:211], a[100:103], a[4:7]\n"
    "v_add_u32 v0, -114688, v0\n"
    "v_max3_f32 v37, v192, v193, v194\n"
    "v_max3_f32 v36, v195, v196, v197\n"
    "v_mfma_f32_16x16x32_bf16 a[8:11], v[208:211], a[104:107], a[8:11]\n"
    "v_add_u32 v1, 114688, v1\n"
    "ds_read_b64_tr_b16 v[244:245], v1 offset:0\n"
    "v_max3_f32 v37, v37, v198, v199\n"
    "v_mfma_f32_16x16x32_bf16 a[20:23], v[212:215], a[104:107], a[20:23]\n"
    "ds_read_b64_tr_b16 v[246:247], v1 offset:1024\n"
    "v_max_f32 v37, v37, v36\n"
    "v_cmp_nge_f32 vcc, s77, v37\n"
    "v_mfma_f32_16x16x32_bf16 a[16:19], v[212:215], a[100:103], a[16:19]\n"
    "s_cbranch_vccnz L_stub111d_%=\n"
    "L_back111d_%=:\n"
    "v_add_u32 v1, -114688, v1\n"
    "v_add_u32 v2, 114688, v2\n"
    "v_mfma_f32_16x16x32_bf16 a[12:15], v[212:215], a[96:99], a[12:15]\n"
    "ds_read_b64_tr_b16 v[248:249], v2 offset:0\n"
    "v_exp_f32 v192, v192\n"
    "v_mfma_f32_16x16x32_bf16 a[24:27], v[216:219], a[96:99], a[24:27]\n"
    "ds_read_b64_tr_b16 v[250:251], v2 offset:1024\n"
    "v_add_u32 v2, -114688, v2\n"
    "v_exp_f32 v193, v193\n"
    "v_mfma_f32_16x16x32_bf16 a[28:31], v[216:219], a[100:103], a[28:31]\n"
    "v_add_u32 v3, 114688, v3\n"
    "ds_read_b64_tr_b16 v[252:253], v3 offset:0\n"
    "v_mfma_f32_16x16x32_bf16 a[32:35], v[216:219], a[104:107], a[32:35]\n"
    "s_waitcnt lgkmcnt(5)\n"
    "ds_read_b64_tr_b16 v[254:255], v3 offset:1024\n"
    "v_exp_f32 v194, v194\n"
    "v_mfma_f32_16x16x32_bf16 a[44:47], v[220:223], a[104:107], a[44:47]\n"
    "v_add_u32 v3, -114688, v3\n"
    "v_add_f32 v22, v22, v192\n"
    "v_mfma_f32_16x16x32_bf16 a[40:43], v[220:223], a[100:103], a[40:43]\n"
    "v_add_u32 v4, 114688, v4\n"
    "ds_read_b64_tr_b16 a[240:241], v4 offset:0\n"
    "v_exp_f32 v195, v195\n"
    "v_mfma_f32_16x16x32_bf16 a[36:39], v[220:223], a[96:99], a[36:39]\n"
    "ds_read_b64_tr_b16 a[242:243], v4 offset:1024\n"
    "v_add_f32 v22, v22, v193\n"
    "v_mfma_f32_16x16x32_bf16 a[48:51], v[224:227], a[96:99], a[48:51]\n"
    "v_add_u32 v4, -114688, v4\n"
    "v_add_u32 v5, 114688, v5\n"
    "v_cvt_pk_bf16_f32 v192, v192, v193\n"
    "v_exp_f32 v196, v196\n"
    "v_mfma_f32_16x16x32_bf16 a[52:55], v[224:227], a[100:103], a[52:55]\n"
    "ds_read_b64_tr_b16 a[244:245], v5 offset:0\n"
    "v_add_f32 v22, v22, v194\n"
    "v_mfma_f32_16x16x32_bf16 a[56:59], v[224:227], a[104:107], a[56:59]\n"
    "ds_read_b64_tr_b16 a[246:247], v5 offset:1024\n"
    "v_add_u32 v5, -114688, v5\n"
    "v_exp_f32 v197, v197\n"
    "v_mfma_f32_16x16x32_bf16 a[68:71], v[228:231], a[104:107], a[68:71]\n"
    "v_add_u32 v6, 114688, v6\n"
    "ds_read_b64_tr_b16 a[248:249], v6 offset:0\n"
    "v_mfma_f32_16x16x32_bf16 a[64:67], v[228:231], a[100:103], a[64:67]\n"
    "ds_read_b64_tr_b16 a[250:251], v6 offset:1024\n"
    "v_add_f32 v22, v22, v195\n"
    "v_mfma_f32_16x16x32_bf16 a[60:63], v[228:231], a[96:99], a[60:63]\n"
    "v_add_u32 v6, -114688, v6\n"
    "v_cvt_pk_bf16_f32 v193, v194, v195\n"
    "v_exp_f32 v198, v198\n"
    "v_mfma_f32_16x16x32_bf16 a[72:75], v[232:235], a[96:99], a[72:75]\n"
    "v_add_u32 v7, 114688, v7\n"
    "ds_read_b64_tr_b16 a[252:253], v7 offset:0\n"
    "ds_read_b64_tr_b16 a[254:255], v7 offset:1024\n"
    "v_add_u32 v7, -114688, v7\n"
    "v_mfma_f32_16x16x32_bf16 a[76:79], v[232:235], a[100:103], a[76:79]\n"
    "v_add_f32 v22, v22, v196\n"
    "v_exp_f32 v199, v199\n"
    "v_mfma_f32_16x16x32_bf16 a[80:83], v[232:235], a[104:107], a[80:83]\n"
    "v_add_f32 v22, v22, v197\n"
    "v_cvt_pk_bf16_f32 v194, v196, v197\n"
    "v_mfma_f32_16x16x32_bf16 a[92:95], v[236:239], a[104:107], a[92:95]\n"
    "v_add_f32 v22, v22, v198\n"
    "v_add_f32 v22, v22, v199\n"
    "v_mfma_f32_16x16x32_bf16 a[88:91], v[236:239], a[100:103], a[88:91]\n"
    "v_cvt_pk_bf16_f32 v195, v198, v199\n"
    "v_max3_f32 v37, v200, v201, v202\n"
    "v_mfma_f32_16x16x32_bf16 a[84:87], v[236:239], a[96:99], a[84:87]\n"
    "v_max3_f32 v36, v203, v204, v205\n"
    "v_max3_f32 v37, v37, v206, v207\n"
    "s_waitcnt vmcnt(23) lgkmcnt(0)\n"
    "s_barrier\n"
    "v_mfma_f32_16x16x32_bf16 v[88:91], v[240:243], v[184:187], v[88:91]\n"
    "ds_read_b128 v[208:211], v8 offset:0\n"
    "v_max_f32 v37, v37, v36\n"
    "v_mfma_f32_16x16x32_bf16 v[100:103], v[244:247], v[184:187], v[100:103]\n"
    "ds_read_b128 v[212:215], v8 offset:1024\n"
    "v_cmp_nge_f32 vcc, s77, v37\n"
    "v_mfma_f32_16x16x32_bf16 v[112:115], v[248:251], v[184:187], v[112:115]\n"
    "s_add_u32 m0, s64, 81920\n"
    "s_add_u32 s71, s67, s68\n"
    "buffer_load_dwordx4 v18, s[44:47], s71 offen offset:0  lds\n"
    "v_mfma_f32_16x16x32_bf16 v[124:127], v[252:255], v[184:187], v[124:127]\n"
    "s_cbranch_vccnz L_stub112d_%=\n"
    "L_back112d_%=:\n"
    "ds_read_b128 v[216:219], v9 offset:0\n"
    "v_mfma_f32_16x16x32_bf16 v[136:139], a[240:243], v[184:187], v[136:139]\n"
    "ds_read_b128 v[220:223], v9 offset:1024\n"
    "v_exp_f32 v200, v200\n"
    "v_mfma_f32_16x16x32_bf16 v[148:151], a[244:247], v[184:187], v[148:151]\n"
    "ds_read_b128 v[224:227], v10 offset:0\n"
    "v_mfma_f32_16x16x32_bf16 v[160:163], a[248:251], v[184:187], v[160:163]\n"
    "v_exp_f32 v201, v201\n"
    "v_mfma_f32_16x16x32_bf16 v[172:175], a[252:255], v[184:187], v[172:175]\n"
    "ds_read_b128 v[228:231], v10 offset:1024\n"
    "v_exp_f32 v202, v202\n"
    "v_mfma_f32_16x16x32_bf16 v[176:179], a[252:255], v[192:195], v[176:179]\n"
    "ds_read_b128 v[232:235], v11 offset:0\n"
    "v_add_f32 v23, v23, v200\n"
    "v_mfma_f32_16x16x32_bf16 v[164:167], a[248:251], v[192:195], v[164:167]\n"
    "ds_read_b128 v[236:239], v11 offset:1024\n"
    "v_exp_f32 v203, v203\n"
    "v_mfma_f32_16x16x32_bf16 v[152:155], a[244:247], v[192:195], v[152:155]\n"
    "s_add_u32 s72, s71, 31744\n"
    "buffer_load_dwordx4 v19, s[44:47], s71 offen offset:2048  lds\n"
    "v_add_f32 v23, v23, v201\n"
    "v_mfma_f32_16x16x32_bf16 v[140:143], a[240:243], v[192:195], v[140:143]\n"
    "v_cvt_pk_bf16_f32 v200, v200, v201\n"
    "v_mfma_f32_16x16x32_bf16 v[128:131], v[252:255], v[192:195], v[128:131]\n"
    "v_exp_f32 v204, v204\n"
    "v_mfma_f32_16x16x32_bf16 v[116:119], v[248:251], v[192:195], v[116:119]\n"
    "v_add_f32 v23, v23, v202\n"
    "v_mfma_f32_16x16x32_bf16 v[104:107], v[244:247], v[192:195], v[104:107]\n"
    "v_exp_f32 v205, v205\n"
    "v_mfma_f32_16x16x32_bf16 v[92:95], v[240:243], v[192:195], v[92:95]\n"
    "s_waitcnt lgkmcnt(1)\n"
    "v_add_f32 v23, v23, v203\n"
    "v_cvt_pk_bf16_f32 v201, v202, v203\n"
    "v_mfma_f32_16x16x32_bf16 v[184:187], v[208:211], v[40:43], v[24:27]\n"
    "s_add_u32 s67, s67, 262144\n"
    "buffer_load_dwordx4 v18, s[44:47], s72 offen offset:1024  lds\n"
    "v_mfma_f32_16x16x32_bf16 v[188:191], v[212:215], v[40:43], v[24:27]\n"
    "v_exp_f32 v206, v206\n"
    "v_mfma_f32_16x16x32_bf16 v[184:187], v[216:219], v[52:55], v[184:187]\n"
    "v_add_f32 v23, v23, v204\n"
    "v_mfma_f32_16x16x32_bf16 v[188:191], v[220:223], v[52:55], v[188:191]\n"
    "v_exp_f32 v207, v207\n"
    "v_mfma_f32_16x16x32_bf16 v[184:187], v[224:227], v[64:67], v[184:187]\n"
    "v_add_f32 v23, v23, v205\n"
    "v_cvt_pk_bf16_f32 v202, v204, v205\n"
    "v_mfma_f32_16x16x32_bf16 v[188:191], v[228:231], v[64:67], v[188:191]\n"
    "v_add_f32 v23, v23, v206\n"
    "v_add_f32 v23, v23, v207\n"
    "v_cvt_pk_bf16_f32 v203, v206, v207\n"
    "v_mfma_f32_16x16x32_bf16 v[184:187], v[232:235], v[76:79], v[184:187]\n"
    "s_waitcnt lgkmcnt(0)\n"
    "s_cmp_eq_u32 s67, 5242880\n"
    "s_cselect_b32 s67, 0, s67\n"
    "buffer_load_dwordx4 v19, s[44:47], s72 offen offset:3072  lds\n"
    "v_mfma_f32_16x16x32_bf16 v[188:191], v[236:239], v[76:79], v[188:191]\n"
    "s_nop 1\n"
    "v_mfma_f32_16x16x32_bf16 v[96:99], v[240:243], v[200:203], v[96:99]\n"
    "s_nop 1\n"
    "v_mfma_f32_16x16x32_bf16 v[108:111], v[244:247], v[200:203], v[108:111]\n"
    "s_nop 0\n"
    "v_mfma_f32_16x16x32_bf16 v[120:123], v[248:251], v[200:203], v[120:123]\n"
    "v_max3_f32 v37, v184, v185, v186\n"
    "v_max3_f32 v36, v187, v188, v189\n"
    "v_max3_f32 v37, v37, v190, v191\n"
    "v_mfma_f32_16x16x32_bf16 v[132:135], v[252:255], v[200:203], v[132:135]\n"
    "v_max_f32 v37, v37, v36\n"
    "v_cmp_nge_f32 vcc, s77, v37\n"
    "v_mfma_f32_16x16x32_bf16 v[144:147], a[240:243], v[200:203], v[144:147]\n"
    "s_cbranch_vccnz L_stub100d_%=\n"
    "L_back100d_%=:\n"
    "v_mfma_f32_16x16x32_bf16 v[156:159], a[244:247], v[200:203], v[156:159]\n"
    "v_exp_f32 v184, v184\n"
    "v_mfma_f32_16x16x32_bf16 v[168:171], a[248:251], v[200:203], v[168:171]\n"
    "v_exp_f32 v185, v185\n"
    "v_mfma_f32_16x16x32_bf16 v[180:183], a[252:255], v[200:203], v[180:183]\n"
    "v_exp_f32 v186, v186\n"
    "s_waitcnt vmcnt(8)\n"
    "s_mov_b32 s66, 0\n"
    "L_r1_%=:\n"
    "s_cmp_lg_u32 s66, 1\n"
    "s_cbranch_scc1 L_h1_%=\n"
    "v_accvgpr_read_b32 v37, a213\n"
    "buffer_store_dword a212, v37, s[92:95], 0 offen offset:256 sc1\n"
    "L_h1_%=:\n"
    "s_cmp_lg_u32 s66, 3\n"
    "s_cbranch_scc1 L_h3_%=\n"
    "v_accvgpr_read_b32 v37, a214\n"
    "buffer_load_dword a212, v37, s[92:95], 0 offen offset:256 sc1\n"
    "L_h3_%=:\n"
    "s_cmp_lg_u32 s66, 4\n"
    "s_cbranch_scc1 L_h4_%=\n"
    "v_accvgpr_read_b32 v37, a211\n"
    "v_accvgpr_read_b32 v36, a212\n"
    "v_cmp_ne_u32 vcc, v37, v36\n"
    "s_and_b64 vcc, vcc, 0xff\n"
    "s_cbranch_vccnz L_hslow_%=\n"
    "L_hok_%=:\n"
    "v_accvgpr_read_b32 v37, a214\n"
    "buffer_load_dword a212, v37, s[92:95], 0 offen offset:0 sc1\n"
    "L_h4_%=:\n"
    "s_waitcnt vmcnt(12)\n"
    "s_barrier\n"
    "v_mfma_f32_16x16x32_bf16 v[192:195], v[208:211], v[44:47], v[28:31]\n"
    "ds_read_b128 a[96:99], v16 offset:0\n"
    "v_add_f32 v21, v21, v184\n"
    "v_mfma_f32_16x16x32_bf16 v[196:199], v[212:215], v[44:47], v[28:31]\n"
    "ds_read_b64_tr_b16 v[240:241], v0 offset:32768\n"
    "v_mfma_f32_16x16x32_bf16 v[192:195], v[216:219], v[56:59], v[192:195]\n"
    "s_mov_b32 m0, s70\n"
    "s_add_u32 s71, s69, 40960\n"
    "buffer_load_dwordx4 v20, s[40:43], s69 offen offset:0  lds\n"
    "v_exp_f32 v187, v187\n"
    "v_mfma_f32_16x16x32_bf16 v[196:199], v[220:223], v[56:59], v[196:199]\n"
    "ds_read_b64_tr_b16 v[242:243], v0 offset:33792\n"
    "v_add_f32 v21, v21, v185\n"
    "v_mfma_f32_16x16x32_bf16 v[192:195], v[224:227], v[68:71], v[192:195]\n"
    "ds_read_b128 a[100:103], v16 offset:2048\n"
    "v_cvt_pk_bf16_f32 v184, v184, v185\n"
    "v_mfma_f32_16x16x32_bf16 v[196:199], v[228:231], v[68:71], v[196:199]\n"
    "ds_read_b64_tr_b16 v[244:245], v1 offset:32768\n"
    "v_exp_f32 v188, v188\n"
    "v_mfma_f32_16x16x32_bf16 v[192:195], v[232:235], v[80:83], v[192:195]\n"
    "s_add_u32 m0, s70, 4096\n"
    "s_add_u32 s72, s69, 81920\n"
    "buffer_load_dwordx4 v20, s[40:43], s71 offen offset:0  lds\n"
    "ds_read_b64_tr_b16 v[246:247], v1 offset:33792\n"
    "v_add_f32 v21, v21, v186\n"
    "v_mfma_f32_16x16x32_bf16 v[196:199], v[236:239], v[80:83], v[196:199]\n"
    "ds_read_b128 a[104:107], v16 offset:4096\n"
    "v_exp_f32 v189, v189\n"
    "v_mfma_f32_16x16x32_bf16 v[200:203], v[208:211], v[48:51], v[32:35]\n"
    "v_xor_b32 v16, 16384, v16\n"
    "v_add_f32 v21, v21, v187\n"
    "v_mfma_f32_16x16x32_bf16 v[204:207], v[212:215], v[48:51], v[32:35]\n"
    "ds_read_b64_tr_b16 v[248:249], v2 offset:32768\n"
    "v_cvt_pk_bf16_f32 v185, v186, v187\n"
    "v_mfma_f32_16x16x32_bf16 v[200:203], v[216:219], v[60:63], v[200:203]\n"
    "s_add_u32 m0, s70, 8192\n"
    "s_add_u32 s69, s69, 128\n"
    "buffer_load_dwordx4 v20, s[40:43], s72 offen offset:0  lds\n"
    "ds_read_b64_tr_b16 v[250:251], v2 offset:33792\n"
    "v_exp_f32 v190, v190\n"
    "v_mfma_f32_16x16x32_bf16 v[204:207], v[220:223], v[60:63], v[204:207]\n"
    "ds_read_b64_tr_b16 v[252:253], v3 offset:32768\n"
    "v_add_f32 v21, v21, v188\n"
    "v_mfma_f32_16x16x32_bf16 v[200:203], v[224:227], v[72:75], v[200:203]\n"
    "ds_read_b64_tr_b16 v[254:255], v3 offset:33792\n"
    "v_exp_f32 v191, v191\n"
    "v_mfma_f32_16x16x32_bf16 v[204:207], v[228:231], v[72:75], v[204:207]\n"
    "ds_read_b64_tr_b16 a[240:241], v4 offset:32768\n"
    "v_add_f32 v21, v21, v189\n"
    "v_mfma_f32_16x16x32_bf16 v[200:203], v[232:235], v[84:87], v[200:203]\n"
    "s_cmp_eq_u32 s69, 1280\n"
    "s_cselect_b32 s69, 0, s69\n"
    "s_xor_b32 s70, s70, 16384\n"
    "s_add_u32 m0, s64, 0\n"
    "s_add_u32 s75, s73, s74\n"
    "buffer_load_dwordx4 v12, s[48:51], s75 offen offset:0 nt lds\n"
    "ds_read_b64_tr_b16 a[242:243], v4 offset:33792\n"
    "v_cvt_pk_bf16_f32 v186, v188, v189\n"
    "v_mfma_f32_16x16x32_bf16 v[204:207], v[236:239], v[84:87], v[204:207]\n"
    "s_waitcnt lgkmcnt(6)\n"
    "ds_read_b64_tr_b16 a[244:245], v5 offset:32768\n"
    "v_add_f32 v21, v21, v190\n"
    "v_add_f32 v21, v21, v191\n"
    "v_mfma_f32_16x16x32_bf16 a[0:3], v[240:243], a[96:99], a[0:3]\n"
    "ds_read_b64_tr_b16 a[246:247], v5 offset:33792\n"
    "v_cvt_pk_bf16_f32 v187, v190, v191\n"
    "v_mfma_f32_16x16x32_bf16 a[4:7], v[240:243], a[100:103], a[4:7]\n"
    "ds_read_b64_tr_b16 a[248:249], v6 offset:32768\n"
    "v_max3_f32 v37, v192, v193, v194\n"
    "v_max3_f32 v36, v195, v196, v197\n"
    "v_mfma_f32_16x16x32_bf16 a[8:11], v[240:243], a[104:107], a[8:11]\n"
    "ds_read_b64_tr_b16 a[250:251], v6 offset:33792\n"
    "v_max3_f32 v37, v37, v198, v199\n"
    "v_mfma_f32_16x16x32_bf16 a[20:23], v[244:247], a[104:107], a[20:23]\n"
    "ds_read_b64_tr_b16 a[252:253], v7 offset:32768\n"
    "v_max_f32 v37, v37, v36\n"
    "v_cmp_nge_f32 vcc, s77, v37\n"
    "v_mfma_f32_16x16x32_bf16 a[16:19], v[244:247], a[100:103], a[16:19]\n"
    "s_cbranch_vccnz L_stub101b_%=\n"
    "L_back101b_%=:\n"
    "ds_read_b64_tr_b16 a[254:255], v7 offset:33792\n"
    "buffer_load_dwordx4 v12, s[48:51], s75 offen offset:1024 nt lds\n"
    "v_mfma_f32_16x16x32_bf16 a[12:15], v[244:247], a[96:99], a[12:15]\n"
    "s_waitcnt lgkmcnt(6)\n"
    "ds_read_b64_tr_b16 v[208:209], v0 offset:0\n"
    "v_exp_f32 v192, v192\n"
    "v_mfma_f32_16x16x32_bf16 a[24:27], v[248:251], a[96:99], a[24:27]\n"
    "ds_read_b64_tr_b16 v[210:211], v0 offset:1024\n"
    "buffer_load_dwordx4 v13, s[48:51], s75 offen offset:2048 nt lds\n"
    "v_exp_f32 v193, v193\n"
    "v_mfma_f32_16x16x32_bf16 a[28:31], v[248:251], a[100:103], a[28:31]\n"
    "ds_read_b64_tr_b16 v[212:213], v1 offset:0\n"
    "buffer_load_dwordx4 v13, s[48:51], s75 offen offset:3072 nt lds\n"
    "v_mfma_f32_16x16x32_bf16 a[32:35], v[248:251], a[104:107], a[32:35]\n"
    "ds_read_b64_tr_b16 v[214:215], v1 offset:1024\n"
    "v_exp_f32 v194, v194\n"
    "v_mfma_f32_16x16x32_bf16 a[44:47], v[252:255], a[104:107], a[44:47]\n"
    "ds_read_b64_tr_b16 v[216:217], v2 offset:0\n"
    "v_add_f32 v22, v22, v192\n"
    "v_mfma_f32_16x16x32_bf16 a[40:43], v[252:255], a[100:103], a[40:43]\n"
    "ds_read_b64_tr_b16 v[218:219], v2 offset:1024\n"
    "v_exp_f32 v195, v195\n"
    "v_mfma_f32_16x16x32_bf16 a[36:39], v[252:255], a[96:99], a[36:39]\n"
    "ds_read_b64_tr_b16 v[220:221], v3 offset:0\n"
    "v_add_f32 v22, v22, v193\n"
    "v_mfma_f32_16x16x32_bf16 a[48:51], a[240:243], a[96:99], a[48:51]\n"
    "ds_read_b64_tr_b16 v[222:223], v3 offset:1024\n"
    "v_cvt_pk_bf16_f32 v192, v192, v193\n"
    "v_exp_f32 v196, v196\n"
    "v_mfma_f32_16x16x32_bf16 a[52:55], a[240:243], a[100:103], a[52:55]\n"
    "ds_read_b64_tr_b16 v[224:225], v4 offset:0\n"
    "v_add_f32 v22, v22, v194\n"
    "v_mfma_f32_16x16x32_bf16 a[56:59], a[240:243], a[104:107], a[56:59]\n"
    "s_waitcnt lgkmcnt(6)\n"
    "ds_read_b64_tr_b16 v[226:227], v4 offset:1024\n"
    "v_exp_f32 v197, v197\n"
    "v_mfma_f32_16x16x32_bf16 a[68:71], a[244:247], a[104:107], a[68:71]\n"
    "ds_read_b64_tr_b16 v[228:229], v5 offset:0\n"
    "v_add_f32 v22, v22, v195\n"
    "v_mfma_f32_16x16x32_bf16 a[64:67], a[244:247], a[100:103], a[64:67]\n"
    "ds_read_b64_tr_b16 v[230:231], v5 offset:1024\n"
    "v_cvt_pk_bf16_f32 v193, v194, v195\n"
    "v_mfma_f32_16x16x32_bf16 a[60:63], a[244:247], a[96:99], a[60:63]\n"
    "ds_read_b64_tr_b16 v[232:233], v6 offset:0\n"
    "v_exp_f32 v198, v198\n"
    "v_mfma_f32_16x16x32_bf16 a[72:75], a[248:251], a[96:99], a[72:75]\n"
    "ds_read_b64_tr_b16 v[234:235], v6 offset:1024\n"
    "ds_read_b64_tr_b16 v[236:237], v7 offset:0\n"
    "ds_read_b64_tr_b16 v[238:239], v7 offset:1024\n"
    "v_mfma_f32_16x16x32_bf16 a[76:79], a[248:251], a[100:103], a[76:79]\n"
    "v_add_f32 v22, v22, v196\n"
    "v_exp_f32 v199, v199\n"
    "v_mfma_f32_16x16x32_bf16 a[80:83], a[248:251], a[104:107], a[80:83]\n"
    "v_add_f32 v22, v22, v197\n"
    "v_cvt_pk_bf16_f32 v194, v196, v197\n"
    "v_mfma_f32_16x16x32_bf16 a[92:95], a[252:255], a[104:107], a[92:95]\n"
    "v_add_f32 v22, v22, v198\n"
    "v_add_f32 v22, v22, v199\n"
    "v_mfma_f32_16x16x32_bf16 a[88:91], a[252:255], a[100:103], a[88:91]\n"
    "v_cvt_pk_bf16_f32 v195, v198, v199\n"
    "v_max3_f32 v37, v200, v201, v202\n"
    "v_mfma_f32_16x16x32_bf16 a[84:87], a[252:255], a[96:99], a[84:87]\n"
    "v_max3_f32 v36, v203, v204, v205\n"
    "v_max3_f32 v37, v37, v206, v207\n"
    "s_waitcnt vmcnt(15) lgkmcnt(0)\n"
    "s_barrier\n"
    "v_mfma_f32_16x16x32_bf16 v[88:91], v[208:211], v[184:187], v[88:91]\n"
    "ds_read_b128 v[240:243], v8 offset:16384\n"
    "v_max_f32 v37, v37, v36\n"
    "v_mfma_f32_16x16x32_bf16 v[100:103], v[212:215], v[184:187], v[100:103]\n"
    "ds_read_b128 v[244:247], v8 offset:17408\n"
    "v_cmp_nge_f32 vcc, s77, v37\n"
    "v_mfma_f32_16x16x32_bf16 v[112:115], v[216:219], v[184:187], v[112:115]\n"
    "s_add_u32 m0, s64, 32768\n"
    "s_add_u32 s73, s73, 8192\n"
    "buffer_load_dwordx4 v14, s[52:55], s75 offen offset:0 nt lds\n"
    "v_mfma_f32_16x16x32_bf16 v[124:127], v[220:223], v[184:187], v[124:127]\n"
    "s_cbranch_vccnz L_stub102b_%=\n"
    "L_back102b_%=:\n"
    "ds_read_b128 v[248:251], v9 offset:16384\n"
    "v_mfma_f32_16x16x32_bf16 v[136:139], v[224:227], v[184:187], v[136:139]\n"
    "ds_read_b128 v[252:255], v9 offset:17408\n"
    "v_exp_f32 v200, v200\n"
    "v_mfma_f32_16x16x32_bf16 v[148:151], v[228:231], v[184:187], v[148:151]\n"
    "ds_read_b128 a[240:243], v10 offset:16384\n"
    "buffer_load_dwordx4 v14, s[52:55], s75 offen offset:1024 nt lds\n"
    "v_mfma_f32_16x16x32_bf16 v[160:163], v[232:235], v[184:187], v[160:163]\n"
    "v_exp_f32 v201, v201\n"
    "v_mfma_f32_16x16x32_bf16 v[172:175], v[236:239], v[184:187], v[172:175]\n"
    "ds_read_b128 a[244:247], v10 offset:17408\n"
    "v_exp_f32 v202, v202\n"
    "v_mfma_f32_16x16x32_bf16 v[176:179], v[236:239], v[192:195], v[176:179]\n"
    "ds_read_b128 a[248:251], v11 offset:16384\n"
    "v_add_f32 v23, v23, v200\n"
    "v_mfma_f32_16x16x32_bf16 v[164:167], v[232:235], v[192:195], v[164:167]\n"
    "ds_read_b128 a[252:255], v11 offset:17408\n"
    "buffer_load_dwordx4 v15, s[52:55], s75 offen offset:2048 nt lds\n"
    "v_exp_f32 v203, v203\n"
    "v_mfma_f32_16x16x32_bf16 v[152:155], v[228:231], v[192:195], v[152:155]\n"
    "v_add_f32 v23, v23, v201\n"
    "v_cvt_pk_bf16_f32 v200, v200, v201\n"
    "v_mfma_f32_16x16x32_bf16 v[140:143], v[224:227], v[192:195], v[140:143]\n"
    "v_exp_f32 v204, v204\n"
    "v_mfma_f32_16x16x32_bf16 v[128:131], v[220:223], v[192:195], v[128:131]\n"
    "buffer_load_dwordx4 v15, s[52:55], s75 offen offset:3072 nt lds\n"
    "v_add_f32 v23, v23, v202\n"
    "v_mfma_f32_16x16x32_bf16 v[116:119], v[216:219], v[192:195], v[116:119]\n"
    "v_exp_f32 v205, v205\n"
    "v_mfma_f32_16x16x32_bf16 v[104:107], v[212:215], v[192:195], v[104:107]\n"
    "v_add_f32 v23, v23, v203\n"
    "v_cvt_pk_bf16_f32 v201, v202, v203\n"
    "v_mfma_f32_16x16x32_bf16 v[92:95], v[208:211], v[192:195], v[92:95]\n"
    "s_waitcnt lgkmcnt(1)\n"
    "s_and_b32 s73, s73, 0x1ffff\n"
    "s_add_u32 m0, s64, 65536\n"
    "s_add_u32 s71, s67, s68\n"
    "buffer_load_dwordx4 v18, s[44:47], s71 offen offset:0  lds\n"
    "v_mfma_f32_16x16x32_bf16 v[184:187], v[240:243], v[40:43], v[24:27]\n"
    "v_exp_f32 v206, v206\n"
    "v_mfma_f32_16x16x32_bf16 v[188:191], v[244:247], v[40:43], v[24:27]\n"
    "v_add_f32 v23, v23, v204\n"
    "v_mfma_f32_16x16x32_bf16 v[184:187], v[248:251], v[52:55], v[184:187]\n"
    "v_exp_f32 v207, v207\n"
    "v_mfma_f32_16x16x32_bf16 v[188:191], v[252:255], v[52:55], v[188:191]\n"
    "s_add_u32 s72, s71, 31744\n"
    "buffer_load_dwordx4 v19, s[44:47], s71 offen offset:2048  lds\n"
    "v_add_f32 v23, v23, v205\n"
    "v_mfma_f32_16x16x32_bf16 v[184:187], a[240:243], v[64:67], v[184:187]\n"
    "v_cvt_pk_bf16_f32 v202, v204, v205\n"
    "v_add_f32 v23, v23, v206\n"
    "v_mfma_f32_16x16x32_bf16 v[188:191], a[244:247], v[64:67], v[188:191]\n"
    "v_add_f32 v23, v23, v207\n"
    "v_cvt_pk_bf16_f32 v203, v206, v207\n"
    "v_mfma_f32_16x16x32_bf16 v[184:187], a[248:251], v[76:79], v[184:187]\n"
    "s_waitcnt lgkmcnt(0)\n"
    "v_mfma_f32_16x16x32_bf16 v[188:191], a[252:255], v[76:79], v[188:191]\n"
    "s_nop 1\n"
    "v_mfma_f32_16x16x32_bf16 v[96:99], v[208:211], v[200:203], v[96:99]\n"
    "s_nop 1\n"
    "v_mfma_f32_16x16x32_bf16 v[108:111], v[212:215], v[200:203], v[108:111]\n"
    "s_add_u32 s67, s67, 262144\n"
    "buffer_load_dwordx4 v18, s[44:47], s72 offen offset:1024  lds\n"
    "s_nop 0\n"
    "v_mfma_f32_16x16x32_bf16 v[120:123], v[216:219], v[200:203], v[120:123]\n"
    "v_max3_f32 v37, v184, v185, v186\n"
    "v_max3_f32 v36, v187, v188, v189\n"
    "v_max3_f32 v37, v37, v190, v191\n"
    "v_mfma_f32_16x16x32_bf16 v[132:135], v[220:223], v[200:203], v[132:135]\n"
    "v_max_f32 v37, v37, v36\n"
    "v_cmp_nge_f32 vcc, s77, v37\n"
    "v_mfma_f32_16x16x32_bf16 v[144:147], v[224:227], v[200:203], v[144:147]\n"
    "s_cbranch_vccnz L_stub110b_%=\n"
    "L_back110b_%=:\n"
    "buffer_load_dwordx4 v19, s[44:47], s72 offen offset:3072  lds\n"
    "v_mfma_f32_16x16x32_bf16 v[156:159], v[228:231], v[200:203], v[156:159]\n"
    "v_exp_f32 v184, v184\n"
    "v_mfma_f32_16x16x32_bf16 v[168:171], v[232:235], v[200:203], v[168:171]\n"
    "v_exp_f32 v185, v185\n"
    "v_mfma_f32_16x16x32_bf16 v[180:183], v[236:239], v[200:203], v[180:183]\n"
    "v_exp_f32 v186, v186\n"
    "s_waitcnt vmcnt(15)\n"
    "s_barrier\n"
    "v_mfma_f32_16x16x32_bf16 v[192:195], v[240:243], v[44:47], v[28:31]\n"
    "ds_read_b128 a[96:99], v17 offset:0\n"
    "v_add_f32 v21, v21, v184\n"
    "v_mfma_f32_16x16x32_bf16 v[196:199], v[244:247], v[44:47], v[28:31]\n"
    "ds_read_b64_tr_b16 v[208:209], v0 offset:49152\n"
    "v_mfma_f32_16x16x32_bf16 v[192:195], v[248:251], v[56:59], v[192:195]\n"
    "s_add_u32 m0, s64, 16384\n"
    "s_add_u32 s75, s73, s74\n"
    "buffer_load_dwordx4 v12, s[48:51], s75 offen offset:0 nt lds\n"
    "v_exp_f32 v187, v187\n"
    "v_mfma_f32_16x16x32_bf16 v[196:199], v[252:255], v[56:59], v[196:199]\n"
    "ds_read_b64_tr_b16 v[210:211], v0 offset:50176\n"
    "v_add_f32 v21, v21, v185\n"
    "v_mfma_f32_16x16x32_bf16 v[192:195], a[240:243], v[68:71], v[192:195]\n"
    "ds_read_b128 a[100:103], v17 offset:2048\n"
    "v_cvt_pk_bf16_f32 v184, v184, v185\n"
    "v_mfma_f32_16x16x32_bf16 v[196:199], a[244:247], v[68:71], v[196:199]\n"
    "ds_read_b64_tr_b16 v[212:213], v1 offset:49152\n"
    "v_exp_f32 v188, v188\n"
    "v_mfma_f32_16x16x32_bf16 v[192:195], a[248:251], v[80:83], v[192:195]\n"
    "ds_read_b64_tr_b16 v[214:215], v1 offset:50176\n"
    "v_add_f32 v21, v21, v186\n"
    "v_mfma_f32_16x16x32_bf16 v[196:199], a[252:255], v[80:83], v[196:199]\n"
    "ds_read_b128 a[104:107], v17 offset:4096\n"
    "v_exp_f32 v189, v189\n"
    "v_mfma_f32_16x16x32_bf16 v[200:203], v[240:243], v[48:51], v[32:35]\n"
    "v_xor_b32 v17, 16384, v17\n"
    "v_add_f32 v21, v21, v187\n"
    "v_mfma_f32_16x16x32_bf16 v[204:207], v[244:247], v[48:51], v[32:35]\n"
    "ds_read_b64_tr_b16 v[216:217], v2 offset:49152\n"
    "v_cvt_pk_bf16_f32 v185, v186, v187\n"
    "v_mfma_f32_16x16x32_bf16 v[200:203], v[248:251], v[60:63], v[200:203]\n"
    "ds_read_b64_tr_b16 v[218:219], v2 offset:50176\n"
    "buffer_load_dwordx4 v12, s[48:51], s75 offen offset:1024 nt lds\n"
    "v_exp_f32 v190, v190\n"
    "v_mfma_f32_16x16x32_bf16 v[204:207], v[252:255], v[60:63], v[204:207]\n"
    "ds_read_b64_tr_b16 v[220:221], v3 offset:49152\n"
    "v_add_f32 v21, v21, v188\n"
    "v_mfma_f32_16x16x32_bf16 v[200:203], a[240:243], v[72:75], v[200:203]\n"
    "ds_read_b64_tr_b16 v[222:223], v3 offset:50176\n"
    "v_exp_f32 v191, v191\n"
    "v_mfma_f32_16x16x32_bf16 v[204:207], a[244:247], v[72:75], v[204:207]\n"
    "ds_read_b64_tr_b16 v[224:225], v4 offset:49152\n"
    "v_add_f32 v21, v21, v189\n"
    "v_mfma_f32_16x16x32_bf16 v[200:203], a[248:251], v[84:87], v[200:203]\n"
    "ds_read_b64_tr_b16 v[226:227], v4 offset:50176\n"
    "v_cvt_pk_bf16_f32 v186, v188, v189\n"
    "v_mfma_f32_16x16x32_bf16 v[204:207], a[252:255], v[84:87], v[204:207]\n"
    "s_waitcnt lgkmcnt(6)\n"
    "ds_read_b64_tr_b16 v[228:229], v5 offset:49152\n"
    "v_add_f32 v21, v21, v190\n"
    "v_add_f32 v21, v21, v191\n"
    "v_mfma_f32_16x16x32_bf16 a[0:3], v[208:211], a[96:99], a[0:3]\n"
    "ds_read_b64_tr_b16 v[230:231], v5 offset:50176\n"
    "buffer_load_dwordx4 v13, s[48:51], s75 offen offset:2048 nt lds\n"
    "v_cvt_pk_bf16_f32 v187, v190, v191\n"
    "v_mfma_f32_16x16x32_bf16 a[4:7], v[208:211], a[100:103], a[4:7]\n"
    "ds_read_b64_tr_b16 v[232:233], v6 offset:49152\n"
    "v_max3_f32 v37, v192, v193, v194\n"
    "v_max3_f32 v36, v195, v196, v197\n"
    "v_mfma_f32_16x16x32_bf16 a[8:11], v[208:211], a[104:107], a[8:11]\n"
    "ds_read_b64_tr_b16 v[234:235], v6 offset:50176\n"
    "v_max3_f32 v37, v37, v198, v199\n"
    "v_mfma_f32_16x16x32_bf16 a[20:23], v[212:215], a[104:107], a[20:23]\n"
    "ds_read_b64_tr_b16 v[236:237], v7 offset:49152\n"
    "v_max_f32 v37, v37, v36\n"
    "v_cmp_nge_f32 vcc, s77, v37\n"
    "v_mfma_f32_16x16x32_bf16 a[16:19], v[212:215], a[100:103], a[16:19]\n"
    "s_cbranch_vccnz L_stub111b_%=\n"
    "L_back111b_%=:\n"
    "ds_read_b64_tr_b16 v[238:239], v7 offset:50176\n"
    "v_mfma_f32_16x16x32_bf16 a[12:15], v[212:215], a[96:99], a[12:15]\n"
    "s_waitcnt lgkmcnt(6)\n"
    "ds_read_b64_tr_b16 v[240:241], v0 offset:16384\n"
    "v_exp_f32 v192, v192\n"
    "v_mfma_f32_16x16x32_bf16 a[24:27], v[216:219], a[96:99], a[24:27]\n"
    "ds_read_b64_tr_b16 v[242:243], v0 offset:17408\n"
    "buffer_load_dwordx4 v13, s[48:51], s75 offen offset:3072 nt lds\n"
    "v_exp_f32 v193, v193\n"
    "v_mfma_f32_16x16x32_bf16 a[28:31], v[216:219], a[100:103], a[28:31]\n"
    "ds_read_b64_tr_b16 v[244:245], v1 offset:16384\n"
    "v_mfma_f32_16x16x32_bf16 a[32:35], v[216:219], a[104:107], a[32:35]\n"
    "ds_read_b64_tr_b16 v[246:247], v1 offset:17408\n"
    "v_exp_f32 v194, v194\n"
    "v_mfma_f32_16x16x32_bf16 a[44:47], v[220:223], a[104:107], a[44:47]\n"
    "ds_read_b64_tr_b16 v[248:249], v2 offset:16384\n"
    "v_add_f32 v22, v22, v192\n"
    "v_mfma_f32_16x16x32_bf16 a[40:43], v[220:223], a[100:103], a[40:43]\n"
    "ds_read_b64_tr_b16 v[250:251], v2 offset:17408\n"
    "v_exp_f32 v195, v195\n"
    "v_mfma_f32_16x16x32_bf16 a[36:39], v[220:223], a[96:99], a[36:39]\n"
    "ds_read_b64_tr_b16 v[252:253], v3 offset:16384\n"
    "v_add_f32 v22, v22, v193\n"
    "v_mfma_f32_16x16x32_bf16 a[48:51], v[224:227], a[96:99], a[48:51]\n"
    "ds_read_b64_tr_b16 v[254:255], v3 offset:17408\n"
    "v_cvt_pk_bf16_f32 v192, v192, v193\n"
    "v_exp_f32 v196, v196\n"
    "v_mfma_f32_16x16x32_bf16 a[52:55], v[224:227], a[100:103], a[52:55]\n"
    "ds_read_b64_tr_b16 a[240:241], v4 offset:16384\n"
    "v_add_f32 v22, v22, v194\n"
    "v_mfma_f32_16x16x32_bf16 a[56:59], v[224:227], a[104:107], a[56:59]\n"
    "s_waitcnt lgkmcnt(6)\n"
    "ds_read_b64_tr_b16 a[242:243], v4 offset:17408\n"
    "v_exp_f32 v197, v197\n"
    "v_mfma_f32_16x16x32_bf16 a[68:71], v[228:231], a[104:107], a[68:71]\n"
    "ds_read_b64_tr_b16 a[244:245], v5 offset:16384\n"
    "v_add_f32 v22, v22, v195\n"
    "v_mfma_f32_16x16x32_bf16 a[64:67], v[228:231], a[100:103], a[64:67]\n"
    "ds_read_b64_tr_b16 a[246:247], v5 offset:17408\n"
    "v_cvt_pk_bf16_f32 v193, v194, v195\n"
    "v_mfma_f32_16x16x32_bf16 a[60:63], v[228:231], a[96:99], a[60:63]\n"
    "ds_read_b64_tr_b16 a[248:249], v6 offset:16384\n"
    "v_exp_f32 v198, v198\n"
    "v_mfma_f32_16x16x32_bf16 a[72:75], v[232:235], a[96:99], a[72:75]\n"
    "ds_read_b64_tr_b16 a[250:251], v6 offset:17408\n"
    "ds_read_b64_tr_b16 a[252:253], v7 offset:16384\n"
    "ds_read_b64_tr_b16 a[254:255], v7 offset:17408\n"
    "v_mfma_f32_16x16x32_bf16 a[76:79], v[232:235], a[100:103], a[76:79]\n"
    "v_add_f32 v22, v22, v196\n"
    "v_exp_f32 v199, v199\n"
    "v_mfma_f32_16x16x32_bf16 a[80:83], v[232:235], a[104:107], a[80:83]\n"
    "v_add_f32 v22, v22, v197\n"
    "v_cvt_pk_bf16_f32 v194, v196, v197\n"
    "v_mfma_f32_16x16x32_bf16 a[92:95], v[236:239], a[104:107], a[92:95]\n"
    "v_add_f32 v22, v22, v198\n"
    "v_add_f32 v22, v22, v199\n"
    "v_mfma_f32_16x16x32_bf16 a[88:91], v[236:239], a[100:103], a[88:91]\n"
    "v_cvt_pk_bf16_f32 v195, v198, v199\n"
    "v_max3_f32 v37, v200, v201, v202\n"
    "v_mfma_f32_16x16x32_bf16 a[84:87], v[236:239], a[96:99], a[84:87]\n"
    "v_max3_f32 v36, v203, v204, v205\n"
    "v_max3_f32 v37, v37, v206, v207\n"
    "s_waitcnt vmcnt(12) lgkmcnt(0)\n"
    "s_barrier\n"
    "v_mfma_f32_16x16x32_bf16 v[88:91], v[240:243], v[184:187], v[88:91]\n"
    "ds_read_b128 v[208:211], v8 offset:0\n"
    "v_max_f32 v37, v37, v36\n"
    "v_mfma_f32_16x16x32_bf16 v[100:103], v[244:247], v[184:187], v[100:103]\n"
    "ds_read_b128 v[212:215], v8 offset:1024\n"
    "v_cmp_nge_f32 vcc, s77, v37\n"
    "v_mfma_f32_16x16x32_bf16 v[112:115], v[248:251], v[184:187], v[112:115]\n"
    "s_add_u32 m0, s64, 49152\n"
    "s_add_u32 s73, s73, 8192\n"
    "buffer_load_dwordx4 v14, s[52:55], s75 offen offset:0 nt lds\n"
    "v_mfma_f32_16x16x32_bf16 v[124:127], v[252:255], v[184:187], v[124:127]\n"
    "s_cbranch_vccnz L_stub112b_%=\n"
    "L_back112b_%=:\n"
    "ds_read_b128 v[216:219], v9 offset:0\n"
    "v_mfma_f32_16x16x32_bf16 v[136:139], a[240:243], v[184:187], v[136:139]\n"
    "ds_read_b128 v[220:223], v9 offset:1024\n"
    "v_exp_f32 v200, v200\n"
    "v_mfma_f32_16x16x32_bf16 v[148:151], a[244:247], v[184:187], v[148:151]\n"
    "ds_read_b128 v[224:227], v10 offset:0\n"
    "buffer_load_dwordx4 v14, s[52:55], s75 offen offset:1024 nt lds\n"
    "v_mfma_f32_16x16x32_bf16 v[160:163], a[248:251], v[184:187], v[160:163]\n"
    "v_exp_f32 v201, v201\n"
    "v_mfma_f32_16x16x32_bf16 v[172:175], a[252:255], v[184:187], v[172:175]\n"
    "ds_read_b128 v[228:231], v10 offset:1024\n"
    "v_exp_f32 v202, v202\n"
    "v_mfma_f32_16x16x32_bf16 v[176:179], a[252:255], v[192:195], v[176:179]\n"
    "ds_read_b128 v[232:235], v11 offset:0\n"
    "v_add_f32 v23, v23, v200\n"
    "v_mfma_f32_16x16x32_bf16 v[164:167], a[248:251], v[192:195], v[164:167]\n"
    "ds_read_b128 v[236:239], v11 offset:1024\n"
    "buffer_load_dwordx4 v15, s[52:55], s75 offen offset:2048 nt lds\n"
    "v_exp_f32 v203, v203\n"
    "v_mfma_f32_16x16x32_bf16 v[152:155], a[244:247], v[192:195], v[152:155]\n"
    "v_add_f32 v23, v23, v201\n"
    "v_cvt_pk_bf16_f32 v200, v200, v201\n"
    "v_mfma_f32_16x16x32_bf16 v[140:143], a[240:243], v[192:195], v[140:143]\n"
    "buffer_load_dwordx4 v15, s[52:55], s75 offen offset:3072 nt lds\n"
    "v_mfma_f32_16x16x32_bf16 v[128:131], v[252:255], v[192:195], v[128:131]\n"
    "v_exp_f32 v204, v204\n"
    "v_mfma_f32_16x16x32_bf16 v[116:119], v[248:251], v[192:195], v[116:119]\n"
    "v_add_f32 v23, v23, v202\n"
    "v_mfma_f32_16x16x32_bf16 v[104:107], v[244:247], v[192:195], v[104:107]\n"
    "v_exp_f32 v205, v205\n"
    "v_mfma_f32_16x16x32_bf16 v[92:95], v[240:243], v[192:195], v[92:95]\n"
    "s_waitcnt lgkmcnt(1)\n"
    "s_and_b32 s73, s73, 0x1ffff\n"
    "s_add_u32 m0, s64, 81920\n"
    "s_add_u32 s71, s67, s68\n"
    "buffer_load_dwordx4 v18, s[44:47], s71 offen offset:0  lds\n"
    "v_add_f32 v23, v23, v203\n"
    "v_mfma_f32_16x16x32_bf16 v[184:187], v[208:211], v[40:43], v[24:27]\n"
    "v_cvt_pk_bf16_f32 v201, v202, v203\n"
    "v_mfma_f32_16x16x32_bf16 v[188:191], v[212:215], v[40:43], v[24:27]\n"
    "v_exp_f32 v206, v206\n"
    "v_mfma_f32_16x16x32_bf16 v[184:187], v[216:219], v[52:55], v[184:187]\n"
    "s_add_u32 s72, s71, 31744\n"
    "buffer_load_dwordx4 v19, s[44:47], s71 offen offset:2048  lds\n"
    "v_add_f32 v23, v23, v204\n"
    "v_mfma_f32_16x16x32_bf16 v[188:191], v[220:223], v[52:55], v[188:191]\n"
    "v_exp_f32 v207, v207\n"
    "v_mfma_f32_16x16x32_bf16 v[184:187], v[224:227], v[64:67], v[184:187]\n"
    "v_add_f32 v23, v23, v205\n"
    "v_cvt_pk_bf16_f32 v202, v204, v205\n"
    "v_mfma_f32_16x16x32_bf16 v[188:191], v[228:231], v[64:67], v[188:191]\n"
    "v_add_f32 v23, v23, v206\n"
    "v_add_f32 v23, v23, v207\n"
    "v_cvt_pk_bf16_f32 v203, v206, v207\n"
    "v_mfma_f32_16x16x32_bf16 v[184:187], v[232:235], v[76:79], v[184:187]\n"
    "s_waitcnt lgkmcnt(0)\n"
    "s_add_u32 s67, s67, 262144\n"
    "buffer_load_dwordx4 v18, s[44:47], s72 offen offset:1024  lds\n"
    "v_mfma_f32_16x16x32_bf16 v[188:191], v[236:239], v[76:79], v[188:191]\n"
    "s_nop 1\n"
    "v_mfma_f32_16x16x32_bf16 v[96:99], v[240:243], v[200:203], v[96:99]\n"
    "s_nop 1\n"
    "v_mfma_f32_16x16x32_bf16 v[108:111], v[244:247], v[200:203], v[108:111]\n"
    "s_nop 0\n"
    "v_mfma_f32_16x16x32_bf16 v[120:123], v[248:251], v[200:203], v[120:123]\n"
    "v_max3_f32 v37, v184, v185, v186\n"
    "v_max3_f32 v36, v187, v188, v189\n"
    "v_max3_f32 v37, v37, v190, v191\n"
    "v_mfma_f32_16x16x32_bf16 v[132:135], v[252:255], v[200:203], v[132:135]\n"
    "v_max_f32 v37, v37, v36\n"
    "v_cmp_nge_f32 vcc, s77, v37\n"
    "v_mfma_f32_16x16x32_bf16 v[144:147], a[240:243], v[200:203], v[144:147]\n"
    "s_cmp_eq_u32 s67, 5242880\n"
    "s_cselect_b32 s67, 0, s67\n"
    "buffer_load_dwordx4 v19, s[44:47], s72 offen offset:3072  lds\n"
    "s_cbranch_vccnz L_stub100b_%=\n"
    "L_back100b_%=:\n"
    "v_mfma_f32_16x16x32_bf16 v[156:159], a[244:247], v[200:203], v[156:159]\n"
    "v_exp_f32 v184, v184\n"
    "v_mfma_f32_16x16x32_bf16 v[168:171], a[248:251], v[200:203], v[168:171]\n"
    "v_exp_f32 v185, v185\n"
    "v_mfma_f32_16x16x32_bf16 v[180:183], a[252:255], v[200:203], v[180:183]\n"
    "v_exp_f32 v186, v186\n"
    "s_add_u32 s66, s66, 1\n"
    "s_cmp_lg_u32 s66, 5\n"
    "s_cbranch_scc1 L_r1_%=\n"
    "s_and_b32 s78, s21, 7\n"
    "s_mov_b32 s87, s79\n"
    "s_mul_i32 s39, s78, 40960\n"
    "s_add_u32 s79, s79, s39\n"
    "s_mov_b32 s68, -43008\n"
    "s_mov_b32 s74, 262144\n"
    "v_accvgpr_read_b32 v18, a210\n"
    "v_accvgpr_read_b32 v37, a213\n"
    "buffer_store_dword a212, v37, s[92:95], 0 offen offset:256 sc1\n"
    "s_waitcnt vmcnt(12)\n"
    "s_barrier\n"
    "v_mfma_f32_16x16x32_bf16 v[192:195], v[208:211], v[44:47], v[28:31]\n"
    "ds_read_b128 a[96:99], v16 offset:0\n"
    "v_add_f32 v21, v21, v184\n"
    "v_mfma_f32_16x16x32_bf16 v[196:199], v[212:215], v[44:47], v[28:31]\n"
    "ds_read_b64_tr_b16 v[240:241], v0 offset:32768\n"
    "v_mfma_f32_16x16x32_bf16 v[192:195], v[216:219], v[56:59], v[192:195]\n"
    "s_add_u32 m0, s64, 0\n"
    "s_add_u32 s75, s73, s74\n"
    "buffer_load_dwordx4 v12, s[48:51], s75 offen offset:0 nt lds\n"
    "v_exp_f32 v187, v187\n"
    "v_mfma_f32_16x16x32_bf16 v[196:199], v[220:223], v[56:59], v[196:199]\n"
    "ds_read_b64_tr_b16 v[242:243], v0 offset:33792\n"
    "v_add_f32 v21, v21, v185\n"
    "v_mfma_f32_16x16x32_bf16 v[192:195], v[224:227], v[68:71], v[192:195]\n"
    "ds_read_b128 a[100:103], v16 offset:2048\n"
    "v_cvt_pk_bf16_f32 v184, v184, v185\n"
    "v_mfma_f32_16x16x32_bf16 v[196:199], v[228:231], v[68:71], v[196:199]\n"
    "ds_read_b64_tr_b16 v[244:245], v1 offset:32768\n"
    "v_exp_f32 v188, v188\n"
    "v_mfma_f32_16x16x32_bf16 v[192:195], v[232:235], v[80:83], v[192:195]\n"
    "ds_read_b64_tr_b16 v[246:247], v1 offset:33792\n"
    "v_add_f32 v21, v21, v186\n"
    "v_mfma_f32_16x16x32_bf16 v[196:199], v[236:239], v[80:83], v[196:199]\n"
    "ds_read_b128 a[104:107], v16 offset:4096\n"
    "v_exp_f32 v189, v189\n"
    "v_mfma_f32_16x16x32_bf16 v[200:203], v[208:211], v[48:51], v[32:35]\n"
    "v_xor_b32 v16, 16384, v16\n"
    "v_add_f32 v21, v21, v187\n"
    "v_mfma_f32_16x16x32_bf16 v[204:207], v[212:215], v[48:51], v[32:35]\n"
    "ds_read_b64_tr_b16 v[248:249], v2 offset:32768\n"
    "v_cvt_pk_bf16_f32 v185, v186, v187\n"
    "v_mfma_f32_16x16x32_bf16 v[200:203], v[216:219], v[60:63], v[200:203]\n"
    "ds_read_b64_tr_b16 v[250:251], v2 offset:33792\n"
    "buffer_load_dwordx4 v12, s[48:51], s75 offen offset:1024 nt lds\n"
    "v_exp_f32 v190, v190\n"
    "v_mfma_f32_16x16x32_bf16 v[204:207], v[220:223], v[60:63], v[204:207]\n"
    "ds_read_b64_tr_b16 v[252:253], v3 offset:32768\n"
    "v_add_f32 v21, v21, v188\n"
    "v_mfma_f32_16x16x32_bf16 v[200:203], v[224:227], v[72:75], v[200:203]\n"
    "ds_read_b64_tr_b16 v[254:255], v3 offset:33792\n"
    "v_exp_f32 v191, v191\n"
    "v_mfma_f32_16x16x32_bf16 v[204:207], v[228:231], v[72:75], v[204:207]\n"
    "ds_read_b64_tr_b16 a[240:241], v4 offset:32768\n"
    "v_add_f32 v21, v21, v189\n"
    "v_mfma_f32_16x16x32_bf16 v[200:203], v[232:235], v[84:87], v[200:203]\n"
    "ds_read_b64_tr_b16 a[242:243], v4 offset:33792\n"
    "v_cvt_pk_bf16_f32 v186, v188, v189\n"
    "v_mfma_f32_16x16x32_bf16 v[204:207], v[236:239], v[84:87], v[204:207]\n"
    "s_waitcnt lgkmcnt(6)\n"
    "ds_read_b64_tr_b16 a[244:245], v5 offset:32768\n"
    "v_add_f32 v21, v21, v190\n"
    "v_add_f32 v21, v21, v191\n"
    "v_mfma_f32_16x16x32_bf16 a[0:3], v[240:243], a[96:99], a[0:3]\n"
    "ds_read_b64_tr_b16 a[246:247], v5 offset:33792\n"
    "buffer_load_dwordx4 v13, s[48:51], s75 offen offset:2048 nt lds\n"
    "v_cvt_pk_bf16_f32 v187, v190, v191\n"
    "v_mfma_f32_16x16x32_bf16 a[4:7], v[240:243], a[100:103], a[4:7]\n"
    "ds_read_b64_tr_b16 a[248:249], v6 offset:32768\n"
    "v_max3_f32 v37, v192, v193, v194\n"
    "v_max3_f32 v36, v195, v196, v197\n"
    "v_mfma_f32_16x16x32_bf16 a[8:11], v[240:243], a[104:107], a[8:11]\n"
    "ds_read_b64_tr_b16 a[250:251], v6 offset:33792\n"
    "v_max3_f32 v37, v37, v198, v199\n"
    "v_mfma_f32_16x16x32_bf16 a[20:23], v[244:247], a[104:107], a[20:23]\n"
    "ds_read_b64_tr_b16 a[252:253], v7 offset:32768\n"
    "v_max_f32 v37, v37, v36\n"
    "v_cmp_nge_f32 vcc, s77, v37\n"
    "v_mfma_f32_16x16x32_bf16 a[16:19], v[244:247], a[100:103], a[16:19]\n"
    "s_cbranch_vccnz L_stub101c_%=\n"
    "L_back101c_%=:\n"
    "ds_read_b64_tr_b16 a[254:255], v7 offset:33792\n"
    "v_mfma_f32_16x16x32_bf16 a[12:15], v[244:247], a[96:99], a[12:15]\n"
    "s_waitcnt lgkmcnt(6)\n"
    "ds_read_b64_tr_b16 v[208:209], v0 offset:0\n"
    "v_exp_f32 v192, v192\n"
    "v_mfma_f32_16x16x32_bf16 a[24:27], v[248:251], a[96:99], a[24:27]\n"
    "ds_read_b64_tr_b16 v[210:211], v0 offset:1024\n"
    "buffer_load_dwordx4 v13, s[48:51], s75 offen offset:3072 nt lds\n"
    "v_exp_f32 v193, v193\n"
    "v_mfma_f32_16x16x32_bf16 a[28:31], v[248:251], a[100:103], a[28:31]\n"
    "ds_read_b64_tr_b16 v[212:213], v1 offset:0\n"
    "v_mfma_f32_16x16x32_bf16 a[32:35], v[248:251], a[104:107], a[32:35]\n"
    "ds_read_b64_tr_b16 v[214:215], v1 offset:1024\n"
    "v_exp_f32 v194, v194\n"
    "v_mfma_f32_16x16x32_bf16 a[44:47], v[252:255], a[104:107], a[44:47]\n"
    "ds_read_b64_tr_b16 v[216:217], v2 offset:0\n"
    "v_add_f32 v22, v22, v192\n"
    "v_mfma_f32_16x16x32_bf16 a[40:43], v[252:255], a[100:103], a[40:43]\n"
    "ds_read_b64_tr_b16 v[218:219], v2 offset:1024\n"
    "v_exp_f32 v195, v195\n"
    "v_mfma_f32_16x16x32_bf16 a[36:39], v[252:255], a[96:99], a[36:39]\n"
    "ds_read_b64_tr_b16 v[220:221], v3 offset:0\n"
    "v_add_f32 v22, v22, v193\n"
    "v_mfma_f32_16x16x32_bf16 a[48:51], a[240:243], a[96:99], a[48:51]\n"
    "ds_read_b64_tr_b16 v[222:223], v3 offset:1024\n"
    "v_cvt_pk_bf16_f32 v192, v192, v193\n"
    "v_exp_f32 v196, v196\n"
    "v_mfma_f32_16x16x32_bf16 a[52:55], a[240:243], a[100:103], a[52:55]\n"
    "ds_read_b64_tr_b16 v[224:225], v4 offset:0\n"
    "v_add_f32 v22, v22, v194\n"
    "v_mfma_f32_16x16x32_bf16 a[56:59], a[240:243], a[104:107], a[56:59]\n"
    "s_waitcnt lgkmcnt(6)\n"
    "ds_read_b64_tr_b16 v[226:227], v4 offset:1024\n"
    "v_exp_f32 v197, v197\n"
    "v_mfma_f32_16x16x32_bf16 a[68:71], a[244:247], a[104:107], a[68:71]\n"
    "ds_read_b64_tr_b16 v[228:229], v5 offset:0\n"
    "v_add_f32 v22, v22, v195\n"
    "v_mfma_f32_16x16x32_bf16 a[64:67], a[244:247], a[100:103], a[64:67]\n"
    "ds_read_b64_tr_b16 v[230:231], v5 offset:1024\n"
    "v_cvt_pk_bf16_f32 v193, v194, v195\n"
    "v_mfma_f32_16x16x32_bf16 a[60:63], a[244:247], a[96:99], a[60:63]\n"
    "ds_read_b64_tr_b16 v[232:233], v6 offset:0\n"
    "v_exp_f32 v198, v198\n"
    "v_mfma_f32_16x16x32_bf16 a[72:75], a[248:251], a[96:99], a[72:75]\n"
    "ds_read_b64_tr_b16 v[234:235], v6 offset:1024\n"
    "ds_read_b64_tr_b16 v[236:237], v7 offset:0\n"
    "ds_read_b64_tr_b16 v[238:239], v7 offset:1024\n"
    "v_mfma_f32_16x16x32_bf16 a[76:79], a[248:251], a[100:103], a[76:79]\n"
    "v_add_f32 v22, v22, v196\n"
    "v_exp_f32 v199, v199\n"
    "v_mfma_f32_16x16x32_bf16 a[80:83], a[248:251], a[104:107], a[80:83]\n"
    "v_add_f32 v22, v22, v197\n"
    "v_cvt_pk_bf16_f32 v194, v196, v197\n"
    "v_mfma_f32_16x16x32_bf16 a[92:95], a[252:255], a[104:107], a[92:95]\n"
    "v_add_f32 v22, v22, v198\n"
    "v_add_f32 v22, v22, v199\n"
    "v_mfma_f32_16x16x32_bf16 a[88:91], a[252:255], a[100:103], a[88:91]\n"
    "v_cvt_pk_bf16_f32 v195, v198, v199\n"
    "v_max3_f32 v37, v200, v201, v202\n"
    "v_mfma_f32_16x16x32_bf16 a[84:87], a[252:255], a[96:99], a[84:87]\n"
    "v_max3_f32 v36, v203, v204, v205\n"
    "v_max3_f32 v37, v37, v206, v207\n"
    "s_waitcnt vmcnt(12) lgkmcnt(0)\n"
    "s_barrier\n"
    "v_mfma_f32_16x16x32_bf16 v[88:91], v[208:211], v[184:187], v[88:91]\n"
    "ds_read_b128 v[240:243], v8 offset:16384\n"
    "v_max_f32 v37, v37, v36\n"
    "v_mfma_f32_16x16x32_bf16 v[100:103], v[212:215], v[184:187], v[100:103]\n"
    "ds_read_b128 v[244:247], v8 offset:17408\n"
    "v_cmp_nge_f32 vcc, s77, v37\n"
    "v_mfma_f32_16x16x32_bf16 v[112:115], v[216:219], v[184:187], v[112:115]\n"
    "s_add_u32 m0, s64, 32768\n"
    "s_add_u32 s73, s73, 8192\n"
    "buffer_load_dwordx4 v14, s[52:55], s75 offen offset:0 nt lds\n"
    "v_mfma_f32_16x16x32_bf16 v[124:127], v[220:223], v[184:187], v[124:127]\n"
    "s_cbranch_vccnz L_stub102c_%=\n"
    "L_back102c_%=:\n"
    "ds_read_b128 v[248:251], v9 offset:16384\n"
    "v_mfma_f32_16x16x32_bf16 v[136:139], v[224:227], v[184:187], v[136:139]\n"
    "ds_read_b128 v[252:255], v9 offset:17408\n"
    "v_exp_f32 v200, v200\n"
    "v_mfma_f32_16x16x32_bf16 v[148:151], v[228:231], v[184:187], v[148:151]\n"
    "buffer_load_dwordx4 v14, s[52:55], s75 offen offset:1024 nt lds\n"
    "ds_read_b128 a[240:243], v10 offset:16384\n"
    "v_mfma_f32_16x16x32_bf16 v[160:163], v[232:235], v[184:187], v[160:163]\n"
    "v_exp_f32 v201, v201\n"
    "v_mfma_f32_16x16x32_bf16 v[172:175], v[236:239], v[184:187], v[172:175]\n"
    "ds_read_b128 a[244:247], v10 offset:17408\n"
    "v_exp_f32 v202, v202\n"
    "v_mfma_f32_16x16x32_bf16 v[176:179], v[236:239], v[192:195], v[176:179]\n"
    "ds_read_b128 a[248:251], v11 offset:16384\n"
    "buffer_load_dwordx4 v15, s[52:55], s75 offen offset:2048 nt lds\n"
    "v_add_f32 v23, v23, v200\n"
    "v_mfma_f32_16x16x32_bf16 v[164:167], v[232:235], v[192:195], v[164:167]\n"
    "ds_read_b128 a[252:255], v11 offset:17408\n"
    "v_exp_f32 v203, v203\n"
    "v_mfma_f32_16x16x32_bf16 v[152:155], v[228:231], v[192:195], v[152:155]\n"
    "buffer_load_dwordx4 v15, s[52:55], s75 offen offset:3072 nt lds\n"
    "v_add_f32 v23, v23, v201\n"
    "v_mfma_f32_16x16x32_bf16 v[140:143], v[224:227], v[192:195], v[140:143]\n"
    "v_cvt_pk_bf16_f32 v200, v200, v201\n"
    "v_mfma_f32_16x16x32_bf16 v[128:131], v[220:223], v[192:195], v[128:131]\n"
    "v_exp_f32 v204, v204\n"
    "v_mfma_f32_16x16x32_bf16 v[116:119], v[216:219], v[192:195], v[116:119]\n"
    "s_and_b32 s73, s73, 0x1ffff\n"
    "s_add_u32 m0, s65, 143360\n"
    "s_nop 0\n"
    "buffer_load_dwordx4 v18, s[56:59], s79 offen offset:0  lds\n"
    "v_add_f32 v23, v23, v202\n"
    "v_mfma_f32_16x16x32_bf16 v[104:107], v[212:215], v[192:195], v[104:107]\n"
    "v_exp_f32 v205, v205\n"
    "v_mfma_f32_16x16x32_bf16 v[92:95], v[208:211], v[192:195], v[92:95]\n"
    "s_waitcnt lgkmcnt(1)\n"
    "v_add_f32 v23, v23, v203\n"
    "v_cvt_pk_bf16_f32 v201, v202, v203\n"
    "v_mfma_f32_16x16x32_bf16 v[184:187], v[240:243], v[40:43], v[24:27]\n"
    "s_add_u32 m0, s65, 147328\n"
    "s_nop 0\n"
    "buffer_load_dwordx4 v18, s[56:59], s79 offen offset:128  lds\n"
    "v_mfma_f32_16x16x32_bf16 v[188:191], v[244:247], v[40:43], v[24:27]\n"
    "v_exp_f32 v206, v206\n"
    "v_mfma_f32_16x16x32_bf16 v[184:187], v[248:251], v[52:55], v[184:187]\n"
    "v_add_f32 v23, v23, v204\n"
    "v_mfma_f32_16x16x32_bf16 v[188:191], v[252:255], v[52:55], v[188:191]\n"
    "v_exp_f32 v207, v207\n"
    "v_mfma_f32_16x16x32_bf16 v[184:187], a[240:243], v[64:67], v[184:187]\n"
    "v_add_f32 v23, v23, v205\n"
    "v_cvt_pk_bf16_f32 v202, v204, v205\n"
    "v_mfma_f32_16x16x32_bf16 v[188:191], a[244:247], v[64:67], v[188:191]\n"
    "s_add_u32 m0, s65, 151296\n"
    "s_nop 0\n"
    "buffer_load_dwordx4 v18, s[56:59], s79 offen offset:256  lds\n"
    "v_add_f32 v23, v23, v206\n"
    "v_add_f32 v23, v23, v207\n"
    "v_cvt_pk_bf16_f32 v203, v206, v207\n"
    "v_mfma_f32_16x16x32_bf16 v[184:187], a[248:251], v[76:79], v[184:187]\n"
    "s_waitcnt lgkmcnt(0)\n"
    "v_mfma_f32_16x16x32_bf16 v[188:191], a[252:255], v[76:79], v[188:191]\n"
    "s_nop 1\n"
    "v_mfma_f32_16x16x32_bf16 v[96:99], v[208:211], v[200:203], v[96:99]\n"
    "s_nop 1\n"
    "v_mfma_f32_16x16x32_bf16 v[108:111], v[212:215], v[200:203], v[108:111]\n"
    "s_add_u32 m0, s65, 155264\n"
    "s_nop 0\n"
    "buffer_load_dwordx4 v18, s[56:59], s79 offen offset:384  lds\n"
    "s_nop 0\n"
    "v_mfma_f32_16x16x32_bf16 v[120:123], v[216:219], v[200:203], v[120:123]\n"
    "v_max3_f32 v37, v184, v185, v186\n"
    "v_max3_f32 v36, v187, v188, v189\n"
    "v_max3_f32 v37, v37, v190, v191\n"
    "v_mfma_f32_16x16x32_bf16 v[132:135], v[220:223], v[200:203], v[132:135]\n"
    "v_max_f32 v37, v37, v36\n"
    "v_cmp_nge_f32 vcc, s77, v37\n"
    "v_mfma_f32_16x16x32_bf16 v[144:147], v[224:227], v[200:203], v[144:147]\n"
    "s_cbranch_vccnz L_stub110c_%=\n"
    "L_back110c_%=:\n"
    "v_mfma_f32_16x16x32_bf16 v[156:159], v[228:231], v[200:203], v[156:159]\n"
    "s_add_u32 m0, s65, 159232\n"
    "s_nop 0\n"
    "buffer_load_dwordx4 v18, s[56:59], s79 offen offset:512  lds\n"
    "v_exp_f32 v184, v184\n"
    "v_mfma_f32_16x16x32_bf16 v[168:171], v[232:235], v[200:203], v[168:171]\n"
    "v_exp_f32 v185, v185\n"
    "v_mfma_f32_16x16x32_bf16 v[180:183], v[236:239], v[200:203], v[180:183]\n"
    "v_exp_f32 v186, v186\n"
    "s_waitcnt vmcnt(13)\n"
    "s_barrier\n"
    "v_mfma_f32_16x16x32_bf16 v[192:195], v[240:243], v[44:47], v[28:31]\n"
    "ds_read_b128 a[96:99], v17 offset:0\n"
    "v_add_f32 v21, v21, v184\n"
    "v_mfma_f32_16x16x32_bf16 v[196:199], v[244:247], v[44:47], v[28:31]\n"
    "ds_read_b64_tr_b16 v[208:209], v0 offset:49152\n"
    "v_mfma_f32_16x16x32_bf16 v[192:195], v[248:251], v[56:59], v[192:195]\n"
    "s_add_u32 m0, s64, 16384\n"
    "s_add_u32 s75, s73, s74\n"
    "buffer_load_dwordx4 v12, s[48:51], s75 offen offset:0 nt lds\n"
    "v_exp_f32 v187, v187\n"
    "v_mfma_f32_16x16x32_bf16 v[196:199], v[252:255], v[56:59], v[196:199]\n"
    "ds_read_b64_tr_b16 v[210:211], v0 offset:50176\n"
    "v_add_f32 v21, v21, v185\n"
    "v_cvt_pk_bf16_f32 v184, v184, v185\n"
    "v_mfma_f32_16x16x32_bf16 v[192:195], a[240:243], v[68:71], v[192:195]\n"
    "ds_read_b128 a[100:103], v17 offset:2048\n"
    "v_exp_f32 v188, v188\n"
    "v_mfma_f32_16x16x32_bf16 v[196:199], a[244:247], v[68:71], v[196:199]\n"
    "ds_read_b64_tr_b16 v[212:213], v1 offset:49152\n"
    "v_add_f32 v21, v21, v186\n"
    "v_exp_f32 v189, v189\n"
    "v_mfma_f32_16x16x32_bf16 v[192:195], a[248:251], v[80:83], v[192:195]\n"
    "ds_read_b64_tr_b16 v[214:215], v1 offset:50176\n"
    "v_add_f32 v21, v21, v187\n"
    "v_mfma_f32_16x16x32_bf16 v[196:199], a[252:255], v[80:83], v[196:199]\n"
    "ds_read_b128 a[104:107], v17 offset:4096\n"
    "v_cvt_pk_bf16_f32 v185, v186, v187\n"
    "v_exp_f32 v190, v190\n"
    "v_mfma_f32_16x16x32_bf16 v[200:203], v[240:243], v[48:51], v[32:35]\n"
    "v_xor_b32 v17, 16384, v17\n"
    "v_add_f32 v21, v21, v188\n"
    "v_mfma_f32_16x16x32_bf16 v[204:207], v[244:247], v[48:51], v[32:35]\n"
    "ds_read_b64_tr_b16 v[216:217], v2 offset:49152\n"
    "v_exp_f32 v191, v191\n"
    "v_mfma_f32_16x16x32_bf16 v[200:203], v[248:251], v[60:63], v[200:203]\n"
    "ds_read_b64_tr_b16 v[218:219], v2 offset:50176\n"
    "buffer_load_dwordx4 v12, s[48:51], s75 offen offset:1024 nt lds\n"
    "v_add_f32 v21, v21, v189\n"
    "v_cvt_pk_bf16_f32 v186, v188, v189\n"
    "v_mfma_f32_16x16x32_bf16 v[204:207], v[252:255], v[60:63], v[204:207]\n"
    "ds_read_b64_tr_b16 v[220:221], v3 offset:49152\n"
    "v_add_f32 v21, v21, v190\n"
    "v_add_f32 v21, v21, v191\n"
    "v_mfma_f32_16x16x32_bf16 v[200:203], a[240:243], v[72:75], v[200:203]\n"
    "ds_read_b64_tr_b16 v[222:223], v3 offset:50176\n"
    "v_cvt_pk_bf16_f32 v187, v190, v191\n"
    "v_accvgpr_read_b32 v20, a209\n"
    "v_mfma_f32_16x16x32_bf16 v[204:207], a[244:247], v[72:75], v[204:207]\n"
    "ds_read_b64_tr_b16 v[224:225], v4 offset:49152\n"
    "v_add_u32 v20, s86, v20\n"
    "v_mov_b32 v25, v21\n"
    "s_nop 1\n"
    "v_permlane32_swap_b32 v21, v25\n"
    "v_mfma_f32_16x16x32_bf16 v[200:203], a[248:251], v[84:87], v[200:203]\n"
    "ds_read_b64_tr_b16 v[226:227], v4 offset:50176\n"
    "v_add_f32 v21, v21, v25\n"
    "v_mfma_f32_16x16x32_bf16 v[204:207], a[252:255], v[84:87], v[204:207]\n"
    "s_waitcnt lgkmcnt(6)\n"
    "ds_read_b64_tr_b16 v[228:229], v5 offset:49152\n"
    "v_mov_b32 v25, v21\n"
    "s_nop 1\n"
    "v_permlane16_swap_b32 v21, v25\n"
    "v_mfma_f32_16x16x32_bf16 a[0:3], v[208:211], a[96:99], a[0:3]\n"
    "ds_read_b64_tr_b16 v[230:231], v5 offset:50176\n"
    "buffer_load_dwordx4 v13, s[48:51], s75 offen offset:2048 nt lds\n"
    "v_add_f32 v21, v21, v25\n"
    "v_mfma_f32_16x16x32_bf16 a[4:7], v[208:211], a[100:103], a[4:7]\n"
    "ds_read_b64_tr_b16 v[232:233], v6 offset:49152\n"
    "v_rcp_f32 v24, v21\n"
    "v_mfma_f32_16x16x32_bf16 a[8:11], v[208:211], a[104:107], a[8:11]\n"
    "ds_read_b64_tr_b16 v[234:235], v6 offset:50176\n"
    "v_max3_f32 v37, v192, v193, v194\n"
    "v_max3_f32 v36, v195, v196, v197\n"
    "v_mfma_f32_16x16x32_bf16 a[20:23], v[212:215], a[104:107], a[20:23]\n"
    "ds_read_b64_tr_b16 v[236:237], v7 offset:49152\n"
    "v_max3_f32 v37, v37, v198, v199\n"
    "v_max_f32 v37, v37, v36\n"
    "v_mfma_f32_16x16x32_bf16 a[16:19], v[212:215], a[100:103], a[16:19]\n"
    "ds_read_b64_tr_b16 v[238:239], v7 offset:50176\n"
    "v_cmp_nge_f32 vcc, s77, v37\n"
    "s_cbranch_vccnz L_stub111c_%=\n"
    "L_back111c_%=:\n"
    "v_mfma_f32_16x16x32_bf16 a[12:15], v[212:215], a[96:99], a[12:15]\n"
    "s_waitcnt lgkmcnt(6)\n"
    "ds_read_b64_tr_b16 v[240:241], v0 offset:16384\n"
    "v_exp_f32 v192, v192\n"
    "v_mfma_f32_16x16x32_bf16 a[24:27], v[216:219], a[96:99], a[24:27]\n"
    "ds_read_b64_tr_b16 v[242:243], v0 offset:17408\n"
    "buffer_load_dwordx4 v13, s[48:51], s75 offen offset:3072 nt lds\n"
    "v_exp_f32 v193, v193\n"
    "v_mfma_f32_16x16x32_bf16 a[28:31], v[216:219], a[100:103], a[28:31]\n"
    "ds_read_b64_tr_b16 v[244:245], v1 offset:16384\n"
    "v_exp_f32 v194, v194\n"
    "v_mfma_f32_16x16x32_bf16 a[32:35], v[216:219], a[104:107], a[32:35]\n"
    "ds_read_b64_tr_b16 v[246:247], v1 offset:17408\n"
    "v_add_f32 v22, v22, v192\n"
    "v_exp_f32 v195, v195\n"
    "v_mfma_f32_16x16x32_bf16 a[44:47], v[220:223], a[104:107], a[44:47]\n"
    "ds_read_b64_tr_b16 v[248:249], v2 offset:16384\n"
    "v_add_f32 v22, v22, v193\n"
    "v_mfma_f32_16x16x32_bf16 a[40:43], v[220:223], a[100:103], a[40:43]\n"
    "ds_read_b64_tr_b16 v[250:251], v2 offset:17408\n"
    "v_cvt_pk_bf16_f32 v192, v192, v193\n"
    "v_exp_f32 v196, v196\n"
    "v_mfma_f32_16x16x32_bf16 a[36:39], v[220:223], a[96:99], a[36:39]\n"
    "ds_read_b64_tr_b16 v[252:253], v3 offset:16384\n"
    "v_add_f32 v22, v22, v194\n"
    "v_mfma_f32_16x16x32_bf16 a[48:51], v[224:227], a[96:99], a[48:51]\n"
    "ds_read_b64_tr_b16 v[254:255], v3 offset:17408\n"
    "v_exp_f32 v197, v197\n"
    "v_mfma_f32_16x16x32_bf16 a[52:55], v[224:227], a[100:103], a[52:55]\n"
    "ds_read_b64_tr_b16 a[240:241], v4 offset:16384\n"
    "v_add_f32 v22, v22, v195\n"
    "v_cvt_pk_bf16_f32 v193, v194, v195\n"
    "v_exp_f32 v198, v198\n"
    "v_mfma_f32_16x16x32_bf16 a[56:59], v[224:227], a[104:107], a[56:59]\n"
    "s_waitcnt lgkmcnt(6)\n"
    "ds_read_b64_tr_b16 a[242:243], v4 offset:17408\n"
    "v_add_f32 v22, v22, v196\n"
    "v_mfma_f32_16x16x32_bf16 a[68:71], v[228:231], a[104:107], a[68:71]\n"
    "ds_read_b64_tr_b16 a[244:245], v5 offset:16384\n"
    "v_exp_f32 v199, v199\n"
    "v_mfma_f32_16x16x32_bf16 a[64:67], v[228:231], a[100:103], a[64:67]\n"
    "ds_read_b64_tr_b16 a[246:247], v5 offset:17408\n"
    "v_add_f32 v22, v22, v197\n"
    "v_cvt_pk_bf16_f32 v194, v196, v197\n"
    "v_mfma_f32_16x16x32_bf16 a[60:63], v[228:231], a[96:99], a[60:63]\n"
    "ds_read_b64_tr_b16 a[248:249], v6 offset:16384\n"
    "v_add_f32 v22, v22, v198\n"
    "v_add_f32 v22, v22, v199\n"
    "v_mfma_f32_16x16x32_bf16 a[72:75], v[232:235], a[96:99], a[72:75]\n"
    "ds_read_b64_tr_b16 a[250:251], v6 offset:17408\n"
    "ds_read_b64_tr_b16 a[252:253], v7 offset:16384\n"
    "ds_read_b64_tr_b16 a[254:255], v7 offset:17408\n"
    "v_cvt_pk_bf16_f32 v195, v198, v199\n"
    "v_mov_b32 v29, v22\n"
    "s_nop 1\n"
    "v_permlane32_swap_b32 v22, v29\n"
    "v_mfma_f32_16x16x32_bf16 a[76:79], v[232:235], a[100:103], a[76:79]\n"
    "v_add_f32 v22, v22, v29\n"
    "v_mfma_f32_16x16x32_bf16 a[80:83], v[232:235], a[104:107], a[80:83]\n"
    "v_mov_b32 v29, v22\n"
    "s_nop 1\n"
    "v_permlane16_swap_b32 v22, v29\n"
    "v_mfma_f32_16x16x32_bf16 a[92:95], v[236:239], a[104:107], a[92:95]\n"
    "v_add_f32 v22, v22, v29\n"
    "v_mfma_f32_16x16x32_bf16 a[88:91], v[236:239], a[100:103], a[88:91]\n"
    "v_rcp_f32 v28, v22\n"
    "v_mfma_f32_16x16x32_bf16 a[84:87], v[236:239], a[96:99], a[84:87]\n"
    "v_max3_f32 v37, v200, v201, v202\n"
    "v_max3_f32 v36, v203, v204, v205\n"
    "s_waitcnt vmcnt(13) lgkmcnt(0)\n"
    "s_barrier\n"
    "v_mfma_f32_16x16x32_bf16 v[88:91], v[240:243], v[184:187], v[88:91]\n"
    "v_max3_f32 v37, v37, v206, v207\n"
    "v_max_f32 v37, v37, v36\n"
    "v_mfma_f32_16x16x32_bf16 v[100:103], v[244:247], v[184:187], v[100:103]\n"
    "ds_read_b128 v[208:211], v8 offset:0\n"
    "v_cmp_nge_f32 vcc, s77, v37\n"
    "s_cbranch_vccnz L_stub112c_%=\n"
    "L_back112c_%=:\n"
    "v_mfma_f32_16x16x32_bf16 v[112:115], v[248:251], v[184:187], v[112:115]\n"
    "s_add_u32 m0, s64, 49152\n"
    "s_add_u32 s73, s73, 8192\n"
    "buffer_load_dwordx4 v14, s[52:55], s75 offen offset:0 nt lds\n"
    "ds_read_b128 v[212:215], v8 offset:1024\n"
    "v_exp_f32 v200, v200\n"
    "v_mfma_f32_16x16x32_bf16 v[124:127], v[252:255], v[184:187], v[124:127]\n"
    "ds_read_b128 v[216:219], v9 offset:0\n"
    "v_exp_f32 v201, v201\n"
    "v_mfma_f32_16x16x32_bf16 v[136:139], a[240:243], v[184:187], v[136:139]\n"
    "ds_read_b128 v[220:223], v9 offset:1024\n"
    "v_exp_f32 v202, v202\n"
    "v_mfma_f32_16x16x32_bf16 v[148:151], a[244:247], v[184:187], v[148:151]\n"
    "v_add_f32 v23, v23, v200\n"
    "v_exp_f32 v203, v203\n"
    "v_mfma_f32_16x16x32_bf16 v[160:163], a[248:251], v[184:187], v[160:163]\n"
    "ds_read_b128 v[224:227], v10 offset:0\n"
    "buffer_load_dwordx4 v14, s[52:55], s75 offen offset:1024 nt lds\n"
    "v_add_f32 v23, v23, v201\n"
    "v_mfma_f32_16x16x32_bf16 v[172:175], a[252:255], v[184:187], v[172:175]\n"
    "ds_read_b128 v[228:231], v10 offset:1024\n"
    "v_cvt_pk_bf16_f32 v200, v200, v201\n"
    "v_exp_f32 v204, v204\n"
    "v_mfma_f32_16x16x32_bf16 v[176:179], a[252:255], v[192:195], v[176:179]\n"
    "ds_read_b128 v[232:235], v11 offset:0\n"
    "v_add_f32 v23, v23, v202\n"
    "v_mfma_f32_16x16x32_bf16 v[164:167], a[248:251], v[192:195], v[164:167]\n"
    "ds_read_b128 v[236:239], v11 offset:1024\n"
    "buffer_load_dwordx4 v15, s[52:55], s75 offen offset:2048 nt lds\n"
    "v_exp_f32 v205, v205\n"
    "v_mfma_f32_16x16x32_bf16 v[152:155], a[244:247], v[192:195], v[152:155]\n"
    "v_add_f32 v23, v23, v203\n"
    "v_cvt_pk_bf16_f32 v201, v202, v203\n"
    "v_mfma_f32_16x16x32_bf16 v[140:143], a[240:243], v[192:195], v[140:143]\n"
    "v_exp_f32 v206, v206\n"
    "v_mfma_f32_16x16x32_bf16 v[128:131], v[252:255], v[192:195], v[128:131]\n"
    "buffer_load_dwordx4 v15, s[52:55], s75 offen offset:3072 nt lds\n"
    "v_add_f32 v23, v23, v204\n"
    "v_exp_f32 v207, v207\n"
    "v_mfma_f32_16x16x32_bf16 v[116:119], v[248:251], v[192:195], v[116:119]\n"
    "v_add_f32 v23, v23, v205\n"
    "v_cvt_pk_bf16_f32 v202, v204, v205\n"
    "v_add_f32 v23, v23, v206\n"
    "v_add_f32 v23, v23, v207\n"
    "v_cvt_pk_bf16_f32 v203, v206, v207\n"
    "v_mfma_f32_16x16x32_bf16 v[104:107], v[244:247], v[192:195], v[104:107]\n"
    "s_and_b32 s73, s73, 0x1ffff\n"
    "s_add_u32 m0, s65, 122240\n"
    "s_nop 0\n"
    "buffer_load_dwordx4 v18, s[56:59], s79 offen offset:640  lds\n"
    "v_mfma_f32_16x16x32_bf16 v[92:95], v[240:243], v[192:195], v[92:95]\n"
    "v_mov_b32 v33, v23\n"
    "s_nop 1\n"
    "v_permlane32_swap_b32 v23, v33\n"
    "v_add_f32 v23, v23, v33\n"
    "v_mfma_f32_16x16x32_bf16 v[96:99], v[240:243], v[200:203], v[96:99]\n"
    "v_mov_b32 v33, v23\n"
    "s_nop 1\n"
    "v_permlane16_swap_b32 v23, v33\n"
    "v_add_f32 v23, v23, v33\n"
    "v_mfma_f32_16x16x32_bf16 v[108:111], v[244:247], v[200:203], v[108:111]\n"
    "v_rcp_f32 v32, v23\n"
    "v_mul_f32 v88, v24, v88\n"
    "v_mul_f32 v89, v24, v89\n"
    "v_mfma_f32_16x16x32_bf16 v[120:123], v[248:251], v[200:203], v[120:123]\n"
    "v_mul_f32 v90, v24, v90\n"
    "v_mul_f32 v91, v24, v91\n"
    "v_mul_f32 v100, v24, v100\n"
    "v_mfma_f32_16x16x32_bf16 v[132:135], v[252:255], v[200:203], v[132:135]\n"
    "v_mul_f32 v101, v24, v101\n"
    "v_mul_f32 v102, v24, v102\n"
    "v_mul_f32 v103, v24, v103\n"
    "v_cvt_pk_bf16_f32 v184, v88, v89\n"
    "v_mfma_f32_16x16x32_bf16 v[144:147], a[240:243], v[200:203], v[144:147]\n"
    "s_add_u32 m0, s65, 126208\n"
    "s_nop 0\n"
    "buffer_load_dwordx4 v18, s[56:59], s79 offen offset:768  lds\n"
    "v_cvt_pk_bf16_f32 v185, v90, v91\n"
    "v_cvt_pk_bf16_f32 v186, v100, v101\n"
    "v_cvt_pk_bf16_f32 v187, v102, v103\n"
    "v_mfma_f32_16x16x32_bf16 v[156:159], a[244:247], v[200:203], v[156:159]\n"
    "ds_write_b128 v20, v[184:187] offset:0\n"
    "v_mul_f32 v112, v24, v112\n"
    "v_mul_f32 v113, v24, v113\n"
    "v_mul_f32 v114, v24, v114\n"
    "v_mfma_f32_16x16x32_bf16 v[168:171], a[248:251], v[200:203], v[168:171]\n"
    "v_mul_f32 v115, v24, v115\n"
    "v_mul_f32 v124, v24, v124\n"
    "v_mul_f32 v125, v24, v125\n"
    "v_mfma_f32_16x16x32_bf16 v[180:183], a[252:255], v[200:203], v[180:183]\n"
    "v_mul_f32 v126, v24, v126\n"
    "v_mul_f32 v127, v24, v127\n"
    "v_cvt_pk_bf16_f32 v188, v112, v113\n"
    "v_cvt_pk_bf16_f32 v189, v114, v115\n"
    "v_cvt_pk_bf16_f32 v190, v124, v125\n"
    "v_cvt_pk_bf16_f32 v191, v126, v127\n"
    "ds_write_b128 v20, v[188:191] offset:6144\n"
    "v_accvgpr_read_b32 v16, a208\n"
    "v_accvgpr_read_b32 v17, a209\n"
    "s_mul_i32 s69, s78, 6144\n"
    "v_add_u32 v17, s69, v17\n"
    "s_waitcnt lgkmcnt(0)\n"
    "s_barrier\n"
    "v_pk_mul_f32 v[136:137], v[136:137], v[24:25] op_sel_hi:[1,0]\n"
    "v_pk_mul_f32 v[138:139], v[138:139], v[24:25] op_sel_hi:[1,0]\n"
    "v_pk_mul_f32 v[148:149], v[148:149], v[24:25] op_sel_hi:[1,0]\n"
    "v_pk_mul_f32 v[150:151], v[150:151], v[24:25] op_sel_hi:[1,0]\n"
    "v_cvt_pk_bf16_f32 v196, v136, v137\n"
    "v_cvt_pk_bf16_f32 v197, v138, v139\n"
    "v_cvt_pk_bf16_f32 v198, v148, v149\n"
    "v_cvt_pk_bf16_f32 v199, v150, v151\n"
    "ds_write_b128 v20, v[196:199] offset:12288\n"
    "v_pk_mul_f32 v[160:161], v[160:161], v[24:25] op_sel_hi:[1,0]\n"
    "v_pk_mul_f32 v[162:163], v[162:163], v[24:25] op_sel_hi:[1,0]\n"
    "s_add_u32 m0, s65, 130176\n"
    "s_nop 0\n"
    "buffer_load_dwordx4 v18, s[56:59], s79 offen offset:896  lds\n"
    "v_pk_mul_f32 v[172:173], v[172:173], v[24:25] op_sel_hi:[1,0]\n"
    "v_pk_mul_f32 v[174:175], v[174:175], v[24:25] op_sel_hi:[1,0]\n"
    "v_cvt_pk_bf16_f32 v204, v160, v161\n"
    "v_cvt_pk_bf16_f32 v205, v162, v163\n"
    "v_cvt_pk_bf16_f32 v206, v172, v173\n"
    "v_cvt_pk_bf16_f32 v207, v174, v175\n"
    "ds_write_b128 v20, v[204:207] offset:18432\n"
    "v_pk_mul_f32 v[92:93], v[92:93], v[28:29] op_sel_hi:[1,0]\n"
    "v_pk_mul_f32 v[94:95], v[94:95], v[28:29] op_sel_hi:[1,0]\n"
    "v_pk_mul_f32 v[104:105], v[104:105], v[28:29] op_sel_hi:[1,0]\n"
    "v_pk_mul_f32 v[106:107], v[106:107], v[28:29] op_sel_hi:[1,0]\n"
    "s_add_u32 m0, s65, 134144\n"
    "s_nop 0\n"
    "buffer_load_dwordx4 v18, s[56:59], s79 offen offset:1024  lds\n"
    "v_cvt_pk_bf16_f32 v184, v92, v93\n"
    "v_cvt_pk_bf16_f32 v185, v94, v95\n"
    "v_cvt_pk_bf16_f32 v186, v104, v105\n"
    "v_cvt_pk_bf16_f32 v187, v106, v107\n"
    "ds_write_b128 v20, v[184:187] offset:1024\n"
    "v_pk_mul_f32 v[116:117], v[116:117], v[28:29] op_sel_hi:[1,0]\n"
    "v_pk_mul_f32 v[118:119], v[118:119], v[28:29] op_sel_hi:[1,0]\n"
    "v_pk_mul_f32 v[128:129], v[128:129], v[28:29] op_sel_hi:[1,0]\n"
    "v_pk_mul_f32 v[130:131], v[130:131], v[28:29] op_sel_hi:[1,0]\n"
    "v_cvt_pk_bf16_f32 v188, v116, v117\n"
    "v_cvt_pk_bf16_f32 v189, v118, v119\n"
    "s_add_u32 m0, s65, 138112\n"
    "s_nop 0\n"
    "buffer_load_dwordx4 v18, s[56:59], s79 offen offset:1152  lds\n"
    "v_cvt_pk_bf16_f32 v190, v128, v129\n"
    "v_cvt_pk_bf16_f32 v191, v130, v131\n"
    "ds_write_b128 v20, v[188:191] offset:7168\n"
    "v_pk_mul_f32 v[140:141], v[140:141], v[28:29] op_sel_hi:[1,0]\n"
    "v_pk_mul_f32 v[142:143], v[142:143], v[28:29] op_sel_hi:[1,0]\n"
    "v_pk_mul_f32 v[152:153], v[152:153], v[28:29] op_sel_hi:[1,0]\n"
    "v_pk_mul_f32 v[154:155], v[154:155], v[28:29] op_sel_hi:[1,0]\n"
    "v_cvt_pk_bf16_f32 v196, v140, v141\n"
    "v_cvt_pk_bf16_f32 v197, v142, v143\n"
    "v_cvt_pk_bf16_f32 v198, v152, v153\n"
    "v_cvt_pk_bf16_f32 v199, v154, v155\n"
    "ds_write_b128 v20, v[196:199] offset:13312\n"
    "s_add_u32 s79, s79, 40960\n"
    "s_cmp_ge_u32 s79, 327680\n"
    "s_cselect_b32 s79, s87, s79\n"
    "s_add_u32 m0, s64, 0\n"
    "s_add_u32 s75, s73, s74\n"
    "buffer_load_dwordx4 v12, s[48:51], s75 offen offset:0 nt lds\n"
    "v_pk_mul_f32 v[164:165], v[164:165], v[28:29] op_sel_hi:[1,0]\n"
    "v_pk_mul_f32 v[166:167], v[166:167], v[28:29] op_sel_hi:[1,0]\n"
    "v_pk_mul_f32 v[176:177], v[176:177], v[28:29] op_sel_hi:[1,0]\n"
    "v_pk_mul_f32 v[178:179], v[178:179], v[28:29] op_sel_hi:[1,0]\n"
    "v_cvt_pk_bf16_f32 v204, v164, v165\n"
    "v_cvt_pk_bf16_f32 v205, v166, v167\n"
    "v_cvt_pk_bf16_f32 v206, v176, v177\n"
    "v_cvt_pk_bf16_f32 v207, v178, v179\n"
    "ds_write_b128 v20, v[204:207] offset:19456\n"
    "v_pk_mul_f32 v[96:97], v[96:97], v[32:33] op_sel_hi:[1,0]\n"
    "v_pk_mul_f32 v[98:99], v[98:99], v[32:33] op_sel_hi:[1,0]\n"
    "buffer_load_dwordx4 v12, s[48:51], s75 offen offset:1024 nt lds\n"
    "v_pk_mul_f32 v[108:109], v[108:109], v[32:33] op_sel_hi:[1,0]\n"
    "v_pk_mul_f32 v[110:111], v[110:111], v[32:33] op_sel_hi:[1,0]\n"
    "v_cvt_pk_bf16_f32 v184, v96, v97\n"
    "v_cvt_pk_bf16_f32 v185, v98, v99\n"
    "v_cvt_pk_bf16_f32 v186, v108, v109\n"
    "v_cvt_pk_bf16_f32 v187, v110, v111\n"
    "ds_write_b128 v20, v[184:187] offset:2048\n"
    "v_pk_mul_f32 v[120:121], v[120:121], v[32:33] op_sel_hi:[1,0]\n"
    "v_pk_mul_f32 v[122:123], v[122:123], v[32:33] op_sel_hi:[1,0]\n"
    "v_pk_mul_f32 v[132:133], v[132:133], v[32:33] op_sel_hi:[1,0]\n"
    "v_pk_mul_f32 v[134:135], v[134:135], v[32:33] op_sel_hi:[1,0]\n"
    "buffer_load_dwordx4 v13, s[48:51], s75 offen offset:2048 nt lds\n"
    "v_cvt_pk_bf16_f32 v188, v120, v121\n"
    "v_cvt_pk_bf16_f32 v189, v122, v123\n"
    "v_cvt_pk_bf16_f32 v190, v132, v133\n"
    "v_cvt_pk_bf16_f32 v191, v134, v135\n"
    "ds_write_b128 v20, v[188:191] offset:8192\n"
    "v_pk_mul_f32 v[144:145], v[144:145], v[32:33] op_sel_hi:[1,0]\n"
    "v_pk_mul_f32 v[146:147], v[146:147], v[32:33] op_sel_hi:[1,0]\n"
    "v_pk_mul_f32 v[156:157], v[156:157], v[32:33] op_sel_hi:[1,0]\n"
    "v_pk_mul_f32 v[158:159], v[158:159], v[32:33] op_sel_hi:[1,0]\n"
    "v_cvt_pk_bf16_f32 v196, v144, v145\n"
    "v_cvt_pk_bf16_f32 v197, v146, v147\n"
    "buffer_load_dwordx4 v13, s[48:51], s75 offen offset:3072 nt lds\n"
    "v_cvt_pk_bf16_f32 v198, v156, v157\n"
    "v_cvt_pk_bf16_f32 v199, v158, v159\n"
    "ds_write_b128 v20, v[196:199] offset:14336\n"
    "v_pk_mul_f32 v[168:169], v[168:169], v[32:33] op_sel_hi:[1,0]\n"
    "v_pk_mul_f32 v[170:171], v[170:171], v[32:33] op_sel_hi:[1,0]\n"
    "v_pk_mul_f32 v[180:181], v[180:181], v[32:33] op_sel_hi:[1,0]\n"
    "v_pk_mul_f32 v[182:183], v[182:183], v[32:33] op_sel_hi:[1,0]\n"
    "v_cvt_pk_bf16_f32 v204, v168, v169\n"
    "v_cvt_pk_bf16_f32 v205, v170, v171\n"
    "v_cvt_pk_bf16_f32 v206, v180, v181\n"
    "v_cvt_pk_bf16_f32 v207, v182, v183\n"
    "ds_write_b128 v20, v[204:207] offset:20480\n"
    "v_mov_b32 v36, s76\n"
    "v_accvgpr_read_b32 v184, a0\n"
    "v_accvgpr_read_b32 v185, a1\n"
    "v_accvgpr_read_b32 v186, a2\n"
    "v_accvgpr_read_b32 v187, a3\n"
    "v_accvgpr_read_b32 v188, a12\n"
    "v_accvgpr_read_b32 v189, a13\n"
    "v_accvgpr_read_b32 v190, a14\n"
    "v_accvgpr_read_b32 v191, a15\n"
    "v_pk_mul_f32 v[184:185], v[184:185], v[36:37] op_sel_hi:[1,0]\n"
    "v_pk_mul_f32 v[186:187], v[186:187], v[36:37] op_sel_hi:[1,0]\n"
    "v_pk_mul_f32 v[188:189], v[188:189], v[36:37] op_sel_hi:[1,0]\n"
    "v_pk_mul_f32 v[190:191], v[190:191], v[36:37] op_sel_hi:[1,0]\n"
    "v_cvt_pk_bf16_f32 v40, v184, v185\n"
    "v_cvt_pk_bf16_f32 v42, v188, v189\n"
    "v_cvt_pk_bf16_f32 v41, v186, v187\n"
    "v_cvt_pk_bf16_f32 v43, v190, v191\n"
    "v_accvgpr_read_b32 v192, a4\n"
    "v_accvgpr_read_b32 v193, a5\n"
    "v_accvgpr_read_b32 v194, a6\n"
    "v_accvgpr_read_b32 v195, a7\n"
    "v_accvgpr_read_b32 v196, a16\n"
    "v_accvgpr_read_b32 v197, a17\n"
    "v_accvgpr_read_b32 v198, a18\n"
    "v_accvgpr_read_b32 v199, a19\n"
    "v_pk_mul_f32 v[192:193], v[192:193], v[36:37] op_sel_hi:[1,0]\n"
    "v_pk_mul_f32 v[194:195], v[194:195], v[36:37] op_sel_hi:[1,0]\n"
    "v_pk_mul_f32 v[196:197], v[196:197], v[36:37] op_sel_hi:[1,0]\n"
    "v_pk_mul_f32 v[198:199], v[198:199], v[36:37] op_sel_hi:[1,0]\n"
    "v_cvt_pk_bf16_f32 v44, v192, v193\n"
    "v_cvt_pk_bf16_f32 v46, v196, v197\n"
    "v_cvt_pk_bf16_f32 v45, v194, v195\n"
    "v_cvt_pk_bf16_f32 v47, v198, v199\n"
    "v_permlane16_swap_b32 v40, v42\n"
    "v_permlane16_swap_b32 v41, v43\n"
    "v_accvgpr_read_b32 v200, a8\n"
    "v_accvgpr_read_b32 v201, a9\n"
    "v_accvgpr_read_b32 v202, a10\n"
    "v_accvgpr_read_b32 v203, a11\n"
    "v_accvgpr_read_b32 v204, a20\n"
    "v_accvgpr_read_b32 v205, a21\n"
    "v_accvgpr_read_b32 v206, a22\n"
    "v_accvgpr_read_b32 v207, a23\n"
    "v_pk_mul_f32 v[200:201], v[200:201], v[36:37] op_sel_hi:[1,0]\n"
    "v_pk_mul_f32 v[202:203], v[202:203], v[36:37] op_sel_hi:[1,0]\n"
    "v_pk_mul_f32 v[204:205], v[204:205], v[36:37] op_sel_hi:[1,0]\n"
    "v_pk_mul_f32 v[206:207], v[206:207], v[36:37] op_sel_hi:[1,0]\n"
    "v_cvt_pk_bf16_f32 v48, v200, v201\n"
    "v_cvt_pk_bf16_f32 v50, v204, v205\n"
    "v_cvt_pk_bf16_f32 v49, v202, v203\n"
    "v_cvt_pk_bf16_f32 v51, v206, v207\n"
    "v_permlane16_swap_b32 v44, v46\n"
    "v_permlane16_swap_b32 v45, v47\n"
    "v_accvgpr_read_b32 v184, a24\n"
    "v_accvgpr_read_b32 v185, a25\n"
    "v_accvgpr_read_b32 v186, a26\n"
    "v_accvgpr_read_b32 v187, a27\n"
    "v_accvgpr_read_b32 v188, a36\n"
    "v_accvgpr_read_b32 v189, a37\n"
    "v_accvgpr_read_b32 v190, a38\n"
    "v_accvgpr_read_b32 v191, a39\n"
    "v_pk_mul_f32 v[184:185], v[184:185], v[36:37] op_sel_hi:[1,0]\n"
    "v_pk_mul_f32 v[186:187], v[186:187], v[36:37] op_sel_hi:[1,0]\n"
    "v_pk_mul_f32 v[188:189], v[188:189], v[36:37] op_sel_hi:[1,0]\n"
    "v_pk_mul_f32 v[190:191], v[190:191], v[36:37] op_sel_hi:[1,0]\n"
    "v_cvt_pk_bf16_f32 v52, v184, v185\n"
    "v_cvt_pk_bf16_f32 v54, v188, v189\n"
    "v_cvt_pk_bf16_f32 v53, v186, v187\n"
    "v_cvt_pk_bf16_f32 v55, v190, v191\n"
    "v_permlane16_swap_b32 v48, v50\n"
    "v_permlane16_swap_b32 v49, v51\n"
    "v_accvgpr_read_b32 v192, a28\n"
    "v_accvgpr_read_b32 v193, a29\n"
    "v_accvgpr_read_b32 v194, a30\n"
    "v_accvgpr_read_b32 v195, a31\n"
    "v_accvgpr_read_b32 v196, a40\n"
    "v_accvgpr_read_b32 v197, a41\n"
    "v_accvgpr_read_b32 v198, a42\n"
    "v_accvgpr_read_b32 v199, a43\n"
    "v_pk_mul_f32 v[192:193], v[192:193], v[36:37] op_sel_hi:[1,0]\n"
    "v_pk_mul_f32 v[194:195], v[194:195], v[36:37] op_sel_hi:[1,0]\n"
    "v_pk_mul_f32 v[196:197], v[196:197], v[36:37] op_sel_hi:[1,0]\n"
    "v_pk_mul_f32 v[198:199], v[198:199], v[36:37] op_sel_hi:[1,0]\n"
    "v_cvt_pk_bf16_f32 v56, v192, v193\n"
    "v_cvt_pk_bf16_f32 v58, v196, v197\n"
    "v_cvt_pk_bf16_f32 v57, v194, v195\n"
    "v_cvt_pk_bf16_f32 v59, v198, v199\n"
    "v_permlane16_swap_b32 v52, v54\n"
    "v_permlane16_swap_b32 v53, v55\n"
    "v_accvgpr_read_b32 v200, a32\n"
    "v_accvgpr_read_b32 v201, a33\n"
    "v_accvgpr_read_b32 v202, a34\n"
    "v_accvgpr_read_b32 v203, a35\n"
    "v_accvgpr_read_b32 v204, a44\n"
    "v_accvgpr_read_b32 v205, a45\n"
    "v_accvgpr_read_b32 v206, a46\n"
    "v_accvgpr_read_b32 v207, a47\n"
    "v_pk_mul_f32 v[200:201], v[200:201], v[36:37] op_sel_hi:[1,0]\n"
    "v_pk_mul_f32 v[202:203], v[202:203], v[36:37] op_sel_hi:[1,0]\n"
    "v_pk_mul_f32 v[204:205], v[204:205], v[36:37] op_sel_hi:[1,0]\n"
    "v_pk_mul_f32 v[206:207], v[206:207], v[36:37] op_sel_hi:[1,0]\n"
    "v_cvt_pk_bf16_f32 v60, v200, v201\n"
    "v_cvt_pk_bf16_f32 v62, v204, v205\n"
    "v_cvt_pk_bf16_f32 v61, v202, v203\n"
    "v_cvt_pk_bf16_f32 v63, v206, v207\n"
    "v_permlane16_swap_b32 v56, v58\n"
    "v_permlane16_swap_b32 v57, v59\n"
    "v_accvgpr_read_b32 v184, a48\n"
    "v_accvgpr_read_b32 v185, a49\n"
    "v_accvgpr_read_b32 v186, a50\n"
    "v_accvgpr_read_b32 v187, a51\n"
    "v_accvgpr_read_b32 v188, a60\n"
    "v_accvgpr_read_b32 v189, a61\n"
    "v_accvgpr_read_b32 v190, a62\n"
    "v_accvgpr_read_b32 v191, a63\n"
    "v_pk_mul_f32 v[184:185], v[184:185], v[36:37] op_sel_hi:[1,0]\n"
    "v_pk_mul_f32 v[186:187], v[186:187], v[36:37] op_sel_hi:[1,0]\n"
    "v_pk_mul_f32 v[188:189], v[188:189], v[36:37] op_sel_hi:[1,0]\n"
    "v_pk_mul_f32 v[190:191], v[190:191], v[36:37] op_sel_hi:[1,0]\n"
    "v_cvt_pk_bf16_f32 v64, v184, v185\n"
    "v_cvt_pk_bf16_f32 v66, v188, v189\n"
    "v_cvt_pk_bf16_f32 v65, v186, v187\n"
    "v_cvt_pk_bf16_f32 v67, v190, v191\n"
    "v_permlane16_swap_b32 v60, v62\n"
    "v_permlane16_swap_b32 v61, v63\n"
    "v_accvgpr_read_b32 v192, a52\n"
    "v_accvgpr_read_b32 v193, a53\n"
    "v_accvgpr_read_b32 v194, a54\n"
    "v_accvgpr_read_b32 v195, a55\n"
    "v_accvgpr_read_b32 v196, a64\n"
    "v_accvgpr_read_b32 v197, a65\n"
    "v_accvgpr_read_b32 v198, a66\n"
    "v_accvgpr_read_b32 v199, a67\n"
    "v_pk_mul_f32 v[192:193], v[192:193], v[36:37] op_sel_hi:[1,0]\n"
    "v_pk_mul_f32 v[194:195], v[194:195], v[36:37] op_sel_hi:[1,0]\n"
    "v_pk_mul_f32 v[196:197], v[196:197], v[36:37] op_sel_hi:[1,0]\n"
    "v_pk_mul_f32 v[198:199], v[198:199], v[36:37] op_sel_hi:[1,0]\n"
    "v_cvt_pk_bf16_f32 v68, v192, v193\n"
    "v_cvt_pk_bf16_f32 v70, v196, v197\n"
    "v_cvt_pk_bf16_f32 v69, v194, v195\n"
    "v_cvt_pk_bf16_f32 v71, v198, v199\n"
    "v_permlane16_swap_b32 v64, v66\n"
    "v_permlane16_swap_b32 v65, v67\n"
    "v_accvgpr_read_b32 v200, a56\n"
    "v_accvgpr_read_b32 v201, a57\n"
    "v_accvgpr_read_b32 v202, a58\n"
    "v_accvgpr_read_b32 v203, a59\n"
    "v_accvgpr_read_b32 v204, a68\n"
    "v_accvgpr_read_b32 v205, a69\n"
    "v_accvgpr_read_b32 v206, a70\n"
    "v_accvgpr_read_b32 v207, a71\n"
    "v_pk_mul_f32 v[200:201], v[200:201], v[36:37] op_sel_hi:[1,0]\n"
    "v_pk_mul_f32 v[202:203], v[202:203], v[36:37] op_sel_hi:[1,0]\n"
    "v_pk_mul_f32 v[204:205], v[204:205], v[36:37] op_sel_hi:[1,0]\n"
    "v_pk_mul_f32 v[206:207], v[206:207], v[36:37] op_sel_hi:[1,0]\n"
    "v_cvt_pk_bf16_f32 v72, v200, v201\n"
    "v_cvt_pk_bf16_f32 v74, v204, v205\n"
    "v_cvt_pk_bf16_f32 v73, v202, v203\n"
    "v_cvt_pk_bf16_f32 v75, v206, v207\n"
    "v_permlane16_swap_b32 v68, v70\n"
    "v_permlane16_swap_b32 v69, v71\n"
    "v_accvgpr_read_b32 v184, a72\n"
    "v_accvgpr_read_b32 v185, a73\n"
    "v_accvgpr_read_b32 v186, a74\n"
    "v_accvgpr_read_b32 v187, a75\n"
    "v_accvgpr_read_b32 v188, a84\n"
    "v_accvgpr_read_b32 v189, a85\n"
    "v_accvgpr_read_b32 v190, a86\n"
    "v_accvgpr_read_b32 v191, a87\n"
    "v_pk_mul_f32 v[184:185], v[184:185], v[36:37] op_sel_hi:[1,0]\n"
    "v_pk_mul_f32 v[186:187], v[186:187], v[36:37] op_sel_hi:[1,0]\n"
    "v_pk_mul_f32 v[188:189], v[188:189], v[36:37] op_sel_hi:[1,0]\n"
    "v_pk_mul_f32 v[190:191], v[190:191], v[36:37] op_sel_hi:[1,0]\n"
    "v_cvt_pk_bf16_f32 v76, v184, v185\n"
    "v_cvt_pk_bf16_f32 v78, v188, v189\n"
    "v_cvt_pk_bf16_f32 v77, v186, v187\n"
    "v_cvt_pk_bf16_f32 v79, v190, v191\n"
    "v_permlane16_swap_b32 v72, v74\n"
    "v_permlane16_swap_b32 v73, v75\n"
    "v_accvgpr_read_b32 v192, a76\n"
    "v_accvgpr_read_b32 v193, a77\n"
    "v_accvgpr_read_b32 v194, a78\n"
    "v_accvgpr_read_b32 v195, a79\n"
    "v_accvgpr_read_b32 v196, a88\n"
    "v_accvgpr_read_b32 v197, a89\n"
    "v_accvgpr_read_b32 v198, a90\n"
    "v_accvgpr_read_b32 v199, a91\n"
    "v_pk_mul_f32 v[192:193], v[192:193], v[36:37] op_sel_hi:[1,0]\n"
    "v_pk_mul_f32 v[194:195], v[194:195], v[36:37] op_sel_hi:[1,0]\n"
    "v_pk_mul_f32 v[196:197], v[196:197], v[36:37] op_sel_hi:[1,0]\n"
    "v_pk_mul_f32 v[198:199], v[198:199], v[36:37] op_sel_hi:[1,0]\n"
    "v_cvt_pk_bf16_f32 v80, v192, v193\n"
    "v_cvt_pk_bf16_f32 v82, v196, v197\n"
    "v_cvt_pk_bf16_f32 v81, v194, v195\n"
    "v_cvt_pk_bf16_f32 v83, v198, v199\n"
    "v_permlane16_swap_b32 v76, v78\n"
    "v_permlane16_swap_b32 v77, v79\n"
    "v_accvgpr_read_b32 v200, a80\n"
    "v_accvgpr_read_b32 v201, a81\n"
    "v_accvgpr_read_b32 v202, a82\n"
    "v_accvgpr_read_b32 v203, a83\n"
    "v_accvgpr_read_b32 v204, a92\n"
    "v_accvgpr_read_b32 v205, a93\n"
    "v_accvgpr_read_b32 v206, a94\n"
    "v_accvgpr_read_b32 v207, a95\n"
    "v_pk_mul_f32 v[200:201], v[200:201], v[36:37] op_sel_hi:[1,0]\n"
    "v_pk_mul_f32 v[202:203], v[202:203], v[36:37] op_sel_hi:[1,0]\n"
    "v_pk_mul_f32 v[204:205], v[204:205], v[36:37] op_sel_hi:[1,0]\n"
    "v_pk_mul_f32 v[206:207], v[206:207], v[36:37] op_sel_hi:[1,0]\n"
    "v_cvt_pk_bf16_f32 v84, v200, v201\n"
    "v_cvt_pk_bf16_f32 v86, v204, v205\n"
    "v_cvt_pk_bf16_f32 v85, v202, v203\n"
    "v_cvt_pk_bf16_f32 v87, v206, v207\n"
    "v_permlane16_swap_b32 v80, v82\n"
    "v_permlane16_swap_b32 v81, v83\n"
    "s_nop 1\n"
    "v_permlane16_swap_b32 v84, v86\n"
    "v_permlane16_swap_b32 v85, v87\n"
    "v_mov_b32 v21, 0\n"
    "v_mov_b32 v22, 0\n"
    "v_mov_b32 v23, 0\n"
    "s_waitcnt vmcnt(17) lgkmcnt(0)\n"
    "s_barrier\n"
    "v_mfma_f32_16x16x32_bf16 v[184:187], v[208:211], v[40:43], 0\n"
    "ds_read_b128 v[240:243], v17 offset:0\n"
    "v_mfma_f32_16x16x32_bf16 v[188:191], v[212:215], v[40:43], 0\n"
    "ds_read_b128 v[244:247], v17 offset:1024\n"
    "v_mfma_f32_16x16x32_bf16 v[184:187], v[216:219], v[52:55], v[184:187]\n"
    "ds_read_b128 v[248:251], v17 offset:2048\n"
    "v_mfma_f32_16x16x32_bf16 v[188:191], v[220:223], v[52:55], v[188:191]\n"
    "ds_read_b128 v[252:255], v17 offset:3072\n"
    "v_mfma_f32_16x16x32_bf16 v[184:187], v[224:227], v[64:67], v[184:187]\n"
    "ds_read_b128 a[240:243], v17 offset:4096\n"
    "v_mfma_f32_16x16x32_bf16 v[188:191], v[228:231], v[64:67], v[188:191]\n"
    "ds_read_b128 a[244:247], v17 offset:5120\n"
    "v_mfma_f32_16x16x32_bf16 v[184:187], v[232:235], v[76:79], v[184:187]\n"
    "ds_read_b64_tr_b16 a[248:249], v16 offset:40960\n"
    "v_mfma_f32_16x16x32_bf16 v[188:191], v[236:239], v[76:79], v[188:191]\n"
    "v_mfma_f32_16x16x32_bf16 v[192:195], v[208:211], v[44:47], 0\n"
    "s_nop 1\n"
    "ds_read_b64_tr_b16 a[250:251], v16 offset:43008\n"
    "v_mfma_f32_16x16x32_bf16 v[196:199], v[212:215], v[44:47], 0\n"
    "s_nop 1\n"
    "v_mfma_f32_16x16x32_bf16 v[192:195], v[216:219], v[56:59], v[192:195]\n"
    "v_mfma_f32_16x16x32_bf16 v[196:199], v[220:223], v[56:59], v[196:199]\n"
    "v_max3_f32 v37, v184, v185, v186\n"
    "v_max3_f32 v36, v187, v188, v189\n"
    "v_mfma_f32_16x16x32_bf16 v[192:195], v[224:227], v[68:71], v[192:195]\n"
    "v_max3_f32 v37, v37, v190, v191\n"
    "v_max_f32 v37, v37, v36\n"
    "v_mfma_f32_16x16x32_bf16 v[196:199], v[228:231], v[68:71], v[196:199]\n"
    "v_mov_b32 v36, v37\n"
    "s_nop 1\n"
    "v_permlane32_swap_b32 v37, v36\n"
    "v_mfma_f32_16x16x32_bf16 v[192:195], v[232:235], v[80:83], v[192:195]\n"
    "v_max_f32 v37, v37, v36\n"
    "v_mfma_f32_16x16x32_bf16 v[196:199], v[236:239], v[80:83], v[196:199]\n"
    "v_mov_b32 v36, v37\n"
    "s_nop 1\n"
    "v_permlane16_swap_b32 v37, v36\n"
    "v_mfma_f32_16x16x32_bf16 v[200:203], v[208:211], v[48:51], 0\n"
    "ds_read_b64_tr_b16 v[208:209], v16 offset:28672\n"
    "ds_read_b64_tr_b16 v[210:211], v16 offset:30720\n"
    "v_mfma_f32_16x16x32_bf16 v[204:207], v[212:215], v[48:51], 0\n"
    "ds_read_b64_tr_b16 v[212:213], v16 offset:32768\n"
    "v_max_f32 v37, v37, v36\n"
    "v_sub_f32 v184, v184, v37\n"
    "v_mfma_f32_16x16x32_bf16 v[200:203], v[216:219], v[60:63], v[200:203]\n"
    "ds_read_b64_tr_b16 v[214:215], v16 offset:34816\n"
    "v_sub_f32 v185, v185, v37\n"
    "v_sub_f32 v186, v186, v37\n"
    "v_sub_f32 v187, v187, v37\n"
    "v_mfma_f32_16x16x32_bf16 v[204:207], v[220:223], v[60:63], v[204:207]\n"
    "ds_read_b64_tr_b16 v[216:217], v16 offset:36864\n"
    "v_sub_f32 v188, v188, v37\n"
    "v_sub_f32 v189, v189, v37\n"
    "v_mfma_f32_16x16x32_bf16 v[200:203], v[224:227], v[72:75], v[200:203]\n"
    "v_sub_f32 v190, v190, v37\n"
    "v_sub_f32 v191, v191, v37\n"
    "v_mfma_f32_16x16x32_bf16 v[204:207], v[228:231], v[72:75], v[204:207]\n"
    "ds_read_b64_tr_b16 v[218:219], v16 offset:38912\n"
    "v_sub_f32 v24, 0, v37\n"
    "v_sub_f32 v25, 0, v37\n"
    "v_sub_f32 v26, 0, v37\n"
    "v_mfma_f32_16x16x32_bf16 v[200:203], v[232:235], v[84:87], v[200:203]\n"
    "ds_read_b64_tr_b16 a[252:253], v16 offset:45056\n"
    "v_sub_f32 v27, 0, v37\n"
    "v_exp_f32 v184, v184\n"
    "v_mfma_f32_16x16x32_bf16 v[204:207], v[236:239], v[84:87], v[204:207]\n"
    "s_waitcnt lgkmcnt(5)\n"
    "v_exp_f32 v185, v185\n"
    "v_mfma_f32_16x16x32_bf16 a[0:3], v[208:211], v[240:243], 0\n"
    "ds_read_b64_tr_b16 a[254:255], v16 offset:47104\n"
    "v_exp_f32 v186, v186\n"
    "v_mfma_f32_16x16x32_bf16 a[4:7], v[208:211], v[244:247], 0\n"
    "ds_read_b64_tr_b16 v[220:221], v3 offset:0\n"
    "v_add_f32 v21, v21, v184\n"
    "v_exp_f32 v187, v187\n"
    "v_mfma_f32_16x16x32_bf16 a[8:11], v[208:211], v[248:251], 0\n"
    "ds_read_b64_tr_b16 v[222:223], v3 offset:1024\n"
    "v_add_f32 v21, v21, v185\n"
    "v_mfma_f32_16x16x32_bf16 a[12:15], v[208:211], v[252:255], 0\n"
    "v_cvt_pk_bf16_f32 v184, v184, v185\n"
    "v_exp_f32 v188, v188\n"
    "v_mfma_f32_16x16x32_bf16 a[16:19], v[208:211], a[240:243], 0\n"
    "ds_read_b64_tr_b16 v[224:225], v4 offset:0\n"
    "v_add_f32 v21, v21, v186\n"
    "v_exp_f32 v189, v189\n"
    "v_mfma_f32_16x16x32_bf16 a[20:23], v[208:211], a[244:247], 0\n"
    "s_waitcnt lgkmcnt(4)\n"
    "ds_read_b64_tr_b16 v[226:227], v4 offset:1024\n"
    "v_add_f32 v21, v21, v187\n"
    "v_cvt_pk_bf16_f32 v185, v186, v187\n"
    "v_mfma_f32_16x16x32_bf16 a[44:47], v[212:215], a[244:247], 0\n"
    "v_exp_f32 v190, v190\n"
    "v_mfma_f32_16x16x32_bf16 a[40:43], v[212:215], a[240:243], 0\n"
    "ds_read_b64_tr_b16 v[228:229], v5 offset:0\n"
    "v_add_f32 v21, v21, v188\n"
    "v_exp_f32 v191, v191\n"
    "v_mfma_f32_16x16x32_bf16 a[36:39], v[212:215], v[252:255], 0\n"
    "ds_read_b64_tr_b16 v[230:231], v5 offset:1024\n"
    "v_add_f32 v21, v21, v189\n"
    "v_cvt_pk_bf16_f32 v186, v188, v189\n"
    "v_mfma_f32_16x16x32_bf16 a[32:35], v[212:215], v[248:251], 0\n"
    "v_add_f32 v21, v21, v190\n"
    "v_add_f32 v21, v21, v191\n"
    "v_mfma_f32_16x16x32_bf16 a[28:31], v[212:215], v[244:247], 0\n"
    "ds_read_b64_tr_b16 v[208:209], v0 offset:0\n"
    "v_cvt_pk_bf16_f32 v187, v190, v191\n"
    "v_max3_f32 v37, v192, v193, v194\n"
    "v_mfma_f32_16x16x32_bf16 a[24:27], v[212:215], v[240:243], 0\n"
    "ds_read_b64_tr_b16 v[210:211], v0 offset:1024\n"
    "v_max3_f32 v36, v195, v196, v197\n"
    "v_max3_f32 v37, v37, v198, v199\n"
    "v_max_f32 v37, v37, v36\n"
    "v_mfma_f32_16x16x32_bf16 a[48:51], v[216:219], v[240:243], 0\n"
    "v_mov_b32 v36, v37\n"
    "s_nop 1\n"
    "v_permlane32_swap_b32 v37, v36\n"
    "v_mfma_f32_16x16x32_bf16 a[52:55], v[216:219], v[244:247], 0\n"
    "ds_read_b64_tr_b16 v[232:233], v6 offset:0\n"
    "v_max_f32 v37, v37, v36\n"
    "v_mov_b32 v36, v37\n"
    "s_nop 1\n"
    "v_permlane16_swap_b32 v37, v36\n"
    "v_mfma_f32_16x16x32_bf16 a[56:59], v[216:219], v[248:251], 0\n"
    "ds_read_b64_tr_b16 v[234:235], v6 offset:1024\n"
    "v_max_f32 v37, v37, v36\n"
    "v_mfma_f32_16x16x32_bf16 a[60:63], v[216:219], v[252:255], 0\n"
    "v_sub_f32 v192, v192, v37\n"
    "v_sub_f32 v193, v193, v37\n"
    "v_mfma_f32_16x16x32_bf16 a[64:67], v[216:219], a[240:243], 0\n"
    "ds_read_b64_tr_b16 v[236:237], v7 offset:0\n"
    "v_sub_f32 v194, v194, v37\n"
    "v_sub_f32 v195, v195, v37\n"
    "v_mfma_f32_16x16x32_bf16 a[68:71], v[216:219], a[244:247], 0\n"
    "ds_read_b64_tr_b16 v[238:239], v7 offset:1024\n"
    "v_sub_f32 v196, v196, v37\n"
    "v_sub_f32 v197, v197, v37\n"
    "v_sub_f32 v198, v198, v37\n"
    "v_mfma_f32_16x16x32_bf16 a[92:95], a[248:251], a[244:247], 0\n"
    "v_sub_f32 v199, v199, v37\n"
    "v_sub_f32 v28, 0, v37\n"
    "v_mfma_f32_16x16x32_bf16 a[88:91], a[248:251], a[240:243], 0\n"
    "ds_read_b64_tr_b16 v[212:213], v1 offset:0\n"
    "v_sub_f32 v29, 0, v37\n"
    "v_sub_f32 v30, 0, v37\n"
    "v_mfma_f32_16x16x32_bf16 a[84:87], a[248:251], v[252:255], 0\n"
    "ds_read_b64_tr_b16 v[214:215], v1 offset:1024\n"
    "v_sub_f32 v31, 0, v37\n"
    "v_exp_f32 v192, v192\n"
    "v_mfma_f32_16x16x32_bf16 a[80:83], a[248:251], v[248:251], 0\n"
    "v_exp_f32 v193, v193\n"
    "v_mfma_f32_16x16x32_bf16 a[76:79], a[248:251], v[244:247], 0\n"
    "ds_read_b64_tr_b16 v[216:217], v2 offset:0\n"
    "ds_read_b64_tr_b16 v[218:219], v2 offset:1024\n"
    "v_exp_f32 v194, v194\n"
    "v_add_f32 v22, v22, v192\n"
    "v_mfma_f32_16x16x32_bf16 a[72:75], a[248:251], v[240:243], 0\n"
    "s_waitcnt lgkmcnt(5)\n"
    "v_exp_f32 v195, v195\n"
    "v_mfma_f32_16x16x32_bf16 a[96:99], a[252:255], v[240:243], 0\n"
    "v_add_f32 v22, v22, v193\n"
    "v_cvt_pk_bf16_f32 v192, v192, v193\n"
    "v_mfma_f32_16x16x32_bf16 a[100:103], a[252:255], v[244:247], 0\n"
    "v_exp_f32 v196, v196\n"
    "v_add_f32 v22, v22, v194\n"
    "v_mfma_f32_16x16x32_bf16 a[104:107], a[252:255], v[248:251], 0\n"
    "v_exp_f32 v197, v197\n"
    "v_mfma_f32_16x16x32_bf16 a[108:111], a[252:255], v[252:255], 0\n"
    "v_add_f32 v22, v22, v195\n"
    "v_cvt_pk_bf16_f32 v193, v194, v195\n"
    "v_mfma_f32_16x16x32_bf16 a[112:115], a[252:255], a[240:243], 0\n"
    "v_exp_f32 v198, v198\n"
    "v_add_f32 v22, v22, v196\n"
    "v_mfma_f32_16x16x32_bf16 a[116:119], a[252:255], a[244:247], 0\n"
    "v_exp_f32 v199, v199\n"
    "s_waitcnt vmcnt(13) lgkmcnt(0)\n"
    "s_barrier\n"
    "v_mfma_f32_16x16x32_bf16 v[88:91], v[208:211], v[184:187], 0\n"
    "v_add_f32 v22, v22, v197\n"
    "v_cvt_pk_bf16_f32 v194, v196, v197\n"
    "v_mfma_f32_16x16x32_bf16 v[100:103], v[212:215], v[184:187], 0\n"
    "ds_read_b128 v[240:243], v8 offset:16384\n"
    "v_add_f32 v22, v22, v198\n"
    "v_add_f32 v22, v22, v199\n"
    "v_cvt_pk_bf16_f32 v195, v198, v199\n"
    "v_mfma_f32_16x16x32_bf16 v[112:115], v[216:219], v[184:187], 0\n"
    "s_add_u32 m0, s64, 32768\n"
    "s_add_u32 s73, s73, 8192\n"
    "buffer_load_dwordx4 v14, s[52:55], s75 offen offset:0 nt lds\n"
    "ds_read_b128 v[244:247], v8 offset:17408\n"
    "v_max3_f32 v37, v200, v201, v202\n"
    "v_max3_f32 v36, v203, v204, v205\n"
    "v_mfma_f32_16x16x32_bf16 v[124:127], v[220:223], v[184:187], 0\n"
    "ds_read_b128 v[248:251], v9 offset:16384\n"
    "v_max3_f32 v37, v37, v206, v207\n"
    "v_max_f32 v37, v37, v36\n"
    "v_mfma_f32_16x16x32_bf16 v[136:139], v[224:227], v[184:187], 0\n"
    "ds_read_b128 v[252:255], v9 offset:17408\n"
    "v_mov_b32 v36, v37\n"
    "s_nop 1\n"
    "v_permlane32_swap_b32 v37, v36\n"
    "v_mfma_f32_16x16x32_bf16 v[148:151], v[228:231], v[184:187], 0\n"
    "buffer_load_dwordx4 v14, s[52:55], s75 offen offset:1024 nt lds\n"
    "v_max_f32 v37, v37, v36\n"
    "v_mov_b32 v36, v37\n"
    "s_nop 1\n"
    "v_permlane16_swap_b32 v37, v36\n"
    "v_mfma_f32_16x16x32_bf16 v[160:163], v[232:235], v[184:187], 0\n"
    "ds_read_b128 a[240:243], v10 offset:16384\n"
    "v_max_f32 v37, v37, v36\n"
    "v_mfma_f32_16x16x32_bf16 v[172:175], v[236:239], v[184:187], 0\n"
    "ds_read_b128 a[244:247], v10 offset:17408\n"
    "v_sub_f32 v200, v200, v37\n"
    "v_sub_f32 v201, v201, v37\n"
    "v_sub_f32 v202, v202, v37\n"
    "v_mfma_f32_16x16x32_bf16 v[176:179], v[236:239], v[192:195], 0\n"
    "ds_read_b128 a[248:251], v11 offset:16384\n"
    "buffer_load_dwordx4 v15, s[52:55], s75 offen offset:2048 nt lds\n"
    "v_sub_f32 v203, v203, v37\n"
    "v_sub_f32 v204, v204, v37\n"
    "v_mfma_f32_16x16x32_bf16 v[164:167], v[232:235], v[192:195], 0\n"
    "ds_read_b128 a[252:255], v11 offset:17408\n"
    "v_sub_f32 v205, v205, v37\n"
    "v_sub_f32 v206, v206, v37\n"
    "v_sub_f32 v207, v207, v37\n"
    "v_mfma_f32_16x16x32_bf16 v[152:155], v[228:231], v[192:195], 0\n"
    "buffer_load_dwordx4 v15, s[52:55], s75 offen offset:3072 nt lds\n"
    "v_sub_f32 v32, 0, v37\n"
    "v_sub_f32 v33, 0, v37\n"
    "v_mfma_f32_16x16x32_bf16 v[140:143], v[224:227], v[192:195], 0\n"
    "v_sub_f32 v34, 0, v37\n"
    "v_sub_f32 v35, 0, v37\n"
    "v_mfma_f32_16x16x32_bf16 v[128:131], v[220:223], v[192:195], 0\n"
    "v_exp_f32 v200, v200\n"
    "v_exp_f32 v201, v201\n"
    "v_mfma_f32_16x16x32_bf16 v[116:119], v[216:219], v[192:195], 0\n"
    "s_and_b32 s73, s73, 0x1ffff\n"
    "s_add_u32 m0, s65, 143360\n"
    "s_nop 0\n"
    "buffer_load_dwordx4 v18, s[56:59], s79 offen offset:0  lds\n"
    "v_exp_f32 v202, v202\n"
    "v_mfma_f32_16x16x32_bf16 v[104:107], v[212:215], v[192:195], 0\n"
    "v_add_f32 v23, v23, v200\n"
    "v_mfma_f32_16x16x32_bf16 v[92:95], v[208:211], v[192:195], 0\n"
    "s_waitcnt lgkmcnt(1)\n"
    "v_exp_f32 v203, v203\n"
    "v_add_f32 v23, v23, v201\n"
    "v_mfma_f32_16x16x32_bf16 v[184:187], v[240:243], v[40:43], v[24:27]\n"
    "v_cvt_pk_bf16_f32 v200, v200, v201\n"
    "v_exp_f32 v204, v204\n"
    "v_mfma_f32_16x16x32_bf16 v[188:191], v[244:247], v[40:43], v[24:27]\n"
    "s_add_u32 m0, s65, 147328\n"
    "s_nop 0\n"
    "buffer_load_dwordx4 v18, s[56:59], s79 offen offset:128  lds\n"
    "v_add_f32 v23, v23, v202\n"
    "v_mfma_f32_16x16x32_bf16 v[184:187], v[248:251], v[52:55], v[184:187]\n"
    "v_exp_f32 v205, v205\n"
    "v_add_f32 v23, v23, v203\n"
    "v_mfma_f32_16x16x32_bf16 v[188:191], v[252:255], v[52:55], v[188:191]\n"
    "v_cvt_pk_bf16_f32 v201, v202, v203\n"
    "v_exp_f32 v206, v206\n"
    "v_mfma_f32_16x16x32_bf16 v[184:187], a[240:243], v[64:67], v[184:187]\n"
    "v_add_f32 v23, v23, v204\n"
    "v_mfma_f32_16x16x32_bf16 v[188:191], a[244:247], v[64:67], v[188:191]\n"
    "s_add_u32 m0, s65, 151296\n"
    "s_nop 0\n"
    "buffer_load_dwordx4 v18, s[56:59], s79 offen offset:256  lds\n"
    "v_exp_f32 v207, v207\n"
    "v_add_f32 v23, v23, v205\n"
    "v_cvt_pk_bf16_f32 v202, v204, v205\n"
    "v_add_f32 v23, v23, v206\n"
    "v_add_f32 v23, v23, v207\n"
    "v_cvt_pk_bf16_f32 v203, v206, v207\n"
    "v_mfma_f32_16x16x32_bf16 v[184:187], a[248:251], v[76:79], v[184:187]\n"
    "s_waitcnt lgkmcnt(0)\n"
    "v_mfma_f32_16x16x32_bf16 v[188:191], a[252:255], v[76:79], v[188:191]\n"
    "s_nop 1\n"
    "v_mfma_f32_16x16x32_bf16 v[96:99], v[208:211], v[200:203], 0\n"
    "s_nop 1\n"
    "v_mfma_f32_16x16x32_bf16 v[108:111], v[212:215], v[200:203], 0\n"
    "s_add_u32 m0, s65, 155264\n"
    "s_nop 0\n"
    "buffer_load_dwordx4 v18, s[56:59], s79 offen offset:384  lds\n"
    "s_nop 0\n"
    "v_mfma_f32_16x16x32_bf16 v[120:123], v[216:219], v[200:203], 0\n"
    "v_max3_f32 v37, v184, v185, v186\n"
    "v_max3_f32 v36, v187, v188, v189\n"
    "v_max3_f32 v37, v37, v190, v191\n"
    "v_mfma_f32_16x16x32_bf16 v[132:135], v[220:223], v[200:203], 0\n"
    "v_max_f32 v37, v37, v36\n"
    "v_cmp_nge_f32 vcc, s77, v37\n"
    "v_mfma_f32_16x16x32_bf16 v[144:147], v[224:227], v[200:203], 0\n"
    "s_cbranch_vccnz L_stub210a_%=\n"
    "L_back210a_%=:\n"
    "v_mfma_f32_16x16x32_bf16 v[156:159], v[228:231], v[200:203], 0\n"
    "s_add_u32 m0, s65, 159232\n"
    "s_nop 0\n"
    "buffer_load_dwordx4 v18, s[56:59], s79 offen offset:512  lds\n"
    "v_exp_f32 v184, v184\n"
    "v_mfma_f32_16x16x32_bf16 v[168:171], v[232:235], v[200:203], 0\n"
    "v_exp_f32 v185, v185\n"
    "v_mfma_f32_16x16x32_bf16 v[180:183], v[236:239], v[200:203], 0\n"
    "v_exp_f32 v186, v186\n"
    "s_waitcnt vmcnt(13)\n"
    "s_barrier\n"
    "v_mfma_f32_16x16x32_bf16 v[192:195], v[240:243], v[44:47], v[28:31]\n"
    "ds_read_b128 v[208:211], v17 offset:0\n"
    "v_add_f32 v21, v21, v184\n"
    "v_mfma_f32_16x16x32_bf16 v[196:199], v[244:247], v[44:47], v[28:31]\n"
    "s_add_u32 m0, s64, 16384\n"
    "s_add_u32 s75, s73, s74\n"
    "buffer_load_dwordx4 v12, s[48:51], s75 offen offset:0 nt lds\n"
    "v_mfma_f32_16x16x32_bf16 v[192:195], v[248:251], v[56:59], v[192:195]\n"
    "v_exp_f32 v187, v187\n"
    "v_mfma_f32_16x16x32_bf16 v[196:199], v[252:255], v[56:59], v[196:199]\n"
    "ds_read_b128 v[212:215], v17 offset:1024\n"
    "v_add_f32 v21, v21, v185\n"
    "v_mfma_f32_16x16x32_bf16 v[192:195], a[240:243], v[68:71], v[192:195]\n"
    "ds_read_b128 v[216:219], v17 offset:2048\n"
    "v_cvt_pk_bf16_f32 v184, v184, v185\n"
    "v_mfma_f32_16x16x32_bf16 v[196:199], a[244:247], v[68:71], v[196:199]\n"
    "ds_read_b128 v[220:223], v17 offset:3072\n"
    "v_exp_f32 v188, v188\n"
    "v_mfma_f32_16x16x32_bf16 v[192:195], a[248:251], v[80:83], v[192:195]\n"
    "ds_read_b128 v[224:227], v17 offset:4096\n"
    "v_add_f32 v21, v21, v186\n"
    "v_mfma_f32_16x16x32_bf16 v[196:199], a[252:255], v[80:83], v[196:199]\n"
    "ds_read_b128 v[228:231], v17 offset:5120\n"
    "v_mfma_f32_16x16x32_bf16 v[200:203], v[240:243], v[48:51], v[32:35]\n"
    "s_add_u32 s78, s78, 1\n"
    "s_cmp_eq_u32 s78, 8\n"
    "s_cselect_b32 s39, s68, 6144\n"
    "v_add_u32 v17, s39, v17\n"
    "v_exp_f32 v189, v189\n"
    "v_mfma_f32_16x16x32_bf16 v[204:207], v[244:247], v[48:51], v[32:35]\n"
    "ds_read_b64_tr_b16 v[232:233], v16 offset:20480\n"
    "ds_read_b64_tr_b16 v[234:235], v16 offset:22528\n"
    "ds_read_b64_tr_b16 v[240:241], v16 offset:8192\n"
    "v_add_f32 v21, v21, v187\n"
    "ds_read_b64_tr_b16 v[242:243], v16 offset:10240\n"
    "v_mfma_f32_16x16x32_bf16 v[200:203], v[248:251], v[60:63], v[200:203]\n"
    "buffer_load_dwordx4 v12, s[48:51], s75 offen offset:1024 nt lds\n"
    "v_cvt_pk_bf16_f32 v185, v186, v187\n"
    "v_mfma_f32_16x16x32_bf16 v[204:207], v[252:255], v[60:63], v[204:207]\n"
    "v_exp_f32 v190, v190\n"
    "v_mfma_f32_16x16x32_bf16 v[200:203], a[240:243], v[72:75], v[200:203]\n"
    "v_add_f32 v21, v21, v188\n"
    "ds_read_b64_tr_b16 v[244:245], v16 offset:12288\n"
    "v_mfma_f32_16x16x32_bf16 v[204:207], a[244:247], v[72:75], v[204:207]\n"
    "v_exp_f32 v191, v191\n"
    "v_mfma_f32_16x16x32_bf16 v[200:203], a[248:251], v[84:87], v[200:203]\n"
    "v_add_f32 v21, v21, v189\n"
    "v_cvt_pk_bf16_f32 v186, v188, v189\n"
    "v_mfma_f32_16x16x32_bf16 v[204:207], a[252:255], v[84:87], v[204:207]\n"
    "s_waitcnt lgkmcnt(1)\n"
    "ds_read_b64_tr_b16 v[246:247], v16 offset:14336\n"
    "buffer_load_dwordx4 v13, s[48:51], s75 offen offset:2048 nt lds\n"
    "v_mfma_f32_16x16x32_bf16 a[120:123], v[240:243], v[208:211], 0\n"
    "v_add_f32 v21, v21, v190\n"
    "ds_read_b64_tr_b16 v[248:249], v16 offset:16384\n"
    "v_mfma_f32_16x16x32_bf16 a[124:127], v[240:243], v[212:215], 0\n"
    "v_add_f32 v21, v21, v191\n"
    "v_cvt_pk_bf16_f32 v187, v190, v191\n"
    "v_mfma_f32_16x16x32_bf16 a[128:131], v[240:243], v[216:219], 0\n"
    "v_max3_f32 v37, v192, v193, v194\n"
    "ds_read_b64_tr_b16 v[250:251], v16 offset:18432\n"
    "v_mfma_f32_16x16x32_bf16 a[132:135], v[240:243], v[220:223], 0\n"
    "v_max3_f32 v36, v195, v196, v197\n"
    "ds_read_b64_tr_b16 v[236:237], v16 offset:24576\n"
    "v_mfma_f32_16x16x32_bf16 a[136:139], v[240:243], v[224:227], 0\n"
    "v_max3_f32 v37, v37, v198, v199\n"
    "v_max_f32 v37, v37, v36\n"
    "v_mfma_f32_16x16x32_bf16 a[140:143], v[240:243], v[228:231], 0\n"
    "s_waitcnt lgkmcnt(3)\n"
    "ds_read_b64_tr_b16 v[238:239], v16 offset:26624\n"
    "v_cmp_nge_f32 vcc, s77, v37\n"
    "v_mfma_f32_16x16x32_bf16 a[164:167], v[244:247], v[228:231], 0\n"
    "s_cbranch_vccnz L_stub211a_%=\n"
    "L_back211a_%=:\n"
    "ds_read_b64_tr_b16 v[252:253], v3 offset:16384\n"
    "buffer_load_dwordx4 v13, s[48:51], s75 offen offset:3072 nt lds\n"
    "v_mfma_f32_16x16x32_bf16 a[160:163], v[244:247], v[224:227], 0\n"
    "ds_read_b64_tr_b16 v[254:255], v3 offset:17408\n"
    "v_exp_f32 v192, v192\n"
    "v_mfma_f32_16x16x32_bf16 a[156:159], v[244:247], v[220:223], 0\n"
    "ds_read_b64_tr_b16 a[240:241], v4 offset:16384\n"
    "v_exp_f32 v193, v193\n"
    "v_mfma_f32_16x16x32_bf16 a[152:155], v[244:247], v[216:219], 0\n"
    "ds_read_b64_tr_b16 a[242:243], v4 offset:17408\n"
    "v_mfma_f32_16x16x32_bf16 a[148:151], v[244:247], v[212:215], 0\n"
    "v_exp_f32 v194, v194\n"
    "v_mfma_f32_16x16x32_bf16 a[144:147], v[244:247], v[208:211], 0\n"
    "s_waitcnt lgkmcnt(5)\n"
    "ds_read_b64_tr_b16 a[244:245], v5 offset:16384\n"
    "v_add_f32 v22, v22, v192\n"
    "v_mfma_f32_16x16x32_bf16 a[168:171], v[248:251], v[208:211], 0\n"
    "ds_read_b64_tr_b16 a[246:247], v5 offset:17408\n"
    "v_exp_f32 v195, v195\n"
    "v_mfma_f32_16x16x32_bf16 a[172:175], v[248:251], v[212:215], 0\n"
    "ds_read_b64_tr_b16 v[240:241], v0 offset:16384\n"
    "v_add_f32 v22, v22, v193\n"
    "v_mfma_f32_16x16x32_bf16 a[176:179], v[248:251], v[216:219], 0\n"
    "ds_read_b64_tr_b16 v[242:243], v0 offset:17408\n"
    "v_cvt_pk_bf16_f32 v192, v192, v193\n"
    "v_mfma_f32_16x16x32_bf16 a[180:183], v[248:251], v[220:223], 0\n"
    "ds_read_b64_tr_b16 a[248:249], v6 offset:16384\n"
    "v_exp_f32 v196, v196\n"
    "v_mfma_f32_16x16x32_bf16 a[184:187], v[248:251], v[224:227], 0\n"
    "ds_read_b64_tr_b16 a[250:251], v6 offset:17408\n"
    "v_add_f32 v22, v22, v194\n"
    "v_mfma_f32_16x16x32_bf16 a[188:191], v[248:251], v[228:231], 0\n"
    "v_exp_f32 v197, v197\n"
    "v_mfma_f32_16x16x32_bf16 a[212:215], v[232:235], v[228:231], 0\n"
    "ds_read_b64_tr_b16 a[252:253], v7 offset:16384\n"
    "v_add_f32 v22, v22, v195\n"
    "v_mfma_f32_16x16x32_bf16 a[208:211], v[232:235], v[224:227], 0\n"
    "ds_read_b64_tr_b16 a[254:255], v7 offset:17408\n"
    "v_cvt_pk_bf16_f32 v193, v194, v195\n"
    "v_mfma_f32_16x16x32_bf16 a[204:207], v[232:235], v[220:223], 0\n"
    "ds_read_b64_tr_b16 v[244:245], v1 offset:16384\n"
    "v_exp_f32 v198, v198\n"
    "v_mfma_f32_16x16x32_bf16 a[200:203], v[232:235], v[216:219], 0\n"
    "ds_read_b64_tr_b16 v[246:247], v1 offset:17408\n"
    "v_add_f32 v22, v22, v196\n"
    "v_mfma_f32_16x16x32_bf16 a[196:199], v[232:235], v[212:215], 0\n"
    "ds_read_b64_tr_b16 v[248:249], v2 offset:16384\n"
    "ds_read_b64_tr_b16 v[250:251], v2 offset:17408\n"
    "v_mfma_f32_16x16x32_bf16 a[192:195], v[232:235], v[208:211], 0\n"
    "s_waitcnt lgkmcnt(6)\n"
    "v_exp_f32 v199, v199\n"
    "v_mfma_f32_16x16x32_bf16 a[216:219], v[236:239], v[208:211], 0\n"
    "v_add_f32 v22, v22, v197\n"
    "v_cvt_pk_bf16_f32 v194, v196, v197\n"
    "v_mfma_f32_16x16x32_bf16 a[220:223], v[236:239], v[212:215], 0\n"
    "v_add_f32 v22, v22, v198\n"
    "v_add_f32 v22, v22, v199\n"
    "v_mfma_f32_16x16x32_bf16 a[224:227], v[236:239], v[216:219], 0\n"
    "v_cvt_pk_bf16_f32 v195, v198, v199\n"
    "v_max3_f32 v37, v200, v201, v202\n"
    "v_mfma_f32_16x16x32_bf16 a[228:231], v[236:239], v[220:223], 0\n"
    "v_max3_f32 v36, v203, v204, v205\n"
    "v_max3_f32 v37, v37, v206, v207\n"
    "v_mfma_f32_16x16x32_bf16 a[232:235], v[236:239], v[224:227], 0\n"
    "v_max_f32 v37, v37, v36\n"
    "v_cmp_nge_f32 vcc, s77, v37\n"
    "v_mfma_f32_16x16x32_bf16 a[236:239], v[236:239], v[228:231], 0\n"
    "s_cbranch_vccnz L_stub212a_%=\n"
    "L_back212a_%=:\n"
    "s_waitcnt vmcnt(13) lgkmcnt(0)\n"
    "s_barrier\n"
    "v_mfma_f32_16x16x32_bf16 v[88:91], v[240:243], v[184:187], v[88:91]\n"
    "ds_read_b128 v[208:211], v8 offset:0\n"
    "v_mfma_f32_16x16x32_bf16 v[100:103], v[244:247], v[184:187], v[100:103]\n"
    "ds_read_b128 v[212:215], v8 offset:1024\n"
    "v_mfma_f32_16x16x32_bf16 v[112:115], v[248:251], v[184:187], v[112:115]\n"
    "s_add_u32 m0, s64, 49152\n"
    "s_add_u32 s73, s73, 8192\n"
    "buffer_load_dwordx4 v14, s[52:55], s75 offen offset:0 nt lds\n"
    "v_exp_f32 v200, v200\n"
    "v_mfma_f32_16x16x32_bf16 v[124:127], v[252:255], v[184:187], v[124:127]\n"
    "ds_read_b128 v[216:219], v9 offset:0\n"
    "buffer_load_dwordx4 v14, s[52:55], s75 offen offset:1024 nt lds\n"
    "v_mfma_f32_16x16x32_bf16 v[136:139], a[240:243], v[184:187], v[136:139]\n"
    "ds_read_b128 v[220:223], v9 offset:1024\n"
    "v_exp_f32 v201, v201\n"
    "v_mfma_f32_16x16x32_bf16 v[148:151], a[244:247], v[184:187], v[148:151]\n"
    "v_exp_f32 v202, v202\n"
    "v_mfma_f32_16x16x32_bf16 v[160:163], a[248:251], v[184:187], v[160:163]\n"
    "ds_read_b128 v[224:227], v10 offset:0\n"
    "buffer_load_dwordx4 v15, s[52:55], s75 offen offset:2048 nt lds\n"
    "v_mfma_f32_16x16x32_bf16 v[172:175], a[252:255], v[184:187], v[172:175]\n"
    "ds_read_b128 v[228:231], v10 offset:1024\n"
    "v_add_f32 v23, v23, v200\n"
    "v_mfma_f32_16x16x32_bf16 v[176:179], a[252:255], v[192:195], v[176:179]\n"
    "ds_read_b128 v[232:235], v11 offset:0\n"
    "v_exp_f32 v203, v203\n"
    "v_mfma_f32_16x16x32_bf16 v[164:167], a[248:251], v[192:195], v[164:167]\n"
    "ds_read_b128 v[236:239], v11 offset:1024\n"
    "v_add_f32 v23, v23, v201\n"
    "v_mfma_f32_16x16x32_bf16 v[152:155], a[244:247], v[192:195], v[152:155]\n"
    "buffer_load_dwordx4 v15, s[52:55], s75 offen offset:3072 nt lds\n"
    "v_cvt_pk_bf16_f32 v200, v200, v201\n"
    "v_mfma_f32_16x16x32_bf16 v[140:143], a[240:243], v[192:195], v[140:143]\n"
    "v_exp_f32 v204, v204\n"
    "v_mfma_f32_16x16x32_bf16 v[128:131], v[252:255], v[192:195], v[128:131]\n"
    "s_and_b32 s73, s73, 0x1ffff\n"
    "s_add_u32 m0, s65, 122240\n"
    "s_nop 0\n"
    "buffer_load_dwordx4 v18, s[56:59], s79 offen offset:640  lds\n"
    "v_add_f32 v23, v23, v202\n"
    "v_mfma_f32_16x16x32_bf16 v[116:119], v[248:251], v[192:195], v[116:119]\n"
    "v_exp_f32 v205, v205\n"
    "v_mfma_f32_16x16x32_bf16 v[104:107], v[244:247], v[192:195], v[104:107]\n"
    "v_add_f32 v23, v23, v203\n"
    "v_cvt_pk_bf16_f32 v201, v202, v203\n"
    "v_mfma_f32_16x16x32_bf16 v[92:95], v[240:243], v[192:195], v[92:95]\n"
    "s_waitcnt lgkmcnt(1)\n"
    "s_add_u32 m0, s65, 126208\n"
    "s_nop 0\n"
    "buffer_load_dwordx4 v18, s[56:59], s79 offen offset:768  lds\n"
    "v_mfma_f32_16x16x32_bf16 v[184:187], v[208:211], v[40:43], v[24:27]\n"
    "v_exp_f32 v206, v206\n"
    "v_mfma_f32_16x16x32_bf16 v[188:191], v[212:215], v[40:43], v[24:27]\n"
    "v_add_f32 v23, v23, v204\n"
    "v_mfma_f32_16x16x32_bf16 v[184:187], v[216:219], v[52:55], v[184:187]\n"
    "v_exp_f32 v207, v207\n"
    "v_mfma_f32_16x16x32_bf16 v[188:191], v[220:223], v[52:55], v[188:191]\n"
    "s_add_u32 m0, s65, 130176\n"
    "s_nop 0\n"
    "buffer_load_dwordx4 v18, s[56:59], s79 offen offset:896  lds\n"
    "v_mfma_f32_16x16x32_bf16 v[184:187], v[224:227], v[64:67], v[184:187]\n"
    "v_add_f32 v23, v23, v205\n"
    "v_cvt_pk_bf16_f32 v202, v204, v205\n"
    "v_mfma_f32_16x16x32_bf16 v[188:191], v[228:231], v[64:67], v[188:191]\n"
    "v_add_f32 v23, v23, v206\n"
    "v_add_f32 v23, v23, v207\n"
    "v_cvt_pk_bf16_f32 v203, v206, v207\n"
    "v_mfma_f32_16x16x32_bf16 v[184:187], v[232:235], v[76:79], v[184:187]\n"
    "s_waitcnt lgkmcnt(0)\n"
    "s_add_u32 m0, s65, 134144\n"
    "s_nop 0\n"
    "buffer_load_dwordx4 v18, s[56:59], s79 offen offset:1024  lds\n"
    "v_mfma_f32_16x16x32_bf16 v[188:191], v[236:239], v[76:79], v[188:191]\n"
    "s_nop 1\n"
    "v_mfma_f32_16x16x32_bf16 v[96:99], v[240:243], v[200:203], v[96:99]\n"
    "s_nop 1\n"
    "v_mfma_f32_16x16x32_bf16 v[108:111], v[244:247], v[200:203], v[108:111]\n"
    "s_nop 0\n"
    "v_mfma_f32_16x16x32_bf16 v[120:123], v[248:251], v[200:203], v[120:123]\n"
    "v_max3_f32 v37, v184, v185, v186\n"
    "v_max3_f32 v36, v187, v188, v189\n"
    "v_max3_f32 v37, v37, v190, v191\n"
    "v_mfma_f32_16x16x32_bf16 v[132:135], v[252:255], v[200:203], v[132:135]\n"
    "s_add_u32 m0, s65, 138112\n"
    "s_nop 0\n"
    "buffer_load_dwordx4 v18, s[56:59], s79 offen offset:1152  lds\n"
    "s_add_u32 s79, s79, 40960\n"
    "s_cmp_ge_u32 s79, 327680\n"
    "s_cselect_b32 s79, s87, s79\n"
    "v_max_f32 v37, v37, v36\n"
    "v_cmp_nge_f32 vcc, s77, v37\n"
    "v_mfma_f32_16x16x32_bf16 v[144:147], a[240:243], v[200:203], v[144:147]\n"
    "s_cbranch_vccnz L_stub200a_%=\n"
    "L_back200a_%=:\n"
    "v_mfma_f32_16x16x32_bf16 v[156:159], a[244:247], v[200:203], v[156:159]\n"
    "v_exp_f32 v184, v184\n"
    "v_mfma_f32_16x16x32_bf16 v[168:171], a[248:251], v[200:203], v[168:171]\n"
    "v_exp_f32 v185, v185\n"
    "v_mfma_f32_16x16x32_bf16 v[180:183], a[252:255], v[200:203], v[180:183]\n"
    "v_exp_f32 v186, v186\n"
    "s_mov_b32 s66, 0\n"
    "L_r2_%=:\n"
    "s_cmp_ge_u32 s66, 1\n"
    "s_cbranch_scc1 L_wt_%=\n"
    "v_mbcnt_lo_u32_b32 v37, -1, 0\n"
    "v_mbcnt_hi_u32_b32 v37, -1, v37\n"
    "v_min_u32 v37, 39, v37\n"
    "v_lshlrev_b32 v37, 7, v37\n"
    "buffer_load_dword v19, v37, s[56:59], s81 offen\n"
    "s_add_u32 s81, s81, 5120\n"
    "L_wt_%=:\n"
    "s_cmp_lg_u32 s66, 5\n"
    "s_cbranch_scc1 L_ct_%=\n"
    "v_mbcnt_lo_u32_b32 v37, -1, 0\n"
    "v_and_b32 v37, 3, v37\n"
    "v_lshlrev_b32 v37, 7, v37\n"
    "v_add_u32 v37, s85, v37\n"
    "v_min_u32 v37, L_end_%=-L_pc_%=-4, v37\n"
    "global_load_dword v19, v37, s[88:89]\n"
    "L_ct_%=:\n"
    "s_waitcnt vmcnt(13)\n"
    "s_barrier\n"
    "v_mfma_f32_16x16x32_bf16 v[192:195], v[208:211], v[44:47], v[28:31]\n"
    "ds_read_b128 v[240:243], v17 offset:0\n"
    "v_add_f32 v21, v21, v184\n"
    "v_mfma_f32_16x16x32_bf16 v[196:199], v[212:215], v[44:47], v[28:31]\n"
    "s_add_u32 m0, s64, 0\n"
    "s_add_u32 s75, s73, s74\n"
    "buffer_load_dwordx4 v12, s[48:51], s75 offen offset:0 nt lds\n"
    "v_mfma_f32_16x16x32_bf16 v[192:195], v[216:219], v[56:59], v[192:195]\n"
    "v_exp_f32 v187, v187\n"
    "v_mfma_f32_16x16x32_bf16 v[196:199], v[220:223], v[56:59], v[196:199]\n"
    "ds_read_b128 v[244:247], v17 offset:1024\n"
    "v_add_f32 v21, v21, v185\n"
    "v_mfma_f32_16x16x32_bf16 v[192:195], v[224:227], v[68:71], v[192:195]\n"
    "ds_read_b128 v[248:251], v17 offset:2048\n"
    "v_cvt_pk_bf16_f32 v184, v184, v185\n"
    "v_mfma_f32_16x16x32_bf16 v[196:199], v[228:231], v[68:71], v[196:199]\n"
    "ds_read_b128 v[252:255], v17 offset:3072\n"
    "v_exp_f32 v188, v188\n"
    "v_mfma_f32_16x16x32_bf16 v[192:195], v[232:235], v[80:83], v[192:195]\n"
    "ds_read_b128 a[240:243], v17 offset:4096\n"
    "v_add_f32 v21, v21, v186\n"
    "v_mfma_f32_16x16x32_bf16 v[196:199], v[236:239], v[80:83], v[196:199]\n"
    "ds_read_b128 a[244:247], v17 offset:5120\n"
    "v_mfma_f32_16x16x32_bf16 v[200:203], v[208:211], v[48:51], v[32:35]\n"
    "v_exp_f32 v189, v189\n"
    "v_mfma_f32_16x16x32_bf16 v[204:207], v[212:215], v[48:51], v[32:35]\n"
    "ds_read_b64_tr_b16 a[248:249], v16 offset:40960\n"
    "ds_read_b64_tr_b16 a[250:251], v16 offset:43008\n"
    "ds_read_b64_tr_b16 v[208:209], v16 offset:28672\n"
    "ds_read_b64_tr_b16 v[210:211], v16 offset:30720\n"
    "v_add_f32 v21, v21, v187\n"
    "v_mfma_f32_16x16x32_bf16 v[200:203], v[216:219], v[60:63], v[200:203]\n"
    "buffer_load_dwordx4 v12, s[48:51], s75 offen offset:1024 nt lds\n"
    "v_cvt_pk_bf16_f32 v185, v186, v187\n"
    "v_mfma_f32_16x16x32_bf16 v[204:207], v[220:223], v[60:63], v[204:207]\n"
    "v_exp_f32 v190, v190\n"
    "v_mfma_f32_16x16x32_bf16 v[200:203], v[224:227], v[72:75], v[200:203]\n"
    "v_add_f32 v21, v21, v188\n"
    "ds_read_b64_tr_b16 v[212:213], v16 offset:32768\n"
    "v_mfma_f32_16x16x32_bf16 v[204:207], v[228:231], v[72:75], v[204:207]\n"
    "v_exp_f32 v191, v191\n"
    "v_mfma_f32_16x16x32_bf16 v[200:203], v[232:235], v[84:87], v[200:203]\n"
    "v_add_f32 v21, v21, v189\n"
    "ds_read_b64_tr_b16 v[214:215], v16 offset:34816\n"
    "v_mfma_f32_16x16x32_bf16 v[204:207], v[236:239], v[84:87], v[204:207]\n"
    "s_waitcnt lgkmcnt(2)\n"
    "v_cvt_pk_bf16_f32 v186, v188, v189\n"
    "ds_read_b64_tr_b16 v[216:217], v16 offset:36864\n"
    "v_mfma_f32_16x16x32_bf16 a[0:3], v[208:211], v[240:243], a[0:3]\n"
    "buffer_load_dwordx4 v13, s[48:51], s75 offen offset:2048 nt lds\n"
    "v_add_f32 v21, v21, v190\n"
    "v_mfma_f32_16x16x32_bf16 a[4:7], v[208:211], v[244:247], a[4:7]\n"
    "ds_read_b64_tr_b16 v[218:219], v16 offset:38912\n"
    "v_add_f32 v21, v21, v191\n"
    "v_cvt_pk_bf16_f32 v187, v190, v191\n"
    "v_mfma_f32_16x16x32_bf16 a[8:11], v[208:211], v[248:251], a[8:11]\n"
    "v_max3_f32 v37, v192, v193, v194\n"
    "ds_read_b64_tr_b16 a[252:253], v16 offset:45056\n"
    "v_mfma_f32_16x16x32_bf16 a[12:15], v[208:211], v[252:255], a[12:15]\n"
    "v_max3_f32 v36, v195, v196, v197\n"
    "ds_read_b64_tr_b16 a[254:255], v16 offset:47104\n"
    "v_mfma_f32_16x16x32_bf16 a[16:19], v[208:211], a[240:243], a[16:19]\n"
    "v_max3_f32 v37, v37, v198, v199\n"
    "v_max_f32 v37, v37, v36\n"
    "v_mfma_f32_16x16x32_bf16 a[20:23], v[208:211], a[244:247], a[20:23]\n"
    "s_waitcnt lgkmcnt(4)\n"
    "ds_read_b64_tr_b16 v[220:221], v3 offset:0\n"
    "v_cmp_nge_f32 vcc, s77, v37\n"
    "v_mfma_f32_16x16x32_bf16 a[44:47], v[212:215], a[244:247], a[44:47]\n"
    "s_cbranch_vccnz L_stub201b_%=\n"
    "L_back201b_%=:\n"
    "ds_read_b64_tr_b16 v[222:223], v3 offset:1024\n"
    "buffer_load_dwordx4 v13, s[48:51], s75 offen offset:3072 nt lds\n"
    "v_mfma_f32_16x16x32_bf16 a[40:43], v[212:215], a[240:243], a[40:43]\n"
    "ds_read_b64_tr_b16 v[224:225], v4 offset:0\n"
    "v_exp_f32 v192, v192\n"
    "v_mfma_f32_16x16x32_bf16 a[36:39], v[212:215], v[252:255], a[36:39]\n"
    "v_exp_f32 v193, v193\n"
    "v_mfma_f32_16x16x32_bf16 a[32:35], v[212:215], v[248:251], a[32:35]\n"
    "ds_read_b64_tr_b16 v[226:227], v4 offset:1024\n"
    "v_mfma_f32_16x16x32_bf16 a[28:31], v[212:215], v[244:247], a[28:31]\n"
    "ds_read_b64_tr_b16 v[228:229], v5 offset:0\n"
    "v_exp_f32 v194, v194\n"
    "v_mfma_f32_16x16x32_bf16 a[24:27], v[212:215], v[240:243], a[24:27]\n"
    "s_waitcnt lgkmcnt(5)\n"
    "ds_read_b64_tr_b16 v[230:231], v5 offset:1024\n"
    "v_add_f32 v22, v22, v192\n"
    "v_mfma_f32_16x16x32_bf16 a[48:51], v[216:219], v[240:243], a[48:51]\n"
    "ds_read_b64_tr_b16 v[208:209], v0 offset:0\n"
    "v_exp_f32 v195, v195\n"
    "v_mfma_f32_16x16x32_bf16 a[52:55], v[216:219], v[244:247], a[52:55]\n"
    "v_add_f32 v22, v22, v193\n"
    "ds_read_b64_tr_b16 v[210:211], v0 offset:1024\n"
    "v_mfma_f32_16x16x32_bf16 a[56:59], v[216:219], v[248:251], a[56:59]\n"
    "v_cvt_pk_bf16_f32 v192, v192, v193\n"
    "ds_read_b64_tr_b16 v[232:233], v6 offset:0\n"
    "v_mfma_f32_16x16x32_bf16 a[60:63], v[216:219], v[252:255], a[60:63]\n"
    "v_exp_f32 v196, v196\n"
    "v_mfma_f32_16x16x32_bf16 a[64:67], v[216:219], a[240:243], a[64:67]\n"
    "ds_read_b64_tr_b16 v[234:235], v6 offset:1024\n"
    "v_add_f32 v22, v22, v194\n"
    "v_mfma_f32_16x16x32_bf16 a[68:71], v[216:219], a[244:247], a[68:71]\n"
    "ds_read_b64_tr_b16 v[236:237], v7 offset:0\n"
    "v_exp_f32 v197, v197\n"
    "v_mfma_f32_16x16x32_bf16 a[92:95], a[248:251], a[244:247], a[92:95]\n"
    "ds_read_b64_tr_b16 v[238:239], v7 offset:1024\n"
    "v_add_f32 v22, v22, v195\n"
    "v_mfma_f32_16x16x32_bf16 a[88:91], a[248:251], a[240:243], a[88:91]\n"
    "ds_read_b64_tr_b16 v[212:213], v1 offset:0\n"
    "v_cvt_pk_bf16_f32 v193, v194, v195\n"
    "v_mfma_f32_16x16x32_bf16 a[84:87], a[248:251], v[252:255], a[84:87]\n"
    "v_exp_f32 v198, v198\n"
    "v_mfma_f32_16x16x32_bf16 a[80:83], a[248:251], v[248:251], a[80:83]\n"
    "ds_read_b64_tr_b16 v[214:215], v1 offset:1024\n"
    "v_add_f32 v22, v22, v196\n"
    "v_mfma_f32_16x16x32_bf16 a[76:79], a[248:251], v[244:247], a[76:79]\n"
    "ds_read_b64_tr_b16 v[216:217], v2 offset:0\n"
    "ds_read_b64_tr_b16 v[218:219], v2 offset:1024\n"
    "v_mfma_f32_16x16x32_bf16 a[72:75], a[248:251], v[240:243], a[72:75]\n"
    "v_exp_f32 v199, v199\n"
    "v_mfma_f32_16x16x32_bf16 a[96:99], a[252:255], v[240:243], a[96:99]\n"
    "v_add_f32 v22, v22, v197\n"
    "v_cvt_pk_bf16_f32 v194, v196, v197\n"
    "v_mfma_f32_16x16x32_bf16 a[100:103], a[252:255], v[244:247], a[100:103]\n"
    "v_add_f32 v22, v22, v198\n"
    "v_add_f32 v22, v22, v199\n"
    "v_mfma_f32_16x16x32_bf16 a[104:107], a[252:255], v[248:251], a[104:107]\n"
    "v_cvt_pk_bf16_f32 v195, v198, v199\n"
    "v_max3_f32 v37, v200, v201, v202\n"
    "v_mfma_f32_16x16x32_bf16 a[108:111], a[252:255], v[252:255], a[108:111]\n"
    "v_max3_f32 v36, v203, v204, v205\n"
    "v_max3_f32 v37, v37, v206, v207\n"
    "v_mfma_f32_16x16x32_bf16 a[112:115], a[252:255], a[240:243], a[112:115]\n"
    "v_max_f32 v37, v37, v36\n"
    "v_cmp_nge_f32 vcc, s77, v37\n"
    "v_mfma_f32_16x16x32_bf16 a[116:119], a[252:255], a[244:247], a[116:119]\n"
    "s_cbranch_vccnz L_stub202b_%=\n"
    "L_back202b_%=:\n"
    "s_waitcnt vmcnt(13) lgkmcnt(0)\n"
    "s_barrier\n"
    "v_mfma_f32_16x16x32_bf16 v[88:91], v[208:211], v[184:187], v[88:91]\n"
    "ds_read_b128 v[240:243], v8 offset:16384\n"
    "v_mfma_f32_16x16x32_bf16 v[100:103], v[212:215], v[184:187], v[100:103]\n"
    "ds_read_b128 v[244:247], v8 offset:17408\n"
    "v_mfma_f32_16x16x32_bf16 v[112:115], v[216:219], v[184:187], v[112:115]\n"
    "s_add_u32 m0, s64, 32768\n"
    "s_add_u32 s73, s73, 8192\n"
    "buffer_load_dwordx4 v14, s[52:55], s75 offen offset:0 nt lds\n"
    "v_exp_f32 v200, v200\n"
    "v_mfma_f32_16x16x32_bf16 v[124:127], v[220:223], v[184:187], v[124:127]\n"
    "ds_read_b128 v[248:251], v9 offset:16384\n"
    "buffer_load_dwordx4 v14, s[52:55], s75 offen offset:1024 nt lds\n"
    "v_mfma_f32_16x16x32_bf16 v[136:139], v[224:227], v[184:187], v[136:139]\n"
    "ds_read_b128 v[252:255], v9 offset:17408\n"
    "v_exp_f32 v201, v201\n"
    "v_mfma_f32_16x16x32_bf16 v[148:151], v[228:231], v[184:187], v[148:151]\n"
    "v_exp_f32 v202, v202\n"
    "v_mfma_f32_16x16x32_bf16 v[160:163], v[232:235], v[184:187], v[160:163]\n"
    "ds_read_b128 a[240:243], v10 offset:16384\n"
    "v_add_f32 v23, v23, v200\n"
    "v_mfma_f32_16x16x32_bf16 v[172:175], v[236:239], v[184:187], v[172:175]\n"
    "ds_read_b128 a[244:247], v10 offset:17408\n"
    "buffer_load_dwordx4 v15, s[52:55], s75 offen offset:2048 nt lds\n"
    "v_mfma_f32_16x16x32_bf16 v[176:179], v[236:239], v[192:195], v[176:179]\n"
    "ds_read_b128 a[248:251], v11 offset:16384\n"
    "v_exp_f32 v203, v203\n"
    "v_mfma_f32_16x16x32_bf16 v[164:167], v[232:235], v[192:195], v[164:167]\n"
    "ds_read_b128 a[252:255], v11 offset:17408\n"
    "v_add_f32 v23, v23, v201\n"
    "v_mfma_f32_16x16x32_bf16 v[152:155], v[228:231], v[192:195], v[152:155]\n"
    "buffer_load_dwordx4 v15, s[52:55], s75 offen offset:3072 nt lds\n"
    "v_cvt_pk_bf16_f32 v200, v200, v201\n"
    "v_mfma_f32_16x16x32_bf16 v[140:143], v[224:227], v[192:195], v[140:143]\n"
    "v_exp_f32 v204, v204\n"
    "v_mfma_f32_16x16x32_bf16 v[128:131], v[220:223], v[192:195], v[128:131]\n"
    "v_add_f32 v23, v23, v202\n"
    "v_mfma_f32_16x16x32_bf16 v[116:119], v[216:219], v[192:195], v[116:119]\n"
    "s_and_b32 s73, s73, 0x1ffff\n"
    "s_add_u32 m0, s65, 143360\n"
    "s_nop 0\n"
    "buffer_load_dwordx4 v18, s[56:59], s79 offen offset:0  lds\n"
    "v_exp_f32 v205, v205\n"
    "v_mfma_f32_16x16x32_bf16 v[104:107], v[212:215], v[192:195], v[104:107]\n"
    "v_add_f32 v23, v23, v203\n"
    "v_cvt_pk_bf16_f32 v201, v202, v203\n"
    "v_mfma_f32_16x16x32_bf16 v[92:95], v[208:211], v[192:195], v[92:95]\n"
    "s_waitcnt lgkmcnt(1)\n"
    "v_exp_f32 v206, v206\n"
    "v_mfma_f32_16x16x32_bf16 v[184:187], v[240:243], v[40:43], v[24:27]\n"
    "s_add_u32 m0, s65, 147328\n"
    "s_nop 0\n"
    "buffer_load_dwordx4 v18, s[56:59], s79 offen offset:128  lds\n"
    "v_mfma_f32_16x16x32_bf16 v[188:191], v[244:247], v[40:43], v[24:27]\n"
    "v_add_f32 v23, v23, v204\n"
    "v_mfma_f32_16x16x32_bf16 v[184:187], v[248:251], v[52:55], v[184:187]\n"
    "v_exp_f32 v207, v207\n"
    "v_mfma_f32_16x16x32_bf16 v[188:191], v[252:255], v[52:55], v[188:191]\n"
    "v_add_f32 v23, v23, v205\n"
    "v_cvt_pk_bf16_f32 v202, v204, v205\n"
    "v_mfma_f32_16x16x32_bf16 v[184:187], a[240:243], v[64:67], v[184:187]\n"
    "s_add_u32 m0, s65, 151296\n"
    "s_nop 0\n"
    "buffer_load_dwordx4 v18, s[56:59], s79 offen offset:256  lds\n"
    "v_mfma_f32_16x16x32_bf16 v[188:191], a[244:247], v[64:67], v[188:191]\n"
    "v_add_f32 v23, v23, v206\n"
    "v_add_f32 v23, v23, v207\n"
    "v_cvt_pk_bf16_f32 v203, v206, v207\n"
    "v_mfma_f32_16x16x32_bf16 v[184:187], a[248:251], v[76:79], v[184:187]\n"
    "s_waitcnt lgkmcnt(0)\n"
    "v_mfma_f32_16x16x32_bf16 v[188:191], a[252:255], v[76:79], v[188:191]\n"
    "s_nop 1\n"
    "v_mfma_f32_16x16x32_bf16 v[96:99], v[208:211], v[200:203], v[96:99]\n"
    "s_nop 1\n"
    "v_mfma_f32_16x16x32_bf16 v[108:111], v[212:215], v[200:203], v[108:111]\n"
    "s_add_u32 m0, s65, 155264\n"
    "s_nop 0\n"
    "buffer_load_dwordx4 v18, s[56:59], s79 offen offset:384  lds\n"
    "s_nop 0\n"
    "v_mfma_f32_16x16x32_bf16 v[120:123], v[216:219], v[200:203], v[120:123]\n"
    "v_max3_f32 v37, v184, v185, v186\n"
    "v_max3_f32 v36, v187, v188, v189\n"
    "v_max3_f32 v37, v37, v190, v191\n"
    "v_mfma_f32_16x16x32_bf16 v[132:135], v[220:223], v[200:203], v[132:135]\n"
    "v_max_f32 v37, v37, v36\n"
    "v_cmp_nge_f32 vcc, s77, v37\n"
    "v_mfma_f32_16x16x32_bf16 v[144:147], v[224:227], v[200:203], v[144:147]\n"
    "s_cbranch_vccnz L_stub210b_%=\n"
    "L_back210b_%=:\n"
    "v_mfma_f32_16x16x32_bf16 v[156:159], v[228:231], v[200:203], v[156:159]\n"
    "s_add_u32 m0, s65, 159232\n"
    "s_nop 0\n"
    "buffer_load_dwordx4 v18, s[56:59], s79 offen offset:512  lds\n"
    "v_exp_f32 v184, v184\n"
    "v_mfma_f32_16x16x32_bf16 v[168:171], v[232:235], v[200:203], v[168:171]\n"
    "v_exp_f32 v185, v185\n"
    "v_mfma_f32_16x16x32_bf16 v[180:183], v[236:239], v[200:203], v[180:183]\n"
    "v_exp_f32 v186, v186\n"
    "s_waitcnt vmcnt(13)\n"
    "s_barrier\n"
    "v_mfma_f32_16x16x32_bf16 v[192:195], v[240:243], v[44:47], v[28:31]\n"
    "ds_read_b128 v[208:211], v17 offset:0\n"
    "v_add_f32 v21, v21, v184\n"
    "v_mfma_f32_16x16x32_bf16 v[196:199], v[244:247], v[44:47], v[28:31]\n"
    "s_add_u32 m0, s64, 16384\n"
    "s_add_u32 s75, s73, s74\n"
    "buffer_load_dwordx4 v12, s[48:51], s75 offen offset:0 nt lds\n"
    "v_mfma_f32_16x16x32_bf16 v[192:195], v[248:251], v[56:59], v[192:195]\n"
    "v_exp_f32 v187, v187\n"
    "v_mfma_f32_16x16x32_bf16 v[196:199], v[252:255], v[56:59], v[196:199]\n"
    "ds_read_b128 v[212:215], v17 offset:1024\n"
    "v_add_f32 v21, v21, v185\n"
    "v_mfma_f32_16x16x32_bf16 v[192:195], a[240:243], v[68:71], v[192:195]\n"
    "ds_read_b128 v[216:219], v17 offset:2048\n"
    "v_cvt_pk_bf16_f32 v184, v184, v185\n"
    "v_mfma_f32_16x16x32_bf16 v[196:199], a[244:247], v[68:71], v[196:199]\n"
    "ds_read_b128 v[220:223], v17 offset:3072\n"
    "v_exp_f32 v188, v188\n"
    "v_mfma_f32_16x16x32_bf16 v[192:195], a[248:251], v[80:83], v[192:195]\n"
    "ds_read_b128 v[224:227], v17 offset:4096\n"
    "v_add_f32 v21, v21, v186\n"
    "v_mfma_f32_16x16x32_bf16 v[196:199], a[252:255], v[80:83], v[196:199]\n"
    "ds_read_b128 v[228:231], v17 offset:5120\n"
    "v_mfma_f32_16x16x32_bf16 v[200:203], v[240:243], v[48:51], v[32:35]\n"
    "s_add_u32 s78, s78, 1\n"
    "s_cmp_eq_u32 s78, 8\n"
    "s_cselect_b32 s39, s68, 6144\n"
    "v_add_u32 v17, s39, v17\n"
    "v_exp_f32 v189, v189\n"
    "v_mfma_f32_16x16x32_bf16 v[204:207], v[244:247], v[48:51], v[32:35]\n"
    "ds_read_b64_tr_b16 v[232:233], v16 offset:20480\n"
    "ds_read_b64_tr_b16 v[234:235], v16 offset:22528\n"
    "ds_read_b64_tr_b16 v[240:241], v16 offset:8192\n"
    "v_add_f32 v21, v21, v187\n"
    "ds_read_b64_tr_b16 v[242:243], v16 offset:10240\n"
    "v_mfma_f32_16x16x32_bf16 v[200:203], v[248:251], v[60:63], v[200:203]\n"
    "buffer_load_dwordx4 v12, s[48:51], s75 offen offset:1024 nt lds\n"
    "v_cvt_pk_bf16_f32 v185, v186, v187\n"
    "v_mfma_f32_16x16x32_bf16 v[204:207], v[252:255], v[60:63], v[204:207]\n"
    "v_exp_f32 v190, v190\n"
    "v_mfma_f32_16x16x32_bf16 v[200:203], a[240:243], v[72:75], v[200:203]\n"
    "v_add_f32 v21, v21, v188\n"
    "ds_read_b64_tr_b16 v[244:245], v16 offset:12288\n"
    "v_mfma_f32_16x16x32_bf16 v[204:207], a[244:247], v[72:75], v[204:207]\n"
    "v_exp_f32 v191, v191\n"
    "v_mfma_f32_16x16x32_bf16 v[200:203], a[248:251], v[84:87], v[200:203]\n"
    "v_add_f32 v21, v21, v189\n"
    "v_cvt_pk_bf16_f32 v186, v188, v189\n"
    "v_mfma_f32_16x16x32_bf16 v[204:207], a[252:255], v[84:87], v[204:207]\n"
    "s_waitcnt lgkmcnt(1)\n"
    "ds_read_b64_tr_b16 v[246:247], v16 offset:14336\n"
    "buffer_load_dwordx4 v13, s[48:51], s75 offen offset:2048 nt lds\n"
    "v_mfma_f32_16x16x32_bf16 a[120:123], v[240:243], v[208:211], a[120:123]\n"
    "v_add_f32 v21, v21, v190\n"
    "ds_read_b64_tr_b16 v[248:249], v16 offset:16384\n"
    "v_mfma_f32_16x16x32_bf16 a[124:127], v[240:243], v[212:215], a[124:127]\n"
    "v_add_f32 v21, v21, v191\n"
    "v_cvt_pk_bf16_f32 v187, v190, v191\n"
    "v_mfma_f32_16x16x32_bf16 a[128:131], v[240:243], v[216:219], a[128:131]\n"
    "v_max3_f32 v37, v192, v193, v194\n"
    "ds_read_b64_tr_b16 v[250:251], v16 offset:18432\n"
    "v_mfma_f32_16x16x32_bf16 a[132:135], v[240:243], v[220:223], a[132:135]\n"
    "v_max3_f32 v36, v195, v196, v197\n"
    "ds_read_b64_tr_b16 v[236:237], v16 offset:24576\n"
    "v_mfma_f32_16x16x32_bf16 a[136:139], v[240:243], v[224:227], a[136:139]\n"
    "v_max3_f32 v37, v37, v198, v199\n"
    "v_max_f32 v37, v37, v36\n"
    "v_mfma_f32_16x16x32_bf16 a[140:143], v[240:243], v[228:231], a[140:143]\n"
    "s_waitcnt lgkmcnt(3)\n"
    "ds_read_b64_tr_b16 v[238:239], v16 offset:26624\n"
    "v_cmp_nge_f32 vcc, s77, v37\n"
    "v_mfma_f32_16x16x32_bf16 a[164:167], v[244:247], v[228:231], a[164:167]\n"
    "s_cbranch_vccnz L_stub211b_%=\n"
    "L_back211b_%=:\n"
    "ds_read_b64_tr_b16 v[252:253], v3 offset:16384\n"
    "buffer_load_dwordx4 v13, s[48:51], s75 offen offset:3072 nt lds\n"
    "v_mfma_f32_16x16x32_bf16 a[160:163], v[244:247], v[224:227], a[160:163]\n"
    "ds_read_b64_tr_b16 v[254:255], v3 offset:17408\n"
    "v_exp_f32 v192, v192\n"
    "v_mfma_f32_16x16x32_bf16 a[156:159], v[244:247], v[220:223], a[156:159]\n"
    "ds_read_b64_tr_b16 a[240:241], v4 offset:16384\n"
    "v_exp_f32 v193, v193\n"
    "v_mfma_f32_16x16x32_bf16 a[152:155], v[244:247], v[216:219], a[152:155]\n"
    "ds_read_b64_tr_b16 a[242:243], v4 offset:17408\n"
    "v_mfma_f32_16x16x32_bf16 a[148:151], v[244:247], v[212:215], a[148:151]\n"
    "v_exp_f32 v194, v194\n"
    "v_mfma_f32_16x16x32_bf16 a[144:147], v[244:247], v[208:211], a[144:147]\n"
    "s_waitcnt lgkmcnt(5)\n"
    "ds_read_b64_tr_b16 a[244:245], v5 offset:16384\n"
    "v_add_f32 v22, v22, v192\n"
    "v_mfma_f32_16x16x32_bf16 a[168:171], v[248:251], v[208:211], a[168:171]\n"
    "ds_read_b64_tr_b16 a[246:247], v5 offset:17408\n"
    "v_exp_f32 v195, v195\n"
    "v_mfma_f32_16x16x32_bf16 a[172:175], v[248:251], v[212:215], a[172:175]\n"
    "ds_read_b64_tr_b16 v[240:241], v0 offset:16384\n"
    "v_add_f32 v22, v22, v193\n"
    "v_mfma_f32_16x16x32_bf16 a[176:179], v[248:251], v[216:219], a[176:179]\n"
    "ds_read_b64_tr_b16 v[242:243], v0 offset:17408\n"
    "v_cvt_pk_bf16_f32 v192, v192, v193\n"
    "v_mfma_f32_16x16x32_bf16 a[180:183], v[248:251], v[220:223], a[180:183]\n"
    "ds_read_b64_tr_b16 a[248:249], v6 offset:16384\n"
    "v_exp_f32 v196, v196\n"
    "v_mfma_f32_16x16x32_bf16 a[184:187], v[248:251], v[224:227], a[184:187]\n"
    "ds_read_b64_tr_b16 a[250:251], v6 offset:17408\n"
    "v_add_f32 v22, v22, v194\n"
    "v_mfma_f32_16x16x32_bf16 a[188:191], v[248:251], v[228:231], a[188:191]\n"
    "v_exp_f32 v197, v197\n"
    "v_mfma_f32_16x16x32_bf16 a[212:215], v[232:235], v[228:231], a[212:215]\n"
    "ds_read_b64_tr_b16 a[252:253], v7 offset:16384\n"
    "v_add_f32 v22, v22, v195\n"
    "v_mfma_f32_16x16x32_bf16 a[208:211], v[232:235], v[224:227], a[208:211]\n"
    "ds_read_b64_tr_b16 a[254:255], v7 offset:17408\n"
    "v_cvt_pk_bf16_f32 v193, v194, v195\n"
    "v_mfma_f32_16x16x32_bf16 a[204:207], v[232:235], v[220:223], a[204:207]\n"
    "ds_read_b64_tr_b16 v[244:245], v1 offset:16384\n"
    "v_exp_f32 v198, v198\n"
    "v_mfma_f32_16x16x32_bf16 a[200:203], v[232:235], v[216:219], a[200:203]\n"
    "ds_read_b64_tr_b16 v[246:247], v1 offset:17408\n"
    "v_add_f32 v22, v22, v196\n"
    "v_mfma_f32_16x16x32_bf16 a[196:199], v[232:235], v[212:215], a[196:199]\n"
    "ds_read_b64_tr_b16 v[248:249], v2 offset:16384\n"
    "ds_read_b64_tr_b16 v[250:251], v2 offset:17408\n"
    "v_mfma_f32_16x16x32_bf16 a[192:195], v[232:235], v[208:211], a[192:195]\n"
    "s_waitcnt lgkmcnt(6)\n"
    "v_exp_f32 v199, v199\n"
    "v_mfma_f32_16x16x32_bf16 a[216:219], v[236:239], v[208:211], a[216:219]\n"
    "v_add_f32 v22, v22, v197\n"
    "v_cvt_pk_bf16_f32 v194, v196, v197\n"
    "v_mfma_f32_16x16x32_bf16 a[220:223], v[236:239], v[212:215], a[220:223]\n"
    "v_add_f32 v22, v22, v198\n"
    "v_add_f32 v22, v22, v199\n"
    "v_mfma_f32_16x16x32_bf16 a[224:227], v[236:239], v[216:219], a[224:227]\n"
    "v_cvt_pk_bf16_f32 v195, v198, v199\n"
    "v_max3_f32 v37, v200, v201, v202\n"
    "v_mfma_f32_16x16x32_bf16 a[228:231], v[236:239], v[220:223], a[228:231]\n"
    "v_max3_f32 v36, v203, v204, v205\n"
    "v_max3_f32 v37, v37, v206, v207\n"
    "v_mfma_f32_16x16x32_bf16 a[232:235], v[236:239], v[224:227], a[232:235]\n"
    "v_max_f32 v37, v37, v36\n"
    "v_cmp_nge_f32 vcc, s77, v37\n"
    "v_mfma_f32_16x16x32_bf16 a[236:239], v[236:239], v[228:231], a[236:239]\n"
    "s_cbranch_vccnz L_stub212b_%=\n"
    "L_back212b_%=:\n"
    "s_waitcnt vmcnt(13) lgkmcnt(0)\n"
    "s_barrier\n"
    "v_mfma_f32_16x16x32_bf16 v[88:91], v[240:243], v[184:187], v[88:91]\n"
    "ds_read_b128 v[208:211], v8 offset:0\n"
    "v_mfma_f32_16x16x32_bf16 v[100:103], v[244:247], v[184:187], v[100:103]\n"
    "ds_read_b128 v[212:215], v8 offset:1024\n"
    "v_mfma_f32_16x16x32_bf16 v[112:115], v[248:251], v[184:187], v[112:115]\n"
    "s_add_u32 m0, s64, 49152\n"
    "s_add_u32 s73, s73, 8192\n"
    "buffer_load_dwordx4 v14, s[52:55], s75 offen offset:0 nt lds\n"
    "v_exp_f32 v200, v200\n"
    "v_mfma_f32_16x16x32_bf16 v[124:127], v[252:255], v[184:187], v[124:127]\n"
    "ds_read_b128 v[216:219], v9 offset:0\n"
    "buffer_load_dwordx4 v14, s[52:55], s75 offen offset:1024 nt lds\n"
    "v_mfma_f32_16x16x32_bf16 v[136:139], a[240:243], v[184:187], v[136:139]\n"
    "ds_read_b128 v[220:223], v9 offset:1024\n"
    "v_exp_f32 v201, v201\n"
    "v_mfma_f32_16x16x32_bf16 v[148:151], a[244:247], v[184:187], v[148:151]\n"
    "v_exp_f32 v202, v202\n"
    "v_mfma_f32_16x16x32_bf16 v[160:163], a[248:251], v[184:187], v[160:163]\n"
    "ds_read_b128 v[224:227], v10 offset:0\n"
    "buffer_load_dwordx4 v15, s[52:55], s75 offen offset:2048 nt lds\n"
    "v_mfma_f32_16x16x32_bf16 v[172:175], a[252:255], v[184:187], v[172:175]\n"
    "ds_read_b128 v[228:231], v10 offset:1024\n"
    "v_add_f32 v23, v23, v200\n"
    "v_mfma_f32_16x16x32_bf16 v[176:179], a[252:255], v[192:195], v[176:179]\n"
    "ds_read_b128 v[232:235], v11 offset:0\n"
    "v_exp_f32 v203, v203\n"
    "v_mfma_f32_16x16x32_bf16 v[164:167], a[248:251], v[192:195], v[164:167]\n"
    "ds_read_b128 v[236:239], v11 offset:1024\n"
    "v_add_f32 v23, v23, v201\n"
    "v_mfma_f32_16x16x32_bf16 v[152:155], a[244:247], v[192:195], v[152:155]\n"
    "buffer_load_dwordx4 v15, s[52:55], s75 offen offset:3072 nt lds\n"
    "v_cvt_pk_bf16_f32 v200, v200, v201\n"
    "v_mfma_f32_16x16x32_bf16 v[140:143], a[240:243], v[192:195], v[140:143]\n"
    "v_exp_f32 v204, v204\n"
    "v_mfma_f32_16x16x32_bf16 v[128:131], v[252:255], v[192:195], v[128:131]\n"
    "s_and_b32 s73, s73, 0x1ffff\n"
    "s_add_u32 m0, s65, 122240\n"
    "s_nop 0\n"
    "buffer_load_dwordx4 v18, s[56:59], s79 offen offset:640  lds\n"
    "v_add_f32 v23, v23, v202\n"
    "v_mfma_f32_16x16x32_bf16 v[116:119], v[248:251], v[192:195], v[116:119]\n"
    "v_exp_f32 v205, v205\n"
    "v_mfma_f32_16x16x32_bf16 v[104:107], v[244:247], v[192:195], v[104:107]\n"
    "v_add_f32 v23, v23, v203\n"
    "v_cvt_pk_bf16_f32 v201, v202, v203\n"
    "v_mfma_f32_16x16x32_bf16 v[92:95], v[240:243], v[192:195], v[92:95]\n"
    "s_waitcnt lgkmcnt(1)\n"
    "s_add_u32 m0, s65, 126208\n"
    "s_nop 0\n"
    "buffer_load_dwordx4 v18, s[56:59], s79 offen offset:768  lds\n"
    "v_mfma_f32_16x16x32_bf16 v[184:187], v[208:211], v[40:43], v[24:27]\n"
    "v_exp_f32 v206, v206\n"
    "v_mfma_f32_16x16x32_bf16 v[188:191], v[212:215], v[40:43], v[24:27]\n"
    "v_add_f32 v23, v23, v204\n"
    "v_mfma_f32_16x16x32_bf16 v[184:187], v[216:219], v[52:55], v[184:187]\n"
    "v_exp_f32 v207, v207\n"
    "v_mfma_f32_16x16x32_bf16 v[188:191], v[220:223], v[52:55], v[188:191]\n"
    "s_add_u32 m0, s65, 130176\n"
    "s_nop 0\n"
    "buffer_load_dwordx4 v18, s[56:59], s79 offen offset:896  lds\n"
    "v_mfma_f32_16x16x32_bf16 v[184:187], v[224:227], v[64:67], v[184:187]\n"
    "v_add_f32 v23, v23, v205\n"
    "v_cvt_pk_bf16_f32 v202, v204, v205\n"
    "v_mfma_f32_16x16x32_bf16 v[188:191], v[228:231], v[64:67], v[188:191]\n"
    "v_add_f32 v23, v23, v206\n"
    "v_add_f32 v23, v23, v207\n"
    "v_cvt_pk_bf16_f32 v203, v206, v207\n"
    "v_mfma_f32_16x16x32_bf16 v[184:187], v[232:235], v[76:79], v[184:187]\n"
    "s_waitcnt lgkmcnt(0)\n"
    "s_add_u32 m0, s65, 134144\n"
    "s_nop 0\n"
    "buffer_load_dwordx4 v18, s[56:59], s79 offen offset:1024  lds\n"
    "v_mfma_f32_16x16x32_bf16 v[188:191], v[236:239], v[76:79], v[188:191]\n"
    "s_nop 1\n"
    "v_mfma_f32_16x16x32_bf16 v[96:99], v[240:243], v[200:203], v[96:99]\n"
    "s_nop 1\n"
    "v_mfma_f32_16x16x32_bf16 v[108:111], v[244:247], v[200:203], v[108:111]\n"
    "s_nop 0\n"
    "v_mfma_f32_16x16x32_bf16 v[120:123], v[248:251], v[200:203], v[120:123]\n"
    "v_max3_f32 v37, v184, v185, v186\n"
    "v_max3_f32 v36, v187, v188, v189\n"
    "v_max3_f32 v37, v37, v190, v191\n"
    "v_mfma_f32_16x16x32_bf16 v[132:135], v[252:255], v[200:203], v[132:135]\n"
    "s_add_u32 m0, s65, 138112\n"
    "s_nop 0\n"
    "buffer_load_dwordx4 v18, s[56:59], s79 offen offset:1152  lds\n"
    "s_add_u32 s79, s79, 40960\n"
    "s_cmp_ge_u32 s79, 327680\n"
    "s_cselect_b32 s79, s87, s79\n"
    "v_max_f32 v37, v37, v36\n"
    "v_cmp_nge_f32 vcc, s77, v37\n"
    "v_mfma_f32_16x16x32_bf16 v[144:147], a[240:243], v[200:203], v[144:147]\n"
    "s_cbranch_vccnz L_stub200b_%=\n"
    "L_back200b_%=:\n"
    "v_mfma_f32_16x16x32_bf16 v[156:159], a[244:247], v[200:203], v[156:159]\n"
    "v_exp_f32 v184, v184\n"
    "v_mfma_f32_16x16x32_bf16 v[168:171], a[248:251], v[200:203], v[168:171]\n"
    "v_exp_f32 v185, v185\n"
    "v_mfma_f32_16x16x32_bf16 v[180:183], a[252:255], v[200:203], v[180:183]\n"
    "v_exp_f32 v186, v186\n"
    "s_add_u32 s66, s66, 1\n"
    "s_cmp_lg_u32 s66, 6\n"
    "s_cbranch_scc1 L_r2_%=\n"
    "s_waitcnt vmcnt(13)\n"
    "s_barrier\n"
    "v_mfma_f32_16x16x32_bf16 v[192:195], v[208:211], v[44:47], v[28:31]\n"
    "ds_read_b128 v[240:243], v17 offset:0\n"
    "v_add_f32 v21, v21, v184\n"
    "v_mfma_f32_16x16x32_bf16 v[196:199], v[212:215], v[44:47], v[28:31]\n"
    "s_add_u32 m0, s64, 0\n"
    "s_add_u32 s71, s80, 0\n"
    "buffer_load_dwordx4 v18, s[56:59], s71 offen offset:0  lds\n"
    "v_mfma_f32_16x16x32_bf16 v[192:195], v[216:219], v[56:59], v[192:195]\n"
    "v_exp_f32 v187, v187\n"
    "v_mfma_f32_16x16x32_bf16 v[196:199], v[220:223], v[56:59], v[196:199]\n"
    "ds_read_b128 v[244:247], v17 offset:1024\n"
    "v_add_f32 v21, v21, v185\n"
    "v_mfma_f32_16x16x32_bf16 v[192:195], v[224:227], v[68:71], v[192:195]\n"
    "ds_read_b128 v[248:251], v17 offset:2048\n"
    "v_cvt_pk_bf16_f32 v184, v184, v185\n"
    "v_mfma_f32_16x16x32_bf16 v[196:199], v[228:231], v[68:71], v[196:199]\n"
    "ds_read_b128 v[252:255], v17 offset:3072\n"
    "v_exp_f32 v188, v188\n"
    "v_mfma_f32_16x16x32_bf16 v[192:195], v[232:235], v[80:83], v[192:195]\n"
    "ds_read_b128 a[240:243], v17 offset:4096\n"
    "v_add_f32 v21, v21, v186\n"
    "v_mfma_f32_16x16x32_bf16 v[196:199], v[236:239], v[80:83], v[196:199]\n"
    "s_add_u32 s71, s80, 9216\n"
    "buffer_load_dwordx4 v18, s[56:59], s71 offen offset:1024  lds\n"
    "ds_read_b128 a[244:247], v17 offset:5120\n"
    "v_mfma_f32_16x16x32_bf16 v[200:203], v[208:211], v[48:51], v[32:35]\n"
    "v_exp_f32 v189, v189\n"
    "v_mfma_f32_16x16x32_bf16 v[204:207], v[212:215], v[48:51], v[32:35]\n"
    "ds_read_b64_tr_b16 a[248:249], v16 offset:40960\n"
    "ds_read_b64_tr_b16 a[250:251], v16 offset:43008\n"
    "ds_read_b64_tr_b16 v[208:209], v16 offset:28672\n"
    "ds_read_b64_tr_b16 v[210:211], v16 offset:30720\n"
    "v_add_f32 v21, v21, v187\n"
    "v_mfma_f32_16x16x32_bf16 v[200:203], v[216:219], v[60:63], v[200:203]\n"
    "v_cvt_pk_bf16_f32 v185, v186, v187\n"
    "ds_read_b64_tr_b16 v[212:213], v16 offset:32768\n"
    "v_mfma_f32_16x16x32_bf16 v[204:207], v[220:223], v[60:63], v[204:207]\n"
    "v_exp_f32 v190, v190\n"
    "v_mfma_f32_16x16x32_bf16 v[200:203], v[224:227], v[72:75], v[200:203]\n"
    "v_add_f32 v21, v21, v188\n"
    "ds_read_b64_tr_b16 v[214:215], v16 offset:34816\n"
    "v_mfma_f32_16x16x32_bf16 v[204:207], v[228:231], v[72:75], v[204:207]\n"
    "v_exp_f32 v191, v191\n"
    "v_mfma_f32_16x16x32_bf16 v[200:203], v[232:235], v[84:87], v[200:203]\n"
    "s_add_u32 s71, s80, 18432\n"
    "buffer_load_dwordx4 v18, s[56:59], s71 offen offset:2048  lds\n"
    "v_add_f32 v21, v21, v189\n"
    "v_mfma_f32_16x16x32_bf16 v[204:207], v[236:239], v[84:87], v[204:207]\n"
    "s_waitcnt lgkmcnt(2)\n"
    "v_cvt_pk_bf16_f32 v186, v188, v189\n"
    "ds_read_b64_tr_b16 v[216:217], v16 offset:36864\n"
    "v_mfma_f32_16x16x32_bf16 a[0:3], v[208:211], v[240:243], a[0:3]\n"
    "v_add_f32 v21, v21, v190\n"
    "ds_read_b64_tr_b16 v[218:219], v16 offset:38912\n"
    "v_mfma_f32_16x16x32_bf16 a[4:7], v[208:211], v[244:247], a[4:7]\n"
    "v_add_f32 v21, v21, v191\n"
    "v_cvt_pk_bf16_f32 v187, v190, v191\n"
    "v_mfma_f32_16x16x32_bf16 a[8:11], v[208:211], v[248:251], a[8:11]\n"
    "v_max3_f32 v37, v192, v193, v194\n"
    "ds_read_b64_tr_b16 a[252:253], v16 offset:45056\n"
    "v_mfma_f32_16x16x32_bf16 a[12:15], v[208:211], v[252:255], a[12:15]\n"
    "v_max3_f32 v36, v195, v196, v197\n"
    "ds_read_b64_tr_b16 a[254:255], v16 offset:47104\n"
    "v_mfma_f32_16x16x32_bf16 a[16:19], v[208:211], a[240:243], a[16:19]\n"
    "v_max3_f32 v37, v37, v198, v199\n"
    "v_max_f32 v37, v37, v36\n"
    "v_mfma_f32_16x16x32_bf16 a[20:23], v[208:211], a[244:247], a[20:23]\n"
    "s_waitcnt lgkmcnt(4)\n"
    "s_add_u32 s71, s80, 27648\n"
    "buffer_load_dwordx4 v18, s[56:59], s71 offen offset:3072  lds\n"
    "ds_read_b64_tr_b16 v[220:221], v3 offset:0\n"
    "v_cmp_nge_f32 vcc, s77, v37\n"
    "v_mfma_f32_16x16x32_bf16 a[44:47], v[212:215], a[244:247], a[44:47]\n"
    "s_cbranch_vccnz L_stub201c_%=\n"
    "L_back201c_%=:\n"
    "ds_read_b64_tr_b16 v[222:223], v3 offset:1024\n"
    "v_mfma_f32_16x16x32_bf16 a[40:43], v[212:215], a[240:243], a[40:43]\n"
    "ds_read_b64_tr_b16 v[224:225], v4 offset:0\n"
    "v_exp_f32 v192, v192\n"
    "v_mfma_f32_16x16x32_bf16 a[36:39], v[212:215], v[252:255], a[36:39]\n"
    "v_exp_f32 v193, v193\n"
    "v_mfma_f32_16x16x32_bf16 a[32:35], v[212:215], v[248:251], a[32:35]\n"
    "ds_read_b64_tr_b16 v[226:227], v4 offset:1024\n"
    "v_mfma_f32_16x16x32_bf16 a[28:31], v[212:215], v[244:247], a[28:31]\n"
    "ds_read_b64_tr_b16 v[228:229], v5 offset:0\n"
    "v_exp_f32 v194, v194\n"
    "v_mfma_f32_16x16x32_bf16 a[24:27], v[212:215], v[240:243], a[24:27]\n"
    "s_waitcnt lgkmcnt(5)\n"
    "ds_read_b64_tr_b16 v[230:231], v5 offset:1024\n"
    "v_add_f32 v22, v22, v192\n"
    "v_mfma_f32_16x16x32_bf16 a[48:51], v[216:219], v[240:243], a[48:51]\n"
    "ds_read_b64_tr_b16 v[208:209], v0 offset:0\n"
    "v_exp_f32 v195, v195\n"
    "v_mfma_f32_16x16x32_bf16 a[52:55], v[216:219], v[244:247], a[52:55]\n"
    "v_add_f32 v22, v22, v193\n"
    "ds_read_b64_tr_b16 v[210:211], v0 offset:1024\n"
    "v_mfma_f32_16x16x32_bf16 a[56:59], v[216:219], v[248:251], a[56:59]\n"
    "v_cvt_pk_bf16_f32 v192, v192, v193\n"
    "ds_read_b64_tr_b16 v[232:233], v6 offset:0\n"
    "v_mfma_f32_16x16x32_bf16 a[60:63], v[216:219], v[252:255], a[60:63]\n"
    "v_exp_f32 v196, v196\n"
    "v_mfma_f32_16x16x32_bf16 a[64:67], v[216:219], a[240:243], a[64:67]\n"
    "ds_read_b64_tr_b16 v[234:235], v6 offset:1024\n"
    "v_add_f32 v22, v22, v194\n"
    "v_mfma_f32_16x16x32_bf16 a[68:71], v[216:219], a[244:247], a[68:71]\n"
    "ds_read_b64_tr_b16 v[236:237], v7 offset:0\n"
    "v_exp_f32 v197, v197\n"
    "v_mfma_f32_16x16x32_bf16 a[92:95], a[248:251], a[244:247], a[92:95]\n"
    "ds_read_b64_tr_b16 v[238:239], v7 offset:1024\n"
    "v_add_f32 v22, v22, v195\n"
    "v_mfma_f32_16x16x32_bf16 a[88:91], a[248:251], a[240:243], a[88:91]\n"
    "ds_read_b64_tr_b16 v[212:213], v1 offset:0\n"
    "v_cvt_pk_bf16_f32 v193, v194, v195\n"
    "v_mfma_f32_16x16x32_bf16 a[84:87], a[248:251], v[252:255], a[84:87]\n"
    "v_exp_f32 v198, v198\n"
    "v_mfma_f32_16x16x32_bf16 a[80:83], a[248:251], v[248:251], a[80:83]\n"
    "ds_read_b64_tr_b16 v[214:215], v1 offset:1024\n"
    "v_add_f32 v22, v22, v196\n"
    "v_mfma_f32_16x16x32_bf16 a[76:79], a[248:251], v[244:247], a[76:79]\n"
    "ds_read_b64_tr_b16 v[216:217], v2 offset:0\n"
    "ds_read_b64_tr_b16 v[218:219], v2 offset:1024\n"
    "v_mfma_f32_16x16x32_bf16 a[72:75], a[248:251], v[240:243], a[72:75]\n"
    "v_exp_f32 v199, v199\n"
    "v_mfma_f32_16x16x32_bf16 a[96:99], a[252:255], v[240:243], a[96:99]\n"
    "v_add_f32 v22, v22, v197\n"
    "v_cvt_pk_bf16_f32 v194, v196, v197\n"
    "v_mfma_f32_16x16x32_bf16 a[100:103], a[252:255], v[244:247], a[100:103]\n"
    "v_add_f32 v22, v22, v198\n"
    "v_add_f32 v22, v22, v199\n"
    "v_mfma_f32_16x16x32_bf16 a[104:107], a[252:255], v[248:251], a[104:107]\n"
    "v_cvt_pk_bf16_f32 v195, v198, v199\n"
    "v_max3_f32 v37, v200, v201, v202\n"
    "v_mfma_f32_16x16x32_bf16 a[108:111], a[252:255], v[252:255], a[108:111]\n"
    "v_max3_f32 v36, v203, v204, v205\n"
    "v_max3_f32 v37, v37, v206, v207\n"
    "v_mfma_f32_16x16x32_bf16 a[112:115], a[252:255], a[240:243], a[112:115]\n"
    "v_max_f32 v37, v37, v36\n"
    "v_cmp_nge_f32 vcc, s77, v37\n"
    "v_mfma_f32_16x16x32_bf16 a[116:119], a[252:255], a[244:247], a[116:119]\n"
    "s_cbranch_vccnz L_stub202c_%=\n"
    "L_back202c_%=:\n"
    "s_waitcnt vmcnt(13) lgkmcnt(0)\n"
    "s_barrier\n"
    "v_mfma_f32_16x16x32_bf16 v[88:91], v[208:211], v[184:187], v[88:91]\n"
    "ds_read_b128 v[240:243], v8 offset:16384\n"
    "v_mfma_f32_16x16x32_bf16 v[100:103], v[212:215], v[184:187], v[100:103]\n"
    "ds_read_b128 v[244:247], v8 offset:17408\n"
    "v_mfma_f32_16x16x32_bf16 v[112:115], v[216:219], v[184:187], v[112:115]\n"
    "s_add_u32 m0, s64, 32768\n"
    "s_add_u32 s71, s80, 163840\n"
    "buffer_load_dwordx4 v18, s[56:59], s71 offen offset:0  lds\n"
    "v_exp_f32 v200, v200\n"
    "v_mfma_f32_16x16x32_bf16 v[124:127], v[220:223], v[184:187], v[124:127]\n"
    "ds_read_b128 v[248:251], v9 offset:16384\n"
    "v_mfma_f32_16x16x32_bf16 v[136:139], v[224:227], v[184:187], v[136:139]\n"
    "ds_read_b128 v[252:255], v9 offset:17408\n"
    "v_exp_f32 v201, v201\n"
    "v_mfma_f32_16x16x32_bf16 v[148:151], v[228:231], v[184:187], v[148:151]\n"
    "v_exp_f32 v202, v202\n"
    "v_mfma_f32_16x16x32_bf16 v[160:163], v[232:235], v[184:187], v[160:163]\n"
    "s_add_u32 s71, s80, 173056\n"
    "buffer_load_dwordx4 v18, s[56:59], s71 offen offset:1024  lds\n"
    "ds_read_b128 a[240:243], v10 offset:16384\n"
    "v_mfma_f32_16x16x32_bf16 v[172:175], v[236:239], v[184:187], v[172:175]\n"
    "ds_read_b128 a[244:247], v10 offset:17408\n"
    "v_add_f32 v23, v23, v200\n"
    "v_mfma_f32_16x16x32_bf16 v[176:179], v[236:239], v[192:195], v[176:179]\n"
    "ds_read_b128 a[248:251], v11 offset:16384\n"
    "v_exp_f32 v203, v203\n"
    "v_mfma_f32_16x16x32_bf16 v[164:167], v[232:235], v[192:195], v[164:167]\n"
    "ds_read_b128 a[252:255], v11 offset:17408\n"
    "v_add_f32 v23, v23, v201\n"
    "v_mfma_f32_16x16x32_bf16 v[152:155], v[228:231], v[192:195], v[152:155]\n"
    "s_add_u32 s71, s80, 182272\n"
    "buffer_load_dwordx4 v18, s[56:59], s71 offen offset:2048  lds\n"
    "v_cvt_pk_bf16_f32 v200, v200, v201\n"
    "v_mfma_f32_16x16x32_bf16 v[140:143], v[224:227], v[192:195], v[140:143]\n"
    "v_exp_f32 v204, v204\n"
    "v_mfma_f32_16x16x32_bf16 v[128:131], v[220:223], v[192:195], v[128:131]\n"
    "s_add_u32 s71, s80, 191488\n"
    "buffer_load_dwordx4 v18, s[56:59], s71 offen offset:3072  lds\n"
    "v_add_f32 v23, v23, v202\n"
    "v_mfma_f32_16x16x32_bf16 v[116:119], v[216:219], v[192:195], v[116:119]\n"
    "v_exp_f32 v205, v205\n"
    "v_mfma_f32_16x16x32_bf16 v[104:107], v[212:215], v[192:195], v[104:107]\n"
    "v_add_f32 v23, v23, v203\n"
    "v_cvt_pk_bf16_f32 v201, v202, v203\n"
    "v_mfma_f32_16x16x32_bf16 v[92:95], v[208:211], v[192:195], v[92:95]\n"
    "s_waitcnt lgkmcnt(1)\n"
    "v_exp_f32 v206, v206\n"
    "v_mfma_f32_16x16x32_bf16 v[184:187], v[240:243], v[40:43], v[24:27]\n"
    "s_add_u32 m0, s64, 147456\n"
    "s_add_u32 s71, s80, 128\n"
    "buffer_load_dwordx4 v18, s[56:59], s71 offen offset:0  lds\n"
    "v_mfma_f32_16x16x32_bf16 v[188:191], v[244:247], v[40:43], v[24:27]\n"
    "v_add_f32 v23, v23, v204\n"
    "v_mfma_f32_16x16x32_bf16 v[184:187], v[248:251], v[52:55], v[184:187]\n"
    "v_exp_f32 v207, v207\n"
    "v_mfma_f32_16x16x32_bf16 v[188:191], v[252:255], v[52:55], v[188:191]\n"
    "v_add_f32 v23, v23, v205\n"
    "v_cvt_pk_bf16_f32 v202, v204, v205\n"
    "v_mfma_f32_16x16x32_bf16 v[184:187], a[240:243], v[64:67], v[184:187]\n"
    "s_add_u32 s71, s80, 9344\n"
    "buffer_load_dwordx4 v18, s[56:59], s71 offen offset:1024  lds\n"
    "v_add_f32 v23, v23, v206\n"
    "v_mfma_f32_16x16x32_bf16 v[188:191], a[244:247], v[64:67], v[188:191]\n"
    "v_add_f32 v23, v23, v207\n"
    "v_cvt_pk_bf16_f32 v203, v206, v207\n"
    "v_mfma_f32_16x16x32_bf16 v[184:187], a[248:251], v[76:79], v[184:187]\n"
    "s_waitcnt lgkmcnt(0)\n"
    "v_mfma_f32_16x16x32_bf16 v[188:191], a[252:255], v[76:79], v[188:191]\n"
    "s_nop 1\n"
    "v_mfma_f32_16x16x32_bf16 v[96:99], v[208:211], v[200:203], v[96:99]\n"
    "s_nop 1\n"
    "v_mfma_f32_16x16x32_bf16 v[108:111], v[212:215], v[200:203], v[108:111]\n"
    "s_add_u32 s71, s80, 18560\n"
    "buffer_load_dwordx4 v18, s[56:59], s71 offen offset:2048  lds\n"
    "s_nop 0\n"
    "v_mfma_f32_16x16x32_bf16 v[120:123], v[216:219], v[200:203], v[120:123]\n"
    "v_max3_f32 v37, v184, v185, v186\n"
    "v_max3_f32 v36, v187, v188, v189\n"
    "v_max3_f32 v37, v37, v190, v191\n"
    "v_mfma_f32_16x16x32_bf16 v[132:135], v[220:223], v[200:203], v[132:135]\n"
    "v_max_f32 v37, v37, v36\n"
    "v_cmp_nge_f32 vcc, s77, v37\n"
    "v_mfma_f32_16x16x32_bf16 v[144:147], v[224:227], v[200:203], v[144:147]\n"
    "s_cbranch_vccnz L_stub210c_%=\n"
    "L_back210c_%=:\n"
    "v_mfma_f32_16x16x32_bf16 v[156:159], v[228:231], v[200:203], v[156:159]\n"
    "s_add_u32 s71, s80, 27776\n"
    "buffer_load_dwordx4 v18, s[56:59], s71 offen offset:3072  lds\n"
    "v_exp_f32 v184, v184\n"
    "v_mfma_f32_16x16x32_bf16 v[168:171], v[232:235], v[200:203], v[168:171]\n"
    "v_exp_f32 v185, v185\n"
    "v_mfma_f32_16x16x32_bf16 v[180:183], v[236:239], v[200:203], v[180:183]\n"
    "v_exp_f32 v186, v186\n"
    "s_waitcnt vmcnt(12)\n"
    "s_barrier\n"
    "v_mfma_f32_16x16x32_bf16 v[192:195], v[240:243], v[44:47], v[28:31]\n"
    "ds_read_b128 v[208:211], v17 offset:0\n"
    "v_add_f32 v21, v21, v184\n"
    "v_mfma_f32_16x16x32_bf16 v[196:199], v[244:247], v[44:47], v[28:31]\n"
    "s_add_u32 m0, s64, 16384\n"
    "s_add_u32 s71, s80, 163968\n"
    "buffer_load_dwordx4 v18, s[56:59], s71 offen offset:0  lds\n"
    "v_mfma_f32_16x16x32_bf16 v[192:195], v[248:251], v[56:59], v[192:195]\n"
    "v_exp_f32 v187, v187\n"
    "v_mfma_f32_16x16x32_bf16 v[196:199], v[252:255], v[56:59], v[196:199]\n"
    "ds_read_b128 v[212:215], v17 offset:1024\n"
    "v_add_f32 v21, v21, v185\n"
    "v_mfma_f32_16x16x32_bf16 v[192:195], a[240:243], v[68:71], v[192:195]\n"
    "ds_read_b128 v[216:219], v17 offset:2048\n"
    "v_cvt_pk_bf16_f32 v184, v184, v185\n"
    "v_exp_f32 v188, v188\n"
    "v_mfma_f32_16x16x32_bf16 v[196:199], a[244:247], v[68:71], v[196:199]\n"
    "ds_read_b128 v[220:223], v17 offset:3072\n"
    "v_add_f32 v21, v21, v186\n"
    "v_mfma_f32_16x16x32_bf16 v[192:195], a[248:251], v[80:83], v[192:195]\n"
    "ds_read_b128 v[224:227], v17 offset:4096\n"
    "v_exp_f32 v189, v189\n"
    "v_mfma_f32_16x16x32_bf16 v[196:199], a[252:255], v[80:83], v[196:199]\n"
    "ds_read_b128 v[228:231], v17 offset:5120\n"
    "v_add_f32 v21, v21, v187\n"
    "v_mfma_f32_16x16x32_bf16 v[200:203], v[240:243], v[48:51], v[32:35]\n"
    "s_add_u32 s78, s78, 1\n"
    "s_cmp_eq_u32 s78, 8\n"
    "s_cselect_b32 s39, s68, 6144\n"
    "v_add_u32 v17, s39, v17\n"
    "v_cvt_pk_bf16_f32 v185, v186, v187\n"
    "v_exp_f32 v190, v190\n"
    "v_mfma_f32_16x16x32_bf16 v[204:207], v[244:247], v[48:51], v[32:35]\n"
    "s_add_u32 s71, s80, 173184\n"
    "buffer_load_dwordx4 v18, s[56:59], s71 offen offset:1024  lds\n"
    "ds_read_b64_tr_b16 v[232:233], v16 offset:20480\n"
    "ds_read_b64_tr_b16 v[234:235], v16 offset:22528\n"
    "ds_read_b64_tr_b16 v[240:241], v16 offset:8192\n"
    "v_add_f32 v21, v21, v188\n"
    "ds_read_b64_tr_b16 v[242:243], v16 offset:10240\n"
    "v_mfma_f32_16x16x32_bf16 v[200:203], v[248:251], v[60:63], v[200:203]\n"
    "v_exp_f32 v191, v191\n"
    "v_mfma_f32_16x16x32_bf16 v[204:207], v[252:255], v[60:63], v[204:207]\n"
    "v_add_f32 v21, v21, v189\n"
    "v_cvt_pk_bf16_f32 v186, v188, v189\n"
    "v_mfma_f32_16x16x32_bf16 v[200:203], a[240:243], v[72:75], v[200:203]\n"
    "v_add_f32 v21, v21, v190\n"
    "v_add_f32 v21, v21, v191\n"
    "v_mfma_f32_16x16x32_bf16 v[204:207], a[244:247], v[72:75], v[204:207]\n"
    "v_cvt_pk_bf16_f32 v187, v190, v191\n"
    "ds_read_b64_tr_b16 v[244:245], v16 offset:12288\n"
    "v_mfma_f32_16x16x32_bf16 v[200:203], a[248:251], v[84:87], v[200:203]\n"
    "v_mov_b32 v25, v21\n"
    "s_nop 1\n"
    "v_permlane32_swap_b32 v21, v25\n"
    "v_mfma_f32_16x16x32_bf16 v[204:207], a[252:255], v[84:87], v[204:207]\n"
    "s_waitcnt lgkmcnt(1)\n"
    "s_add_u32 s71, s80, 182400\n"
    "buffer_load_dwordx4 v18, s[56:59], s71 offen offset:2048  lds\n"
    "v_add_f32 v21, v21, v25\n"
    "v_mfma_f32_16x16x32_bf16 a[120:123], v[240:243], v[208:211], a[120:123]\n"
    "ds_read_b64_tr_b16 v[246:247], v16 offset:14336\n"
    "v_mov_b32 v25, v21\n"
    "s_nop 1\n"
    "v_permlane16_swap_b32 v21, v25\n"
    "v_mfma_f32_16x16x32_bf16 a[124:127], v[240:243], v[212:215], a[124:127]\n"
    "ds_read_b64_tr_b16 v[248:249], v16 offset:16384\n"
    "v_add_f32 v21, v21, v25\n"
    "v_mfma_f32_16x16x32_bf16 a[128:131], v[240:243], v[216:219], a[128:131]\n"
    "v_rcp_f32 v24, v21\n"
    "v_mfma_f32_16x16x32_bf16 a[132:135], v[240:243], v[220:223], a[132:135]\n"
    "ds_read_b64_tr_b16 v[250:251], v16 offset:18432\n"
    "v_max3_f32 v37, v192, v193, v194\n"
    "v_max3_f32 v36, v195, v196, v197\n"
    "v_mfma_f32_16x16x32_bf16 a[136:139], v[240:243], v[224:227], a[136:139]\n"
    "ds_read_b64_tr_b16 v[236:237], v16 offset:24576\n"
    "v_max3_f32 v37, v37, v198, v199\n"
    "v_max_f32 v37, v37, v36\n"
    "v_mfma_f32_16x16x32_bf16 a[140:143], v[240:243], v[228:231], a[140:143]\n"
    "s_add_u32 s71, s80, 191616\n"
    "buffer_load_dwordx4 v18, s[56:59], s71 offen offset:3072  lds\n"
    "ds_read_b64_tr_b16 v[238:239], v16 offset:26624\n"
    "v_cmp_nge_f32 vcc, s77, v37\n"
    "s_cbranch_vccnz L_stub211c_%=\n"
    "L_back211c_%=:\n"
    "s_waitcnt lgkmcnt(4)\n"
    "v_mfma_f32_16x16x32_bf16 a[164:167], v[244:247], v[228:231], a[164:167]\n"
    "ds_read_b64_tr_b16 v[252:253], v3 offset:16384\n"
    "v_exp_f32 v192, v192\n"
    "v_mfma_f32_16x16x32_bf16 a[160:163], v[244:247], v[224:227], a[160:163]\n"
    "ds_read_b64_tr_b16 v[254:255], v3 offset:17408\n"
    "v_exp_f32 v193, v193\n"
    "v_mfma_f32_16x16x32_bf16 a[156:159], v[244:247], v[220:223], a[156:159]\n"
    "ds_read_b64_tr_b16 a[240:241], v4 offset:16384\n"
    "v_exp_f32 v194, v194\n"
    "v_mfma_f32_16x16x32_bf16 a[152:155], v[244:247], v[216:219], a[152:155]\n"
    "ds_read_b64_tr_b16 a[242:243], v4 offset:17408\n"
    "v_add_f32 v22, v22, v192\n"
    "v_mfma_f32_16x16x32_bf16 a[148:151], v[244:247], v[212:215], a[148:151]\n"
    "v_exp_f32 v195, v195\n"
    "v_mfma_f32_16x16x32_bf16 a[144:147], v[244:247], v[208:211], a[144:147]\n"
    "s_waitcnt lgkmcnt(5)\n"
    "ds_read_b64_tr_b16 a[244:245], v5 offset:16384\n"
    "v_add_f32 v22, v22, v193\n"
    "v_mfma_f32_16x16x32_bf16 a[168:171], v[248:251], v[208:211], a[168:171]\n"
    "ds_read_b64_tr_b16 a[246:247], v5 offset:17408\n"
    "v_cvt_pk_bf16_f32 v192, v192, v193\n"
    "v_exp_f32 v196, v196\n"
    "v_mfma_f32_16x16x32_bf16 a[172:175], v[248:251], v[212:215], a[172:175]\n"
    "ds_read_b64_tr_b16 v[240:241], v0 offset:16384\n"
    "v_add_f32 v22, v22, v194\n"
    "v_mfma_f32_16x16x32_bf16 a[176:179], v[248:251], v[216:219], a[176:179]\n"
    "ds_read_b64_tr_b16 v[242:243], v0 offset:17408\n"
    "v_exp_f32 v197, v197\n"
    "v_mfma_f32_16x16x32_bf16 a[180:183], v[248:251], v[220:223], a[180:183]\n"
    "ds_read_b64_tr_b16 a[248:249], v6 offset:16384\n"
    "v_add_f32 v22, v22, v195\n"
    "v_mfma_f32_16x16x32_bf16 a[184:187], v[248:251], v[224:227], a[184:187]\n"
    "ds_read_b64_tr_b16 a[250:251], v6 offset:17408\n"
    "v_cvt_pk_bf16_f32 v193, v194, v195\n"
    "v_exp_f32 v198, v198\n"
    "v_mfma_f32_16x16x32_bf16 a[188:191], v[248:251], v[228:231], a[188:191]\n"
    "v_add_f32 v22, v22, v196\n"
    "ds_read_b64_tr_b16 a[252:253], v7 offset:16384\n"
    "v_mfma_f32_16x16x32_bf16 a[212:215], v[232:235], v[228:231], a[212:215]\n"
    "v_exp_f32 v199, v199\n"
    "v_mfma_f32_16x16x32_bf16 a[208:211], v[232:235], v[224:227], a[208:211]\n"
    "ds_read_b64_tr_b16 a[254:255], v7 offset:17408\n"
    "v_add_f32 v22, v22, v197\n"
    "v_cvt_pk_bf16_f32 v194, v196, v197\n"
    "v_mfma_f32_16x16x32_bf16 a[204:207], v[232:235], v[220:223], a[204:207]\n"
    "ds_read_b64_tr_b16 v[244:245], v1 offset:16384\n"
    "v_add_f32 v22, v22, v198\n"
    "v_add_f32 v22, v22, v199\n"
    "v_mfma_f32_16x16x32_bf16 a[200:203], v[232:235], v[216:219], a[200:203]\n"
    "ds_read_b64_tr_b16 v[246:247], v1 offset:17408\n"
    "v_cvt_pk_bf16_f32 v195, v198, v199\n"
    "v_mfma_f32_16x16x32_bf16 a[196:199], v[232:235], v[212:215], a[196:199]\n"
    "ds_read_b64_tr_b16 v[248:249], v2 offset:16384\n"
    "ds_read_b64_tr_b16 v[250:251], v2 offset:17408\n"
    "v_mov_b32 v29, v22\n"
    "s_nop 1\n"
    "v_permlane32_swap_b32 v22, v29\n"
    "v_mfma_f32_16x16x32_bf16 a[192:195], v[232:235], v[208:211], a[192:195]\n"
    "s_waitcnt lgkmcnt(6)\n"
    "v_add_f32 v22, v22, v29\n"
    "v_mfma_f32_16x16x32_bf16 a[216:219], v[236:239], v[208:211], a[216:219]\n"
    "v_mov_b32 v29, v22\n"
    "s_nop 1\n"
    "v_permlane16_swap_b32 v22, v29\n"
    "v_mfma_f32_16x16x32_bf16 a[220:223], v[236:239], v[212:215], a[220:223]\n"
    "v_add_f32 v22, v22, v29\n"
    "v_rcp_f32 v28, v22\n"
    "v_mfma_f32_16x16x32_bf16 a[224:227], v[236:239], v[216:219], a[224:227]\n"
    "v_max3_f32 v37, v200, v201, v202\n"
    "v_max3_f32 v36, v203, v204, v205\n"
    "v_mfma_f32_16x16x32_bf16 a[228:231], v[236:239], v[220:223], a[228:231]\n"
    "v_max3_f32 v37, v37, v206, v207\n"
    "v_max_f32 v37, v37, v36\n"
    "v_mfma_f32_16x16x32_bf16 a[232:235], v[236:239], v[224:227], a[232:235]\n"
    "v_cmp_nge_f32 vcc, s77, v37\n"
    "s_cbranch_vccnz L_stub212c_%=\n"
    "L_back212c_%=:\n"
    "v_mfma_f32_16x16x32_bf16 a[236:239], v[236:239], v[228:231], a[236:239]\n"
    "v_exp_f32 v200, v200\n"
    "s_waitcnt lgkmcnt(0)\n"
    "s_barrier\n"
    "v_mfma_f32_16x16x32_bf16 v[88:91], v[240:243], v[184:187], v[88:91]\n"
    "v_exp_f32 v201, v201\n"
    "v_mfma_f32_16x16x32_bf16 v[100:103], v[244:247], v[184:187], v[100:103]\n"
    "s_add_u32 m0, s64, 49152\n"
    "s_add_u32 s71, s80, 256\n"
    "buffer_load_dwordx4 v18, s[56:59], s71 offen offset:0  lds\n"
    "v_mfma_f32_16x16x32_bf16 v[112:115], v[248:251], v[184:187], v[112:115]\n"
    "v_exp_f32 v202, v202\n"
    "v_mfma_f32_16x16x32_bf16 v[124:127], v[252:255], v[184:187], v[124:127]\n"
    "v_add_f32 v23, v23, v200\n"
    "v_mfma_f32_16x16x32_bf16 v[136:139], a[240:243], v[184:187], v[136:139]\n"
    "v_exp_f32 v203, v203\n"
    "v_mfma_f32_16x16x32_bf16 v[148:151], a[244:247], v[184:187], v[148:151]\n"
    "s_add_u32 s71, s80, 9472\n"
    "buffer_load_dwordx4 v18, s[56:59], s71 offen offset:1024  lds\n"
    "v_add_f32 v23, v23, v201\n"
    "v_mfma_f32_16x16x32_bf16 v[160:163], a[248:251], v[184:187], v[160:163]\n"
    "v_cvt_pk_bf16_f32 v200, v200, v201\n"
    "v_exp_f32 v204, v204\n"
    "v_mfma_f32_16x16x32_bf16 v[172:175], a[252:255], v[184:187], v[172:175]\n"
    "s_add_u32 s71, s80, 18688\n"
    "buffer_load_dwordx4 v18, s[56:59], s71 offen offset:2048  lds\n"
    "v_add_f32 v23, v23, v202\n"
    "v_mfma_f32_16x16x32_bf16 v[176:179], a[252:255], v[192:195], v[176:179]\n"
    "v_exp_f32 v205, v205\n"
    "v_mfma_f32_16x16x32_bf16 v[164:167], a[248:251], v[192:195], v[164:167]\n"
    "v_add_f32 v23, v23, v203\n"
    "v_cvt_pk_bf16_f32 v201, v202, v203\n"
    "v_mfma_f32_16x16x32_bf16 v[152:155], a[244:247], v[192:195], v[152:155]\n"
    "v_exp_f32 v206, v206\n"
    "v_mfma_f32_16x16x32_bf16 v[140:143], a[240:243], v[192:195], v[140:143]\n"
    "s_add_u32 s71, s80, 27904\n"
    "buffer_load_dwordx4 v18, s[56:59], s71 offen offset:3072  lds\n"
    "v_add_f32 v23, v23, v204\n"
    "v_mfma_f32_16x16x32_bf16 v[128:131], v[252:255], v[192:195], v[128:131]\n"
    "v_exp_f32 v207, v207\n"
    "v_mfma_f32_16x16x32_bf16 v[116:119], v[248:251], v[192:195], v[116:119]\n"
    "v_add_f32 v23, v23, v205\n"
    "v_cvt_pk_bf16_f32 v202, v204, v205\n"
    "v_add_f32 v23, v23, v206\n"
    "v_add_f32 v23, v23, v207\n"
    "v_cvt_pk_bf16_f32 v203, v206, v207\n"
    "v_mfma_f32_16x16x32_bf16 v[104:107], v[244:247], v[192:195], v[104:107]\n"
    "v_mov_b32 v33, v23\n"
    "s_nop 1\n"
    "v_permlane32_swap_b32 v23, v33\n"
    "v_mfma_f32_16x16x32_bf16 v[92:95], v[240:243], v[192:195], v[92:95]\n"
    "v_add_f32 v23, v23, v33\n"
    "v_mfma_f32_16x16x32_bf16 v[96:99], v[240:243], v[200:203], v[96:99]\n"
    "v_mov_b32 v33, v23\n"
    "s_nop 1\n"
    "v_permlane16_swap_b32 v23, v33\n"
    "v_add_f32 v23, v23, v33\n"
    "v_mfma_f32_16x16x32_bf16 v[108:111], v[244:247], v[200:203], v[108:111]\n"
    "s_add_u32 m0, s64, 114688\n"
    "s_add_u32 s71, s80, 164096\n"
    "buffer_load_dwordx4 v18, s[56:59], s71 offen offset:0  lds\n"
    "v_rcp_f32 v32, v23\n"
    "v_mul_f32 v88, v24, v88\n"
    "v_mul_f32 v89, v24, v89\n"
    "v_mfma_f32_16x16x32_bf16 v[120:123], v[248:251], v[200:203], v[120:123]\n"
    "v_mul_f32 v90, v24, v90\n"
    "v_mul_f32 v91, v24, v91\n"
    "v_mul_f32 v100, v24, v100\n"
    "v_mfma_f32_16x16x32_bf16 v[132:135], v[252:255], v[200:203], v[132:135]\n"
    "v_mul_f32 v101, v24, v101\n"
    "v_mul_f32 v102, v24, v102\n"
    "v_mul_f32 v103, v24, v103\n"
    "v_cvt_pk_bf16_f32 v184, v88, v89\n"
    "v_mfma_f32_16x16x32_bf16 v[144:147], a[240:243], v[200:203], v[144:147]\n"
    "v_cvt_pk_bf16_f32 v185, v90, v91\n"
    "v_cvt_pk_bf16_f32 v186, v100, v101\n"
    "v_cvt_pk_bf16_f32 v187, v102, v103\n"
    "v_mfma_f32_16x16x32_bf16 v[156:159], a[244:247], v[200:203], v[156:159]\n"
    "s_add_u32 s71, s80, 173312\n"
    "buffer_load_dwordx4 v18, s[56:59], s71 offen offset:1024  lds\n"
    "ds_write_b128 v20, v[184:187] offset:0\n"
    "v_mul_f32 v112, v24, v112\n"
    "v_mul_f32 v113, v24, v113\n"
    "v_mul_f32 v114, v24, v114\n"
    "v_mfma_f32_16x16x32_bf16 v[168:171], a[248:251], v[200:203], v[168:171]\n"
    "v_mul_f32 v115, v24, v115\n"
    "v_mul_f32 v124, v24, v124\n"
    "v_mul_f32 v125, v24, v125\n"
    "v_mfma_f32_16x16x32_bf16 v[180:183], a[252:255], v[200:203], v[180:183]\n"
    "v_mul_f32 v126, v24, v126\n"
    "v_mul_f32 v127, v24, v127\n"
    "v_cvt_pk_bf16_f32 v188, v112, v113\n"
    "v_cvt_pk_bf16_f32 v189, v114, v115\n"
    "v_cvt_pk_bf16_f32 v190, v124, v125\n"
    "v_cvt_pk_bf16_f32 v191, v126, v127\n"
    "ds_write_b128 v20, v[188:191] offset:6144\n"
    "s_nop 2\n"
    "v_pk_mul_f32 v[136:137], v[136:137], v[24:25] op_sel_hi:[1,0]\n"
    "v_pk_mul_f32 v[138:139], v[138:139], v[24:25] op_sel_hi:[1,0]\n"
    "v_pk_mul_f32 v[148:149], v[148:149], v[24:25] op_sel_hi:[1,0]\n"
    "v_pk_mul_f32 v[150:151], v[150:151], v[24:25] op_sel_hi:[1,0]\n"
    "v_cvt_pk_bf16_f32 v196, v136, v137\n"
    "v_cvt_pk_bf16_f32 v197, v138, v139\n"
    "v_cvt_pk_bf16_f32 v198, v148, v149\n"
    "v_cvt_pk_bf16_f32 v199, v150, v151\n"
    "ds_write_b128 v20, v[196:199] offset:12288\n"
    "v_pk_mul_f32 v[160:161], v[160:161], v[24:25] op_sel_hi:[1,0]\n"
    "v_pk_mul_f32 v[162:163], v[162:163], v[24:25] op_sel_hi:[1,0]\n"
    "v_pk_mul_f32 v[172:173], v[172:173], v[24:25] op_sel_hi:[1,0]\n"
    "v_pk_mul_f32 v[174:175], v[174:175], v[24:25] op_sel_hi:[1,0]\n"
    "v_cvt_pk_bf16_f32 v204, v160, v161\n"
    "v_cvt_pk_bf16_f32 v205, v162, v163\n"
    "v_cvt_pk_bf16_f32 v206, v172, v173\n"
    "v_cvt_pk_bf16_f32 v207, v174, v175\n"
    "ds_write_b128 v20, v[204:207] offset:18432\n"
    "v_pk_mul_f32 v[92:93], v[92:93], v[28:29] op_sel_hi:[1,0]\n"
    "v_pk_mul_f32 v[94:95], v[94:95], v[28:29] op_sel_hi:[1,0]\n"
    "v_pk_mul_f32 v[104:105], v[104:105], v[28:29] op_sel_hi:[1,0]\n"
    "v_pk_mul_f32 v[106:107], v[106:107], v[28:29] op_sel_hi:[1,0]\n"
    "v_cvt_pk_bf16_f32 v184, v92, v93\n"
    "v_cvt_pk_bf16_f32 v185, v94, v95\n"
    "v_cvt_pk_bf16_f32 v186, v104, v105\n"
    "v_cvt_pk_bf16_f32 v187, v106, v107\n"
    "ds_write_b128 v20, v[184:187] offset:1024\n"
    "v_pk_mul_f32 v[116:117], v[116:117], v[28:29] op_sel_hi:[1,0]\n"
    "v_pk_mul_f32 v[118:119], v[118:119], v[28:29] op_sel_hi:[1,0]\n"
    "v_pk_mul_f32 v[128:129], v[128:129], v[28:29] op_sel_hi:[1,0]\n"
    "v_pk_mul_f32 v[130:131], v[130:131], v[28:29] op_sel_hi:[1,0]\n"
    "v_cvt_pk_bf16_f32 v188, v116, v117\n"
    "v_cvt_pk_bf16_f32 v189, v118, v119\n"
    "v_cvt_pk_bf16_f32 v190, v128, v129\n"
    "v_cvt_pk_bf16_f32 v191, v130, v131\n"
    "ds_write_b128 v20, v[188:191] offset:7168\n"
    "v_pk_mul_f32 v[140:141], v[140:141], v[28:29] op_sel_hi:[1,0]\n"
    "v_pk_mul_f32 v[142:143], v[142:143], v[28:29] op_sel_hi:[1,0]\n"
    "v_pk_mul_f32 v[152:153], v[152:153], v[28:29] op_sel_hi:[1,0]\n"
    "v_pk_mul_f32 v[154:155], v[154:155], v[28:29] op_sel_hi:[1,0]\n"
    "v_cvt_pk_bf16_f32 v196, v140, v141\n"
    "v_cvt_pk_bf16_f32 v197, v142, v143\n"
    "v_cvt_pk_bf16_f32 v198, v152, v153\n"
    "v_cvt_pk_bf16_f32 v199, v154, v155\n"
    "ds_write_b128 v20, v[196:199] offset:13312\n"
    "v_pk_mul_f32 v[164:165], v[164:165], v[28:29] op_sel_hi:[1,0]\n"
    "v_pk_mul_f32 v[166:167], v[166:167], v[28:29] op_sel_hi:[1,0]\n"
    "v_pk_mul_f32 v[176:177], v[176:177], v[28:29] op_sel_hi:[1,0]\n"
    "v_pk_mul_f32 v[178:179], v[178:179], v[28:29] op_sel_hi:[1,0]\n"
    "v_cvt_pk_bf16_f32 v204, v164, v165\n"
    "v_cvt_pk_bf16_f32 v205, v166, v167\n"
    "v_cvt_pk_bf16_f32 v206, v176, v177\n"
    "v_cvt_pk_bf16_f32 v207, v178, v179\n"
    "ds_write_b128 v20, v[204:207] offset:19456\n"
    "v_pk_mul_f32 v[96:97], v[96:97], v[32:33] op_sel_hi:[1,0]\n"
    "v_pk_mul_f32 v[98:99], v[98:99], v[32:33] op_sel_hi:[1,0]\n"
    "v_pk_mul_f32 v[108:109], v[108:109], v[32:33] op_sel_hi:[1,0]\n"
    "v_pk_mul_f32 v[110:111], v[110:111], v[32:33] op_sel_hi:[1,0]\n"
    "v_cvt_pk_bf16_f32 v184, v96, v97\n"
    "v_cvt_pk_bf16_f32 v185, v98, v99\n"
    "v_cvt_pk_bf16_f32 v186, v108, v109\n"
    "v_cvt_pk_bf16_f32 v187, v110, v111\n"
    "ds_write_b128 v20, v[184:187] offset:2048\n"
    "v_pk_mul_f32 v[120:121], v[120:121], v[32:33] op_sel_hi:[1,0]\n"
    "v_pk_mul_f32 v[122:123], v[122:123], v[32:33] op_sel_hi:[1,0]\n"
    "v_pk_mul_f32 v[132:133], v[132:133], v[32:33] op_sel_hi:[1,0]\n"
    "v_pk_mul_f32 v[134:135], v[134:135], v[32:33] op_sel_hi:[1,0]\n"
    "v_cvt_pk_bf16_f32 v188, v120, v121\n"
    "v_cvt_pk_bf16_f32 v189, v122, v123\n"
    "v_cvt_pk_bf16_f32 v190, v132, v133\n"
    "v_cvt_pk_bf16_f32 v191, v134, v135\n"
    "ds_write_b128 v20, v[188:191] offset:8192\n"
    "v_pk_mul_f32 v[144:145], v[144:145], v[32:33] op_sel_hi:[1,0]\n"
    "v_pk_mul_f32 v[146:147], v[146:147], v[32:33] op_sel_hi:[1,0]\n"
    "v_pk_mul_f32 v[156:157], v[156:157], v[32:33] op_sel_hi:[1,0]\n"
    "v_pk_mul_f32 v[158:159], v[158:159], v[32:33] op_sel_hi:[1,0]\n"
    "v_cvt_pk_bf16_f32 v196, v144, v145\n"
    "v_cvt_pk_bf16_f32 v197, v146, v147\n"
    "v_cvt_pk_bf16_f32 v198, v156, v157\n"
    "v_cvt_pk_bf16_f32 v199, v158, v159\n"
    "ds_write_b128 v20, v[196:199] offset:14336\n"
    "v_pk_mul_f32 v[168:169], v[168:169], v[32:33] op_sel_hi:[1,0]\n"
    "v_pk_mul_f32 v[170:171], v[170:171], v[32:33] op_sel_hi:[1,0]\n"
    "v_pk_mul_f32 v[180:181], v[180:181], v[32:33] op_sel_hi:[1,0]\n"
    "v_pk_mul_f32 v[182:183], v[182:183], v[32:33] op_sel_hi:[1,0]\n"
    "v_cvt_pk_bf16_f32 v204, v168, v169\n"
    "v_cvt_pk_bf16_f32 v205, v170, v171\n"
    "v_cvt_pk_bf16_f32 v206, v180, v181\n"
    "v_cvt_pk_bf16_f32 v207, v182, v183\n"
    "ds_write_b128 v20, v[204:207] offset:20480\n"
    "v_subrev_u32 v17, s86, v20\n"
    "v_add_u32 v0, -114688, v16\n"
    "s_waitcnt vmcnt(18) lgkmcnt(0)\n"
    "s_barrier\n"
    "ds_read_b64_tr_b16 a[240:241], v0 offset:0\n"
    "ds_read_b64_tr_b16 a[242:243], v0 offset:2048\n"
    "ds_read_b128 v[40:43], v17 offset:0\n"
    "ds_read_b128 v[44:47], v17 offset:1024\n"
    "ds_read_b128 v[48:51], v17 offset:2048\n"
    "ds_read_b128 v[52:55], v17 offset:3072\n"
    "ds_read_b128 v[56:59], v17 offset:4096\n"
    "ds_read_b128 v[60:63], v17 offset:5120\n"
    "ds_read_b64_tr_b16 a[244:245], v0 offset:4096\n"
    "ds_read_b64_tr_b16 a[246:247], v0 offset:6144\n"
    "ds_read_b128 v[84:87], v17 offset:11264\n"
    "ds_read_b128 v[80:83], v17 offset:10240\n"
    "ds_read_b128 v[76:79], v17 offset:9216\n"
    "ds_read_b128 v[72:75], v17 offset:8192\n"
    "ds_read_b128 v[68:71], v17 offset:7168\n"
    "ds_read_b128 v[64:67], v17 offset:6144\n"
    "v_add_u32 v1, 0, v18\n"
    "v_add_u32 v2, 9216, v18\n"
    "v_add_u32 v3, 18432, v18\n"
    "v_add_u32 v4, 27648, v18\n"
    "v_add_u32 v11, -49152, v16\n"
    "s_waitcnt lgkmcnt(13)\n"
    "v_mfma_f32_16x16x32_bf16 a[0:3], a[240:243], v[40:43], a[0:3]\n"
    "ds_read_b64_tr_b16 a[248:249], v0 offset:8192\n"
    "ds_read_b64_tr_b16 a[250:251], v0 offset:10240\n"
    "s_waitcnt lgkmcnt(14)\n"
    "v_mfma_f32_16x16x32_bf16 a[4:7], a[240:243], v[44:47], a[4:7]\n"
    "ds_read_b128 v[88:91], v17 offset:12288\n"
    "s_waitcnt lgkmcnt(14)\n"
    "v_mfma_f32_16x16x32_bf16 a[8:11], a[240:243], v[48:51], a[8:11]\n"
    "s_add_u32 s71, s80, 182528\n"
    "buffer_load_dwordx4 v18, s[56:59], s71 offen offset:2048  lds\n"
    "ds_read_b128 v[92:95], v17 offset:13312\n"
    "s_waitcnt lgkmcnt(14)\n"
    "v_mfma_f32_16x16x32_bf16 a[12:15], a[240:243], v[52:55], a[12:15]\n"
    "s_waitcnt lgkmcnt(13)\n"
    "ds_read_b128 v[96:99], v17 offset:14336\n"
    "ds_read_b128 v[100:103], v17 offset:15360\n"
    "v_mfma_f32_16x16x32_bf16 a[16:19], a[240:243], v[56:59], a[16:19]\n"
    "s_waitcnt lgkmcnt(14)\n"
    "ds_read_b128 v[104:107], v17 offset:16384\n"
    "v_mfma_f32_16x16x32_bf16 a[20:23], a[240:243], v[60:63], a[20:23]\n"
    "s_waitcnt lgkmcnt(12)\n"
    "ds_read_b128 v[108:111], v17 offset:17408\n"
    "v_mfma_f32_16x16x32_bf16 a[20:23], a[244:247], v[84:87], a[20:23]\n"
    "s_waitcnt lgkmcnt(8)\n"
    "ds_read_b64_tr_b16 a[252:253], v0 offset:12288\n"
    "ds_read_b64_tr_b16 a[254:255], v0 offset:14336\n"
    "v_mfma_f32_16x16x32_bf16 a[16:19], a[244:247], v[80:83], a[16:19]\n"
    "s_add_u32 s71, s80, 191744\n"
    "buffer_load_dwordx4 v18, s[56:59], s71 offen offset:3072  lds\n"
    "ds_read_b128 v[132:135], v17 offset:23552\n"
    "v_mfma_f32_16x16x32_bf16 a[12:15], a[244:247], v[76:79], a[12:15]\n"
    "ds_read_b128 v[128:131], v17 offset:22528\n"
    "v_mfma_f32_16x16x32_bf16 a[8:11], a[244:247], v[72:75], a[8:11]\n"
    "ds_read_b128 v[124:127], v17 offset:21504\n"
    "ds_read_b128 v[120:123], v17 offset:20480\n"
    "v_mfma_f32_16x16x32_bf16 a[4:7], a[244:247], v[68:71], a[4:7]\n"
    "ds_read_b128 v[116:119], v17 offset:19456\n"
    "v_mfma_f32_16x16x32_bf16 a[0:3], a[244:247], v[64:67], a[0:3]\n"
    "ds_read_b128 v[112:115], v17 offset:18432\n"
    "s_waitcnt vmcnt(16)\n"
    "s_barrier\n"
    "s_waitcnt lgkmcnt(9)\n"
    "v_mfma_f32_16x16x32_bf16 a[0:3], a[248:251], v[88:91], a[0:3]\n"
    "ds_read_b64_tr_b16 a[240:241], v0 offset:32768\n"
    "ds_read_b64_tr_b16 a[242:243], v0 offset:34816\n"
    "v_mfma_f32_16x16x32_bf16 a[4:7], a[248:251], v[92:95], a[4:7]\n"
    "s_add_u32 m0, s64, 131072\n"
    "s_add_u32 s71, s80, 384\n"
    "buffer_load_dwordx4 v18, s[56:59], s71 offen offset:0  lds\n"
    "ds_read_b128 v[136:139], v17 offset:24576\n"
    "v_mfma_f32_16x16x32_bf16 a[8:11], a[248:251], v[96:99], a[8:11]\n"
    "ds_read_b128 v[140:143], v17 offset:25600\n"
    "v_mfma_f32_16x16x32_bf16 a[12:15], a[248:251], v[100:103], a[12:15]\n"
    "ds_read_b128 v[144:147], v17 offset:26624\n"
    "ds_read_b128 v[148:151], v17 offset:27648\n"
    "v_mfma_f32_16x16x32_bf16 a[16:19], a[248:251], v[104:107], a[16:19]\n"
    "s_waitcnt lgkmcnt(8)\n"
    "ds_read_b128 v[152:155], v17 offset:28672\n"
    "v_mfma_f32_16x16x32_bf16 a[20:23], a[248:251], v[108:111], a[20:23]\n"
    "ds_read_b128 v[156:159], v17 offset:29696\n"
    "v_mfma_f32_16x16x32_bf16 a[20:23], a[252:255], v[132:135], a[20:23]\n"
    "ds_read_b64_tr_b16 a[244:245], v0 offset:36864\n"
    "ds_read_b64_tr_b16 a[246:247], v0 offset:38912\n"
    "v_mfma_f32_16x16x32_bf16 a[16:19], a[252:255], v[128:131], a[16:19]\n"
    "s_add_u32 s71, s80, 9600\n"
    "buffer_load_dwordx4 v18, s[56:59], s71 offen offset:1024  lds\n"
    "ds_read_b128 v[180:183], v17 offset:35840\n"
    "v_mfma_f32_16x16x32_bf16 a[12:15], a[252:255], v[124:127], a[12:15]\n"
    "ds_read_b128 v[176:179], v17 offset:34816\n"
    "v_mfma_f32_16x16x32_bf16 a[8:11], a[252:255], v[120:123], a[8:11]\n"
    "s_waitcnt lgkmcnt(8)\n"
    "ds_read_b128 v[172:175], v17 offset:33792\n"
    "ds_read_b128 v[168:171], v17 offset:32768\n"
    "v_mfma_f32_16x16x32_bf16 a[4:7], a[252:255], v[116:119], a[4:7]\n"
    "ds_read_b128 v[164:167], v17 offset:31744\n"
    "v_mfma_f32_16x16x32_bf16 a[0:3], a[252:255], v[112:115], a[0:3]\n"
    "s_add_u32 s71, s80, 18816\n"
    "buffer_load_dwordx4 v18, s[56:59], s71 offen offset:2048  lds\n"
    "ds_read_b128 v[160:163], v17 offset:30720\n"
    "v_mfma_f32_16x16x32_bf16 a[0:3], a[240:243], v[136:139], a[0:3]\n"
    "ds_read_b64_tr_b16 a[248:249], v0 offset:40960\n"
    "ds_read_b64_tr_b16 a[250:251], v0 offset:43008\n"
    "v_mfma_f32_16x16x32_bf16 a[4:7], a[240:243], v[140:143], a[4:7]\n"
    "s_waitcnt lgkmcnt(8)\n"
    "ds_read_b128 v[184:187], v17 offset:36864\n"
    "v_mfma_f32_16x16x32_bf16 a[8:11], a[240:243], v[144:147], a[8:11]\n"
    "ds_read_b128 v[188:191], v17 offset:37888\n"
    "v_mfma_f32_16x16x32_bf16 a[12:15], a[240:243], v[148:151], a[12:15]\n"
    "ds_read_b128 v[192:195], v17 offset:38912\n"
    "ds_read_b128 v[196:199], v17 offset:39936\n"
    "v_mfma_f32_16x16x32_bf16 a[16:19], a[240:243], v[152:155], a[16:19]\n"
    "s_add_u32 s71, s80, 28032\n"
    "buffer_load_dwordx4 v18, s[56:59], s71 offen offset:3072  lds\n"
    "ds_read_b128 v[200:203], v17 offset:40960\n"
    "v_mfma_f32_16x16x32_bf16 a[20:23], a[240:243], v[156:159], a[20:23]\n"
    "s_waitcnt lgkmcnt(8)\n"
    "ds_read_b128 v[204:207], v17 offset:41984\n"
    "v_mfma_f32_16x16x32_bf16 a[20:23], a[244:247], v[180:183], a[20:23]\n"
    "s_sub_u32 s72, s65, 65536\n"
    "ds_read_b64_tr_b16 a[252:253], v0 offset:45056\n"
    "ds_read_b64_tr_b16 a[254:255], v0 offset:47104\n"
    "v_mfma_f32_16x16x32_bf16 a[16:19], a[244:247], v[176:179], a[16:19]\n"
    "ds_read_b128 v[228:231], v17 offset:48128\n"
    "v_add_u32 v5, s72, v17\n"
    "s_mul_i32 s82, s36, 126976\n"
    "v_mfma_f32_16x16x32_bf16 a[12:15], a[244:247], v[172:175], a[12:15]\n"
    "ds_read_b128 v[224:227], v17 offset:47104\n"
    "v_mbcnt_lo_u32_b32 v7, -1, 0\n"
    "v_mfma_f32_16x16x32_bf16 a[8:11], a[244:247], v[168:171], a[8:11]\n"
    "ds_read_b128 v[220:223], v17 offset:46080\n"
    "ds_read_b128 v[216:219], v17 offset:45056\n"
    "v_mfma_f32_16x16x32_bf16 a[4:7], a[244:247], v[164:167], a[4:7]\n"
    "s_waitcnt lgkmcnt(8)\n"
    "ds_read_b128 v[212:215], v17 offset:44032\n"
    "v_mbcnt_hi_u32_b32 v7, -1, v7\n"
    "v_mfma_f32_16x16x32_bf16 a[0:3], a[244:247], v[160:163], a[0:3]\n"
    "ds_read_b128 v[208:211], v17 offset:43008\n"
    "v_mul_u32_u24 v7, 513, v7\n"
    "s_waitcnt vmcnt(16)\n"
    "s_barrier\n"
    "v_mfma_f32_16x16x32_bf16 a[0:3], a[248:251], v[184:187], a[0:3]\n"
    "s_add_u32 m0, s64, 0\n"
    "ds_read_b64_tr_b16 a[240:241], v16 offset:32768\n"
    "v_and_b32 v7, 0x7007, v7\n"
    "v_mfma_f32_16x16x32_bf16 a[4:7], a[248:251], v[188:191], a[4:7]\n"
    "s_add_u32 s71, s80, 164224\n"
    "ds_read_b64_tr_b16 a[242:243], v16 offset:34816\n"
    "s_lshl_b32 s38, s36, 2\n"
    "v_mfma_f32_16x16x32_bf16 a[8:11], a[248:251], v[192:195], a[8:11]\n"
    "v_lshl_add_u32 v9, v7, 12, s38\n"
    "buffer_load_dwordx4 v1, s[56:59], s71 offen offset:0  lds\n"
    "v_mfma_f32_16x16x32_bf16 a[12:15], a[248:251], v[196:199], a[12:15]\n"
    "s_waitcnt lgkmcnt(6)\n"
    "v_lshlrev_b32 v7, 2, v7\n"
    "v_mbcnt_lo_u32_b32 v6, -1, 0\n"
    "v_mfma_f32_16x16x32_bf16 a[16:19], a[248:251], v[200:203], a[16:19]\n"
    "v_mbcnt_hi_u32_b32 v6, -1, v6\n"
    "v_lshrrev_b32 v8, 5, v6\n"
    "v_mfma_f32_16x16x32_bf16 a[20:23], a[248:251], v[204:207], a[20:23]\n"
    "v_and_b32 v6, 31, v6\n"
    "v_mfma_f32_16x16x32_bf16 a[20:23], a[252:255], v[228:231], a[20:23]\n"
    "ds_read_b64_tr_b16 a[244:245], v16 offset:36864\n"
    "v_mul_u32_u24 v6, 2560, v6\n"
    "v_mfma_f32_16x16x32_bf16 a[16:19], a[252:255], v[224:227], a[16:19]\n"
    "s_waitcnt lgkmcnt(2)\n"
    "ds_read_b64_tr_b16 a[246:247], v16 offset:38912\n"
    "v_lshl_add_u32 v6, v8, 4, v6\n"
    "v_mfma_f32_16x16x32_bf16 a[12:15], a[252:255], v[220:223], a[12:15]\n"
    "buffer_load_dwordx4 v2, s[56:59], s71 offen offset:1024  lds\n"
    "v_mfma_f32_16x16x32_bf16 a[8:11], a[252:255], v[216:219], a[8:11]\n"
    "s_lshr_b32 s72, s65, 5\n"
    "v_add_u32 v6, s72, v6\n"
    "v_mfma_f32_16x16x32_bf16 a[4:7], a[252:255], v[212:215], a[4:7]\n"
    "s_mov_b64 s[44:45], s[60:61]\n"
    "v_mfma_f32_16x16x32_bf16 a[0:3], a[252:255], v[208:211], a[0:3]\n"
    "s_waitcnt lgkmcnt(2)\n"
    "s_mov_b32 s52, 0\n"
    "s_mov_b64 s[54:55], 0xff\n"
    "s_bitset0_b32 s54, s36\n"
    "v_mfma_f32_16x16x32_bf16 a[24:27], a[240:243], v[40:43], a[24:27]\n"
    "s_mov_b64 s[40:41], s[26:27]\n"
    "ds_read_b64_tr_b16 a[248:249], v16 offset:40960\n"
    "s_mov_b32 s42, 244480\n"
    "v_mfma_f32_16x16x32_bf16 a[28:31], a[240:243], v[44:47], a[28:31]\n"
    "s_mov_b32 s74, 1015808\n"
    "ds_read_b64_tr_b16 a[250:251], v16 offset:43008\n"
    "v_mfma_f32_16x16x32_bf16 a[32:35], a[240:243], v[48:51], a[32:35]\n"
    "buffer_load_dwordx4 v3, s[56:59], s71 offen offset:2048  lds\n"
    "v_mfma_f32_16x16x32_bf16 a[36:39], a[240:243], v[52:55], a[36:39]\n"
    "v_mfma_f32_16x16x32_bf16 a[40:43], a[240:243], v[56:59], a[40:43]\n"
    "v_mfma_f32_16x16x32_bf16 a[44:47], a[240:243], v[60:63], a[44:47]\n"
    "s_waitcnt lgkmcnt(2)\n"
    "v_mfma_f32_16x16x32_bf16 a[44:47], a[244:247], v[84:87], a[44:47]\n"
    "ds_read_b64_tr_b16 a[252:253], v16 offset:45056\n"
    "v_accvgpr_read_b32 v232, a0\n"
    "v_mfma_f32_16x16x32_bf16 a[40:43], a[244:247], v[80:83], a[40:43]\n"
    "ds_read_b64_tr_b16 a[254:255], v16 offset:47104\n"
    "v_accvgpr_read_b32 v233, a1\n"
    "v_mfma_f32_16x16x32_bf16 a[36:39], a[244:247], v[76:79], a[36:39]\n"
    "v_accvgpr_read_b32 v234, a2\n"
    "v_accvgpr_read_b32 v235, a3\n"
    "v_mfma_f32_16x16x32_bf16 a[32:35], a[244:247], v[72:75], a[32:35]\n"
    "buffer_load_dwordx4 v4, s[56:59], s71 offen offset:3072  lds\n"
    "v_cvt_pk_bf16_f32 v240, v232, v233\n"
    "v_mfma_f32_16x16x32_bf16 a[28:31], a[244:247], v[68:71], a[28:31]\n"
    "v_cvt_pk_bf16_f32 v241, v234, v235\n"
    "v_accvgpr_read_b32 v236, a4\n"
    "v_mfma_f32_16x16x32_bf16 a[24:27], a[244:247], v[64:67], a[24:27]\n"
    "v_accvgpr_read_b32 v237, a5\n"
    "v_accvgpr_read_b32 v238, a6\n"
    "s_waitcnt vmcnt(16)\n"
    "s_barrier\n"
    "s_waitcnt lgkmcnt(2)\n"
    "v_mfma_f32_16x16x32_bf16 a[24:27], a[248:251], v[88:91], a[24:27]\n"
    "s_add_u32 m0, s64, 32768\n"
    "ds_read_b64_tr_b16 a[240:241], v0 offset:16384\n"
    "v_accvgpr_read_b32 v239, a7\n"
    "v_mfma_f32_16x16x32_bf16 a[28:31], a[248:251], v[92:95], a[28:31]\n"
    "s_cmp_eq_u32 s36, 0\n"
    "s_cselect_b32 s62, 0, s74\n"
    "ds_read_b64_tr_b16 a[242:243], v0 offset:18432\n"
    "v_cvt_pk_bf16_f32 v242, v236, v237\n"
    "v_mfma_f32_16x16x32_bf16 a[32:35], a[248:251], v[96:99], a[32:35]\n"
    "s_add_u32 s71, s80, 512\n"
    "v_cvt_pk_bf16_f32 v243, v238, v239\n"
    "buffer_store_dwordx4 v[240:243], v5, s[60:63], s82 offen sc1\n"
    "s_getpc_b64 s[88:89]\n"
    "L_jt_%=:\n"
    "s_mul_i32 s72, s36, 260\n"
    "v_mfma_f32_16x16x32_bf16 a[36:39], a[248:251], v[100:103], a[36:39]\n"
    "s_add_u32 s82, s82, 4096\n"
    "v_accvgpr_read_b32 v232, a8\n"
    "buffer_load_dwordx4 v1, s[56:59], s71 offen offset:0  lds\n"
    "v_mfma_f32_16x16x32_bf16 a[40:43], a[248:251], v[104:107], a[40:43]\n"
    "v_accvgpr_read_b32 v233, a9\n"
    "v_accvgpr_read_b32 v234, a10\n"
    "v_mfma_f32_16x16x32_bf16 a[44:47], a[248:251], v[108:111], a[44:47]\n"
    "s_waitcnt lgkmcnt(2)\n"
    "s_add_u32 s72, s72, L_own0_%=-L_jt_%=\n"
    "v_accvgpr_read_b32 v235, a11\n"
    "v_cvt_pk_bf16_f32 v244, v232, v233\n"
    "v_mfma_f32_16x16x32_bf16 a[44:47], a[252:255], v[132:135], a[44:47]\n"
    "ds_read_b64_tr_b16 a[244:245], v0 offset:20480\n"
    "v_cvt_pk_bf16_f32 v245, v234, v235\n"
    "v_mfma_f32_16x16x32_bf16 a[40:43], a[252:255], v[128:131], a[40:43]\n"
    "ds_read_b64_tr_b16 a[246:247], v0 offset:22528\n"
    "v_accvgpr_read_b32 v236, a12\n"
    "v_accvgpr_read_b32 v237, a13\n"
    "v_mfma_f32_16x16x32_bf16 a[36:39], a[252:255], v[124:127], a[36:39]\n"
    "v_accvgpr_read_b32 v238, a14\n"
    "v_accvgpr_read_b32 v239, a15\n"
    "v_mfma_f32_16x16x32_bf16 a[32:35], a[252:255], v[120:123], a[32:35]\n"
    "s_cmp_eq_u32 s36, 1\n"
    "s_cselect_b32 s62, 0, s74\n"
    "v_cvt_pk_bf16_f32 v246, v236, v237\n"
    "v_mfma_f32_16x16x32_bf16 a[28:31], a[252:255], v[116:119], a[28:31]\n"
    "buffer_load_dwordx4 v2, s[56:59], s71 offen offset:1024  lds\n"
    "v_cvt_pk_bf16_f32 v247, v238, v239\n"
    "buffer_store_dwordx4 v[244:247], v5, s[60:63], s82 offen sc1\n"
    "v_mfma_f32_16x16x32_bf16 a[24:27], a[252:255], v[112:115], a[24:27]\n"
    "s_waitcnt lgkmcnt(2)\n"
    "s_add_u32 s82, s82, 4096\n"
    "v_accvgpr_read_b32 v232, a16\n"
    "v_accvgpr_read_b32 v233, a17\n"
    "v_mfma_f32_16x16x32_bf16 a[24:27], a[240:243], v[136:139], a[24:27]\n"
    "ds_read_b64_tr_b16 a[248:249], v0 offset:24576\n"
    "v_accvgpr_read_b32 v234, a18\n"
    "v_mfma_f32_16x16x32_bf16 a[28:31], a[240:243], v[140:143], a[28:31]\n"
    "s_add_u32 s88, s88, s72\n"
    "s_addc_u32 s89, s89, 0\n"
    "ds_read_b64_tr_b16 a[250:251], v0 offset:26624\n"
    "v_accvgpr_read_b32 v235, a19\n"
    "v_cvt_pk_bf16_f32 v248, v232, v233\n"
    "v_mfma_f32_16x16x32_bf16 a[32:35], a[240:243], v[144:147], a[32:35]\n"
    "v_cvt_pk_bf16_f32 v249, v234, v235\n"
    "v_accvgpr_read_b32 v236, a20\n"
    "v_mfma_f32_16x16x32_bf16 a[36:39], a[240:243], v[148:151], a[36:39]\n"
    "v_accvgpr_read_b32 v237, a21\n"
    "v_accvgpr_read_b32 v238, a22\n"
    "v_mfma_f32_16x16x32_bf16 a[40:43], a[240:243], v[152:155], a[40:43]\n"
    "buffer_load_dwordx4 v3, s[56:59], s71 offen offset:2048  lds\n"
    "v_accvgpr_read_b32 v239, a23\n"
    "s_cmp_eq_u32 s36, 2\n"
    "s_cselect_b32 s62, 0, s74\n"
    "v_cvt_pk_bf16_f32 v250, v236, v237\n"
    "v_cvt_pk_bf16_f32 v251, v238, v239\n"
    "buffer_store_dwordx4 v[248:251], v5, s[60:63], s82 offen sc1\n"
    "s_add_u32 s82, s82, 4096\n"
    "v_mfma_f32_16x16x32_bf16 a[44:47], a[240:243], v[156:159], a[44:47]\n"
    "s_waitcnt lgkmcnt(2)\n"
    "s_add_u32 s48, s36, 0\n"
    "v_mfma_f32_16x16x32_bf16 a[44:47], a[244:247], v[180:183], a[44:47]\n"
    "ds_read_b64_tr_b16 a[252:253], v0 offset:28672\n"
    "v_mfma_f32_16x16x32_bf16 a[40:43], a[244:247], v[176:179], a[40:43]\n"
    "ds_read_b64_tr_b16 a[254:255], v0 offset:30720\n"
    "v_mfma_f32_16x16x32_bf16 a[36:39], a[244:247], v[172:175], a[36:39]\n"
    "s_mul_i32 s70, s48, 43\n"
    "buffer_load_dwordx4 v4, s[56:59], s71 offen offset:3072  lds\n"
    "v_mfma_f32_16x16x32_bf16 a[32:35], a[244:247], v[168:171], a[32:35]\n"
    "v_mfma_f32_16x16x32_bf16 a[28:31], a[244:247], v[164:167], a[28:31]\n"
    "v_mfma_f32_16x16x32_bf16 a[24:27], a[244:247], v[160:163], a[24:27]\n"
    "s_waitcnt vmcnt(19)\n"
    "s_barrier\n"
    "s_waitcnt lgkmcnt(2)\n"
    "v_mfma_f32_16x16x32_bf16 a[24:27], a[248:251], v[184:187], a[24:27]\n"
    "s_add_u32 m0, s64, 65536\n"
    "ds_read_b64_tr_b16 a[240:241], v0 offset:49152\n"
    "s_lshr_b32 s70, s70, 7\n"
    "v_mfma_f32_16x16x32_bf16 a[28:31], a[248:251], v[188:191], a[28:31]\n"
    "s_add_u32 s71, s80, 164352\n"
    "ds_read_b64_tr_b16 a[242:243], v0 offset:51200\n"
    "v_mfma_f32_16x16x32_bf16 a[32:35], a[248:251], v[192:195], a[32:35]\n"
    "buffer_load_dwordx4 v1, s[56:59], s71 offen offset:0  lds\n"
    "v_mfma_f32_16x16x32_bf16 a[36:39], a[248:251], v[196:199], a[36:39]\n"
    "s_mul_i32 s76, s48, 81920\n"
    "v_mfma_f32_16x16x32_bf16 a[40:43], a[248:251], v[200:203], a[40:43]\n"
    "v_mfma_f32_16x16x32_bf16 a[44:47], a[248:251], v[204:207], a[44:47]\n"
    "s_waitcnt lgkmcnt(2)\n"
    "v_mfma_f32_16x16x32_bf16 a[44:47], a[252:255], v[228:231], a[44:47]\n"
    "ds_read_b64_tr_b16 a[244:245], v0 offset:53248\n"
    "v_mfma_f32_16x16x32_bf16 a[40:43], a[252:255], v[224:227], a[40:43]\n"
    "s_mul_i32 s75, s70, 245632\n"
    "ds_read_b64_tr_b16 a[246:247], v0 offset:55296\n"
    "v_mfma_f32_16x16x32_bf16 a[36:39], a[252:255], v[220:223], a[36:39]\n"
    "buffer_load_dwordx4 v2, s[56:59], s71 offen offset:1024  lds\n"
    "v_mfma_f32_16x16x32_bf16 a[32:35], a[252:255], v[216:219], a[32:35]\n"
    "v_mfma_f32_16x16x32_bf16 a[28:31], a[252:255], v[212:215], a[28:31]\n"
    "v_mfma_f32_16x16x32_bf16 a[24:27], a[252:255], v[208:211], a[24:27]\n"
    "s_waitcnt lgkmcnt(2)\n"
    "s_sub_u32 s76, s76, s75\n"
    "v_mfma_f32_16x16x32_bf16 a[48:51], a[240:243], v[40:43], a[48:51]\n"
    "ds_read_b64_tr_b16 a[248:249], v0 offset:57344\n"
    "v_mfma_f32_16x16x32_bf16 a[52:55], a[240:243], v[44:47], a[52:55]\n"
    "ds_read_b64_tr_b16 a[250:251], v0 offset:59392\n"
    "v_mfma_f32_16x16x32_bf16 a[56:59], a[240:243], v[48:51], a[56:59]\n"
    "buffer_load_dwordx4 v3, s[56:59], s71 offen offset:2048  lds\n"
    "v_mfma_f32_16x16x32_bf16 a[60:63], a[240:243], v[52:55], a[60:63]\n"
    "s_lshl_b32 s48, s48, 12\n"
    "v_mfma_f32_16x16x32_bf16 a[64:67], a[240:243], v[56:59], a[64:67]\n"
    "v_mfma_f32_16x16x32_bf16 a[68:71], a[240:243], v[60:63], a[68:71]\n"
    "s_waitcnt lgkmcnt(2)\n"
    "v_mfma_f32_16x16x32_bf16 a[68:71], a[244:247], v[84:87], a[68:71]\n"
    "s_add_u32 s49, s36, 8\n"
    "ds_read_b64_tr_b16 a[252:253], v0 offset:61440\n"
    "v_accvgpr_read_b32 v232, a24\n"
    "v_mfma_f32_16x16x32_bf16 a[64:67], a[244:247], v[80:83], a[64:67]\n"
    "ds_read_b64_tr_b16 a[254:255], v0 offset:63488\n"
    "v_accvgpr_read_b32 v233, a25\n"
    "v_mfma_f32_16x16x32_bf16 a[60:63], a[244:247], v[76:79], a[60:63]\n"
    "v_accvgpr_read_b32 v234, a26\n"
    "v_accvgpr_read_b32 v235, a27\n"
    "v_mfma_f32_16x16x32_bf16 a[56:59], a[244:247], v[72:75], a[56:59]\n"
    "buffer_load_dwordx4 v4, s[56:59], s71 offen offset:3072  lds\n"
    "v_cvt_pk_bf16_f32 v240, v232, v233\n"
    "v_mfma_f32_16x16x32_bf16 a[52:55], a[244:247], v[68:71], a[52:55]\n"
    "s_mul_i32 s73, s49, 43\n"
    "v_cvt_pk_bf16_f32 v241, v234, v235\n"
    "v_accvgpr_read_b32 v236, a28\n"
    "v_mfma_f32_16x16x32_bf16 a[48:51], a[244:247], v[64:67], a[48:51]\n"
    "v_accvgpr_read_b32 v237, a29\n"
    "v_accvgpr_read_b32 v238, a30\n"
    "s_waitcnt vmcnt(19)\n"
    "s_barrier\n"
    "s_waitcnt lgkmcnt(2)\n"
    "v_mfma_f32_16x16x32_bf16 a[48:51], a[248:251], v[88:91], a[48:51]\n"
    "s_add_u32 m0, s64, 81920\n"
    "ds_read_b64_tr_b16 a[240:241], v16 offset:0\n"
    "v_accvgpr_read_b32 v239, a31\n"
    "v_mfma_f32_16x16x32_bf16 a[52:55], a[248:251], v[92:95], a[52:55]\n"
    "s_cmp_eq_u32 s36, 3\n"
    "s_cselect_b32 s62, 0, s74\n"
    "ds_read_b64_tr_b16 a[242:243], v16 offset:2048\n"
    "v_cvt_pk_bf16_f32 v242, v236, v237\n"
    "v_mfma_f32_16x16x32_bf16 a[56:59], a[248:251], v[96:99], a[56:59]\n"
    "s_add_u32 s71, s80, 640\n"
    "v_cvt_pk_bf16_f32 v243, v238, v239\n"
    "buffer_store_dwordx4 v[240:243], v5, s[60:63], s82 offen sc1\n"
    "v_mfma_f32_16x16x32_bf16 a[60:63], a[248:251], v[100:103], a[60:63]\n"
    "s_add_u32 s82, s82, 4096\n"
    "v_accvgpr_read_b32 v232, a32\n"
    "buffer_load_dwordx4 v1, s[56:59], s71 offen offset:0  lds\n"
    "v_mfma_f32_16x16x32_bf16 a[64:67], a[248:251], v[104:107], a[64:67]\n"
    "s_lshr_b32 s73, s73, 7\n"
    "v_accvgpr_read_b32 v233, a33\n"
    "v_accvgpr_read_b32 v234, a34\n"
    "v_mfma_f32_16x16x32_bf16 a[68:71], a[248:251], v[108:111], a[68:71]\n"
    "s_waitcnt lgkmcnt(2)\n"
    "v_accvgpr_read_b32 v235, a35\n"
    "v_cvt_pk_bf16_f32 v244, v232, v233\n"
    "v_mfma_f32_16x16x32_bf16 a[68:71], a[252:255], v[132:135], a[68:71]\n"
    "s_mul_i32 s77, s49, 81920\n"
    "ds_read_b64_tr_b16 a[244:245], v16 offset:4096\n"
    "v_cvt_pk_bf16_f32 v245, v234, v235\n"
    "v_mfma_f32_16x16x32_bf16 a[64:67], a[252:255], v[128:131], a[64:67]\n"
    "ds_read_b64_tr_b16 a[246:247], v16 offset:6144\n"
    "v_accvgpr_read_b32 v236, a36\n"
    "v_accvgpr_read_b32 v237, a37\n"
    "v_mfma_f32_16x16x32_bf16 a[60:63], a[252:255], v[124:127], a[60:63]\n"
    "v_accvgpr_read_b32 v238, a38\n"
    "v_accvgpr_read_b32 v239, a39\n"
    "v_mfma_f32_16x16x32_bf16 a[56:59], a[252:255], v[120:123], a[56:59]\n"
    "s_cmp_eq_u32 s36, 4\n"
    "s_cselect_b32 s62, 0, s74\n"
    "v_cvt_pk_bf16_f32 v246, v236, v237\n"
    "v_mfma_f32_16x16x32_bf16 a[52:55], a[252:255], v[116:119], a[52:55]\n"
    "buffer_load_dwordx4 v2, s[56:59], s71 offen offset:1024  lds\n"
    "v_cvt_pk_bf16_f32 v247, v238, v239\n"
    "buffer_store_dwordx4 v[244:247], v5, s[60:63], s82 offen sc1\n"
    "v_mfma_f32_16x16x32_bf16 a[48:51], a[252:255], v[112:115], a[48:51]\n"
    "s_waitcnt lgkmcnt(2)\n"
    "s_add_u32 s82, s82, 4096\n"
    "v_accvgpr_read_b32 v232, a40\n"
    "v_accvgpr_read_b32 v233, a41\n"
    "v_mfma_f32_16x16x32_bf16 a[48:51], a[240:243], v[136:139], a[48:51]\n"
    "s_mul_i32 s75, s73, 245632\n"
    "ds_read_b64_tr_b16 a[248:249], v16 offset:8192\n"
    "v_accvgpr_read_b32 v234, a42\n"
    "v_mfma_f32_16x16x32_bf16 a[52:55], a[240:243], v[140:143], a[52:55]\n"
    "ds_read_b64_tr_b16 a[250:251], v16 offset:10240\n"
    "v_accvgpr_read_b32 v235, a43\n"
    "v_cvt_pk_bf16_f32 v248, v232, v233\n"
    "v_mfma_f32_16x16x32_bf16 a[56:59], a[240:243], v[144:147], a[56:59]\n"
    "s_sub_u32 s77, s77, s75\n"
    "v_cvt_pk_bf16_f32 v249, v234, v235\n"
    "v_accvgpr_read_b32 v236, a44\n"
    "v_mfma_f32_16x16x32_bf16 a[60:63], a[240:243], v[148:151], a[60:63]\n"
    "v_accvgpr_read_b32 v237, a45\n"
    "v_accvgpr_read_b32 v238, a46\n"
    "v_mfma_f32_16x16x32_bf16 a[64:67], a[240:243], v[152:155], a[64:67]\n"
    "buffer_load_dwordx4 v3, s[56:59], s71 offen offset:2048  lds\n"
    "v_accvgpr_read_b32 v239, a47\n"
    "s_cmp_eq_u32 s36, 5\n"
    "s_cselect_b32 s62, 0, s74\n"
    "v_cvt_pk_bf16_f32 v250, v236, v237\n"
    "v_cvt_pk_bf16_f32 v251, v238, v239\n"
    "buffer_store_dwordx4 v[248:251], v5, s[60:63], s82 offen sc1\n"
    "s_add_u32 s82, s82, 4096\n"
    "v_mfma_f32_16x16x32_bf16 a[68:71], a[240:243], v[156:159], a[68:71]\n"
    "s_waitcnt lgkmcnt(2)\n"
    "s_lshl_b32 s49, s49, 12\n"
    "v_mfma_f32_16x16x32_bf16 a[68:71], a[244:247], v[180:183], a[68:71]\n"
    "ds_read_b64_tr_b16 a[252:253], v16 offset:12288\n"
    "v_mfma_f32_16x16x32_bf16 a[64:67], a[244:247], v[176:179], a[64:67]\n"
    "ds_read_b64_tr_b16 a[254:255], v16 offset:14336\n"
    "v_mfma_f32_16x16x32_bf16 a[60:63], a[244:247], v[172:175], a[60:63]\n"
    "buffer_load_dwordx4 v4, s[56:59], s71 offen offset:3072  lds\n"
    "v_mfma_f32_16x16x32_bf16 a[56:59], a[244:247], v[168:171], a[56:59]\n"
    "s_add_u32 s50, s36, 16\n"
    "v_mfma_f32_16x16x32_bf16 a[52:55], a[244:247], v[164:167], a[52:55]\n"
    "v_mfma_f32_16x16x32_bf16 a[48:51], a[244:247], v[160:163], a[48:51]\n"
    "s_waitcnt vmcnt(22)\n"
    "s_barrier\n"
    "s_waitcnt lgkmcnt(2)\n"
    "v_mfma_f32_16x16x32_bf16 a[48:51], a[248:251], v[184:187], a[48:51]\n"
    "s_add_u32 m0, s64, 98304\n"
    "ds_read_b64_tr_b16 a[240:241], v16 offset:16384\n"
    "s_mul_i32 s81, s50, 43\n"
    "v_mfma_f32_16x16x32_bf16 a[52:55], a[248:251], v[188:191], a[52:55]\n"
    "s_add_u32 s71, s80, 164480\n"
    "ds_read_b64_tr_b16 a[242:243], v16 offset:18432\n"
    "v_mfma_f32_16x16x32_bf16 a[56:59], a[248:251], v[192:195], a[56:59]\n"
    "buffer_load_dwordx4 v1, s[56:59], s71 offen offset:0  lds\n"
    "v_mfma_f32_16x16x32_bf16 a[60:63], a[248:251], v[196:199], a[60:63]\n"
    "v_mfma_f32_16x16x32_bf16 a[64:67], a[248:251], v[200:203], a[64:67]\n"
    "s_lshr_b32 s81, s81, 7\n"
    "v_mfma_f32_16x16x32_bf16 a[68:71], a[248:251], v[204:207], a[68:71]\n"
    "s_waitcnt lgkmcnt(2)\n"
    "v_mfma_f32_16x16x32_bf16 a[68:71], a[252:255], v[228:231], a[68:71]\n"
    "ds_read_b64_tr_b16 a[244:245], v16 offset:20480\n"
    "v_mfma_f32_16x16x32_bf16 a[64:67], a[252:255], v[224:227], a[64:67]\n"
    "ds_read_b64_tr_b16 a[246:247], v16 offset:22528\n"
    "v_mfma_f32_16x16x32_bf16 a[60:63], a[252:255], v[220:223], a[60:63]\n"
    "s_mul_i32 s78, s50, 81920\n"
    "buffer_load_dwordx4 v2, s[56:59], s71 offen offset:1024  lds\n"
    "v_mfma_f32_16x16x32_bf16 a[56:59], a[252:255], v[216:219], a[56:59]\n"
    "v_mfma_f32_16x16x32_bf16 a[52:55], a[252:255], v[212:215], a[52:55]\n"
    "v_mfma_f32_16x16x32_bf16 a[48:51], a[252:255], v[208:211], a[48:51]\n"
    "s_waitcnt lgkmcnt(2)\n"
    "v_mfma_f32_16x16x32_bf16 a[72:75], a[240:243], v[40:43], a[72:75]\n"
    "s_mul_i32 s75, s81, 245632\n"
    "ds_read_b64_tr_b16 a[248:249], v16 offset:24576\n"
    "v_mfma_f32_16x16x32_bf16 a[76:79], a[240:243], v[44:47], a[76:79]\n"
    "ds_read_b64_tr_b16 a[250:251], v16 offset:26624\n"
    "v_mfma_f32_16x16x32_bf16 a[80:83], a[240:243], v[48:51], a[80:83]\n"
    "buffer_load_dwordx4 v3, s[56:59], s71 offen offset:2048  lds\n"
    "v_mfma_f32_16x16x32_bf16 a[84:87], a[240:243], v[52:55], a[84:87]\n"
    "s_sub_u32 s78, s78, s75\n"
    "v_mfma_f32_16x16x32_bf16 a[88:91], a[240:243], v[56:59], a[88:91]\n"
    "v_mfma_f32_16x16x32_bf16 a[92:95], a[240:243], v[60:63], a[92:95]\n"
    "s_waitcnt lgkmcnt(2)\n"
    "v_mfma_f32_16x16x32_bf16 a[92:95], a[244:247], v[84:87], a[92:95]\n"
    "ds_read_b64_tr_b16 a[252:253], v16 offset:28672\n"
    "v_accvgpr_read_b32 v232, a48\n"
    "v_mfma_f32_16x16x32_bf16 a[88:91], a[244:247], v[80:83], a[88:91]\n"
    "s_lshl_b32 s50, s50, 12\n"
    "ds_read_b64_tr_b16 a[254:255], v16 offset:30720\n"
    "v_accvgpr_read_b32 v233, a49\n"
    "v_mfma_f32_16x16x32_bf16 a[84:87], a[244:247], v[76:79], a[84:87]\n"
    "v_accvgpr_read_b32 v234, a50\n"
    "v_accvgpr_read_b32 v235, a51\n"
    "v_mfma_f32_16x16x32_bf16 a[80:83], a[244:247], v[72:75], a[80:83]\n"
    "buffer_load_dwordx4 v4, s[56:59], s71 offen offset:3072  lds\n"
    "v_cvt_pk_bf16_f32 v240, v232, v233\n"
    "v_mfma_f32_16x16x32_bf16 a[76:79], a[244:247], v[68:71], a[76:79]\n"
    "v_cvt_pk_bf16_f32 v241, v234, v235\n"
    "v_accvgpr_read_b32 v236, a52\n"
    "v_mfma_f32_16x16x32_bf16 a[72:75], a[244:247], v[64:67], a[72:75]\n"
    "s_add_u32 s51, s36, 24\n"
    "v_accvgpr_read_b32 v237, a53\n"
    "v_accvgpr_read_b32 v238, a54\n"
    "s_waitcnt vmcnt(22)\n"
    "s_barrier\n"
    "s_waitcnt lgkmcnt(2)\n"
    "v_mfma_f32_16x16x32_bf16 a[72:75], a[248:251], v[88:91], a[72:75]\n"
    "s_add_u32 m0, s64, 147456\n"
    "ds_read_b64_tr_b16 a[240:241], v0 offset:0\n"
    "v_accvgpr_read_b32 v239, a55\n"
    "v_mfma_f32_16x16x32_bf16 a[76:79], a[248:251], v[92:95], a[76:79]\n"
    "s_cmp_eq_u32 s36, 6\n"
    "s_cselect_b32 s62, 0, s74\n"
    "ds_read_b64_tr_b16 a[242:243], v0 offset:2048\n"
    "v_cvt_pk_bf16_f32 v242, v236, v237\n"
    "v_mfma_f32_16x16x32_bf16 a[80:83], a[248:251], v[96:99], a[80:83]\n"
    "s_add_u32 s71, s80, 768\n"
    "v_cvt_pk_bf16_f32 v243, v238, v239\n"
    "buffer_store_dwordx4 v[240:243], v5, s[60:63], s82 offen sc1\n"
    "v_mfma_f32_16x16x32_bf16 a[84:87], a[248:251], v[100:103], a[84:87]\n"
    "s_add_u32 s82, s82, 4096\n"
    "v_accvgpr_read_b32 v232, a56\n"
    "buffer_load_dwordx4 v1, s[56:59], s71 offen offset:0  lds\n"
    "v_mfma_f32_16x16x32_bf16 a[88:91], a[248:251], v[104:107], a[88:91]\n"
    "s_mul_i32 s85, s51, 43\n"
    "v_accvgpr_read_b32 v233, a57\n"
    "v_accvgpr_read_b32 v234, a58\n"
    "v_mfma_f32_16x16x32_bf16 a[92:95], a[248:251], v[108:111], a[92:95]\n"
    "s_waitcnt lgkmcnt(2)\n"
    "v_accvgpr_read_b32 v235, a59\n"
    "v_cvt_pk_bf16_f32 v244, v232, v233\n"
    "v_mfma_f32_16x16x32_bf16 a[92:95], a[252:255], v[132:135], a[92:95]\n"
    "s_lshr_b32 s85, s85, 7\n"
    "ds_read_b64_tr_b16 a[244:245], v0 offset:4096\n"
    "v_cvt_pk_bf16_f32 v245, v234, v235\n"
    "v_mfma_f32_16x16x32_bf16 a[88:91], a[252:255], v[128:131], a[88:91]\n"
    "ds_read_b64_tr_b16 a[246:247], v0 offset:6144\n"
    "v_accvgpr_read_b32 v236, a60\n"
    "v_accvgpr_read_b32 v237, a61\n"
    "v_mfma_f32_16x16x32_bf16 a[84:87], a[252:255], v[124:127], a[84:87]\n"
    "v_accvgpr_read_b32 v238, a62\n"
    "v_accvgpr_read_b32 v239, a63\n"
    "v_mfma_f32_16x16x32_bf16 a[80:83], a[252:255], v[120:123], a[80:83]\n"
    "s_cmp_eq_u32 s36, 7\n"
    "s_cselect_b32 s62, 0, s74\n"
    "v_cvt_pk_bf16_f32 v246, v236, v237\n"
    "v_mfma_f32_16x16x32_bf16 a[76:79], a[252:255], v[116:119], a[76:79]\n"
    "buffer_load_dwordx4 v2, s[56:59], s71 offen offset:1024  lds\n"
    "v_cvt_pk_bf16_f32 v247, v238, v239\n"
    "buffer_store_dwordx4 v[244:247], v5, s[60:63], s82 offen sc1\n"
    "v_mfma_f32_16x16x32_bf16 a[72:75], a[252:255], v[112:115], a[72:75]\n"
    "s_waitcnt lgkmcnt(2)\n"
    "s_add_u32 s82, s82, 4096\n"
    "v_accvgpr_read_b32 v232, a64\n"
    "v_accvgpr_read_b32 v233, a65\n"
    "v_mfma_f32_16x16x32_bf16 a[72:75], a[240:243], v[136:139], a[72:75]\n"
    "s_mul_i32 s79, s51, 81920\n"
    "ds_read_b64_tr_b16 a[248:249], v0 offset:8192\n"
    "v_accvgpr_read_b32 v234, a66\n"
    "v_mfma_f32_16x16x32_bf16 a[76:79], a[240:243], v[140:143], a[76:79]\n"
    "ds_read_b64_tr_b16 a[250:251], v0 offset:10240\n"
    "v_accvgpr_read_b32 v235, a67\n"
    "v_cvt_pk_bf16_f32 v248, v232, v233\n"
    "v_mfma_f32_16x16x32_bf16 a[80:83], a[240:243], v[144:147], a[80:83]\n"
    "s_mul_i32 s75, s85, 245632\n"
    "v_cvt_pk_bf16_f32 v249, v234, v235\n"
    "v_accvgpr_read_b32 v236, a68\n"
    "v_mfma_f32_16x16x32_bf16 a[84:87], a[240:243], v[148:151], a[84:87]\n"
    "v_accvgpr_read_b32 v237, a69\n"
    "v_accvgpr_read_b32 v238, a70\n"
    "v_mfma_f32_16x16x32_bf16 a[88:91], a[240:243], v[152:155], a[88:91]\n"
    "buffer_load_dwordx4 v3, s[56:59], s71 offen offset:2048  lds\n"
    "v_accvgpr_read_b32 v239, a71\n"
    "s_cmp_eq_u32 s36, 0\n"
    "s_cselect_b32 s62, 0, s74\n"
    "v_cvt_pk_bf16_f32 v250, v236, v237\n"
    "v_cvt_pk_bf16_f32 v251, v238, v239\n"
    "buffer_store_dwordx4 v[248:251], v5, s[60:63], s82 offen sc1\n"
    "s_add_u32 s82, s82, 4096\n"
    "v_mfma_f32_16x16x32_bf16 a[92:95], a[240:243], v[156:159], a[92:95]\n"
    "s_waitcnt lgkmcnt(2)\n"
    "v_mfma_f32_16x16x32_bf16 a[92:95], a[244:247], v[180:183], a[92:95]\n"
    "s_sub_u32 s79, s79, s75\n"
    "ds_read_b64_tr_b16 a[252:253], v0 offset:12288\n"
    "v_mfma_f32_16x16x32_bf16 a[88:91], a[244:247], v[176:179], a[88:91]\n"
    "ds_read_b64_tr_b16 a[254:255], v0 offset:14336\n"
    "v_mfma_f32_16x16x32_bf16 a[84:87], a[244:247], v[172:175], a[84:87]\n"
    "buffer_load_dwordx4 v4, s[56:59], s71 offen offset:3072  lds\n"
    "v_mfma_f32_16x16x32_bf16 a[80:83], a[244:247], v[168:171], a[80:83]\n"
    "s_lshl_b32 s51, s51, 12\n"
    "v_mfma_f32_16x16x32_bf16 a[76:79], a[244:247], v[164:167], a[76:79]\n"
    "v_mfma_f32_16x16x32_bf16 a[72:75], a[244:247], v[160:163], a[72:75]\n"
    "s_waitcnt vmcnt(22)\n"
    "s_barrier\n"
    "s_waitcnt lgkmcnt(2)\n"
    "v_mfma_f32_16x16x32_bf16 a[72:75], a[248:251], v[184:187], a[72:75]\n"
    "s_add_u32 m0, s64, 16384\n"
    "ds_read_b64_tr_b16 a[240:241], v0 offset:32768\n"
    "v_mfma_f32_16x16x32_bf16 a[76:79], a[248:251], v[188:191], a[76:79]\n"
    "s_add_u32 s71, s80, 164608\n"
    "ds_read_b64_tr_b16 a[242:243], v0 offset:34816\n"
    "s_mov_b32 s46, s74\n"
    "v_mfma_f32_16x16x32_bf16 a[80:83], a[248:251], v[192:195], a[80:83]\n"
    "buffer_load_dwordx4 v1, s[56:59], s71 offen offset:0  lds\n"
    "v_mfma_f32_16x16x32_bf16 a[84:87], a[248:251], v[196:199], a[84:87]\n"
    "v_mfma_f32_16x16x32_bf16 a[88:91], a[248:251], v[200:203], a[88:91]\n"
    "v_mfma_f32_16x16x32_bf16 a[92:95], a[248:251], v[204:207], a[92:95]\n"
    "s_waitcnt lgkmcnt(2)\n"
    "s_mov_b32 s72, 0xffff0000\n"
    "v_mfma_f32_16x16x32_bf16 a[92:95], a[252:255], v[228:231], a[92:95]\n"
    "ds_read_b64_tr_b16 a[244:245], v0 offset:36864\n"
    "v_mfma_f32_16x16x32_bf16 a[88:91], a[252:255], v[224:227], a[88:91]\n"
    "ds_read_b64_tr_b16 a[246:247], v0 offset:38912\n"
    "v_mfma_f32_16x16x32_bf16 a[84:87], a[252:255], v[220:223], a[84:87]\n"
    "buffer_load_dwordx4 v2, s[56:59], s71 offen offset:1024  lds\n"
    "v_mfma_f32_16x16x32_bf16 a[80:83], a[252:255], v[216:219], a[80:83]\n"
    "v_mfma_f32_16x16x32_bf16 a[76:79], a[252:255], v[212:215], a[76:79]\n"
    "v_mfma_f32_16x16x32_bf16 a[72:75], a[252:255], v[208:211], a[72:75]\n"
    "s_waitcnt lgkmcnt(2)\n"
    "v_mfma_f32_16x16x32_bf16 a[96:99], a[240:243], v[40:43], a[96:99]\n"
    "ds_read_b64_tr_b16 a[248:249], v0 offset:40960\n"
    "v_mfma_f32_16x16x32_bf16 a[100:103], a[240:243], v[44:47], a[100:103]\n"
    "ds_read_b64_tr_b16 a[250:251], v0 offset:43008\n"
    "v_mfma_f32_16x16x32_bf16 a[104:107], a[240:243], v[48:51], a[104:107]\n"
    "buffer_load_dwordx4 v3, s[56:59], s71 offen offset:2048  lds\n"
    "v_mfma_f32_16x16x32_bf16 a[108:111], a[240:243], v[52:55], a[108:111]\n"
    "v_mfma_f32_16x16x32_bf16 a[112:115], a[240:243], v[56:59], a[112:115]\n"
    "v_mfma_f32_16x16x32_bf16 a[116:119], a[240:243], v[60:63], a[116:119]\n"
    "s_waitcnt lgkmcnt(2)\n"
    "v_mfma_f32_16x16x32_bf16 a[116:119], a[244:247], v[84:87], a[116:119]\n"
    "ds_read_b64_tr_b16 a[252:253], v0 offset:45056\n"
    "v_accvgpr_read_b32 v232, a72\n"
    "v_mfma_f32_16x16x32_bf16 a[112:115], a[244:247], v[80:83], a[112:115]\n"
    "ds_read_b64_tr_b16 a[254:255], v0 offset:47104\n"
    "v_accvgpr_read_b32 v233, a73\n"
    "v_mfma_f32_16x16x32_bf16 a[108:111], a[244:247], v[76:79], a[108:111]\n"
    "v_accvgpr_read_b32 v234, a74\n"
    "v_accvgpr_read_b32 v235, a75\n"
    "v_mfma_f32_16x16x32_bf16 a[104:107], a[244:247], v[72:75], a[104:107]\n"
    "buffer_load_dwordx4 v4, s[56:59], s71 offen offset:3072  lds\n"
    "v_cvt_pk_bf16_f32 v240, v232, v233\n"
    "v_mfma_f32_16x16x32_bf16 a[100:103], a[244:247], v[68:71], a[100:103]\n"
    "v_cvt_pk_bf16_f32 v241, v234, v235\n"
    "v_accvgpr_read_b32 v236, a76\n"
    "v_mfma_f32_16x16x32_bf16 a[96:99], a[244:247], v[64:67], a[96:99]\n"
    "v_accvgpr_read_b32 v237, a77\n"
    "v_accvgpr_read_b32 v238, a78\n"
    "s_waitcnt vmcnt(22)\n"
    "s_barrier\n"
    "s_waitcnt lgkmcnt(2)\n"
    "v_mfma_f32_16x16x32_bf16 a[96:99], a[248:251], v[88:91], a[96:99]\n"
    "s_add_u32 m0, s64, 49152\n"
    "ds_read_b64_tr_b16 a[240:241], v11 offset:0\n"
    "v_accvgpr_read_b32 v239, a79\n"
    "v_mfma_f32_16x16x32_bf16 a[100:103], a[248:251], v[92:95], a[100:103]\n"
    "s_cmp_eq_u32 s36, 1\n"
    "s_cselect_b32 s62, 0, s74\n"
    "ds_read_b64_tr_b16 a[242:243], v11 offset:2048\n"
    "v_cvt_pk_bf16_f32 v242, v236, v237\n"
    "v_mfma_f32_16x16x32_bf16 a[104:107], a[248:251], v[96:99], a[104:107]\n"
    "s_add_u32 s71, s80, 896\n"
    "v_cvt_pk_bf16_f32 v243, v238, v239\n"
    "buffer_store_dwordx4 v[240:243], v5, s[60:63], s82 offen sc1\n"
    "v_mfma_f32_16x16x32_bf16 a[108:111], a[248:251], v[100:103], a[108:111]\n"
    "s_add_u32 s82, s82, 4096\n"
    "v_accvgpr_read_b32 v232, a80\n"
    "buffer_load_dwordx4 v1, s[56:59], s71 offen offset:0  lds\n"
    "v_mfma_f32_16x16x32_bf16 a[112:115], a[248:251], v[104:107], a[112:115]\n"
    "v_accvgpr_read_b32 v233, a81\n"
    "v_accvgpr_read_b32 v234, a82\n"
    "v_mfma_f32_16x16x32_bf16 a[116:119], a[248:251], v[108:111], a[116:119]\n"
    "s_waitcnt lgkmcnt(2)\n"
    "v_accvgpr_read_b32 v235, a83\n"
    "v_cvt_pk_bf16_f32 v244, v232, v233\n"
    "v_mfma_f32_16x16x32_bf16 a[116:119], a[252:255], v[132:135], a[116:119]\n"
    "ds_read_b64_tr_b16 a[244:245], v11 offset:4096\n"
    "v_cvt_pk_bf16_f32 v245, v234, v235\n"
    "v_mfma_f32_16x16x32_bf16 a[112:115], a[252:255], v[128:131], a[112:115]\n"
    "ds_read_b64_tr_b16 a[246:247], v11 offset:6144\n"
    "v_accvgpr_read_b32 v236, a84\n"
    "v_accvgpr_read_b32 v237, a85\n"
    "v_mfma_f32_16x16x32_bf16 a[108:111], a[252:255], v[124:127], a[108:111]\n"
    "v_accvgpr_read_b32 v238, a86\n"
    "v_accvgpr_read_b32 v239, a87\n"
    "v_mfma_f32_16x16x32_bf16 a[104:107], a[252:255], v[120:123], a[104:107]\n"
    "s_cmp_eq_u32 s36, 2\n"
    "s_cselect_b32 s62, 0, s74\n"
    "v_cvt_pk_bf16_f32 v246, v236, v237\n"
    "v_mfma_f32_16x16x32_bf16 a[100:103], a[252:255], v[116:119], a[100:103]\n"
    "buffer_load_dwordx4 v2, s[56:59], s71 offen offset:1024  lds\n"
    "v_cvt_pk_bf16_f32 v247, v238, v239\n"
    "buffer_store_dwordx4 v[244:247], v5, s[60:63], s82 offen sc1\n"
    "v_mfma_f32_16x16x32_bf16 a[96:99], a[252:255], v[112:115], a[96:99]\n"
    "s_waitcnt lgkmcnt(2)\n"
    "s_add_u32 s82, s82, 4096\n"
    "v_accvgpr_read_b32 v232, a88\n"
    "v_accvgpr_read_b32 v233, a89\n"
    "v_mfma_f32_16x16x32_bf16 a[96:99], a[240:243], v[136:139], a[96:99]\n"
    "ds_read_b64_tr_b16 a[248:249], v11 offset:8192\n"
    "v_accvgpr_read_b32 v234, a90\n"
    "v_mfma_f32_16x16x32_bf16 a[100:103], a[240:243], v[140:143], a[100:103]\n"
    "ds_read_b64_tr_b16 a[250:251], v11 offset:10240\n"
    "v_accvgpr_read_b32 v235, a91\n"
    "v_cvt_pk_bf16_f32 v248, v232, v233\n"
    "v_mfma_f32_16x16x32_bf16 a[104:107], a[240:243], v[144:147], a[104:107]\n"
    "v_cvt_pk_bf16_f32 v249, v234, v235\n"
    "v_accvgpr_read_b32 v236, a92\n"
    "v_mfma_f32_16x16x32_bf16 a[108:111], a[240:243], v[148:151], a[108:111]\n"
    "v_accvgpr_read_b32 v237, a93\n"
    "v_accvgpr_read_b32 v238, a94\n"
    "v_mfma_f32_16x16x32_bf16 a[112:115], a[240:243], v[152:155], a[112:115]\n"
    "buffer_load_dwordx4 v3, s[56:59], s71 offen offset:2048  lds\n"
    "v_accvgpr_read_b32 v239, a95\n"
    "s_cmp_eq_u32 s36, 3\n"
    "s_cselect_b32 s62, 0, s74\n"
    "v_cvt_pk_bf16_f32 v250, v236, v237\n"
    "v_cvt_pk_bf16_f32 v251, v238, v239\n"
    "buffer_store_dwordx4 v[248:251], v5, s[60:63], s82 offen sc1\n"
    "s_add_u32 s82, s82, 4096\n"
    "v_mfma_f32_16x16x32_bf16 a[116:119], a[240:243], v[156:159], a[116:119]\n"
    "s_waitcnt lgkmcnt(2)\n"
    "v_mfma_f32_16x16x32_bf16 a[116:119], a[244:247], v[180:183], a[116:119]\n"
    "ds_read_b64_tr_b16 a[252:253], v11 offset:12288\n"
    "v_mfma_f32_16x16x32_bf16 a[112:115], a[244:247], v[176:179], a[112:115]\n"
    "ds_read_b64_tr_b16 a[254:255], v11 offset:14336\n"
    "v_mfma_f32_16x16x32_bf16 a[108:111], a[244:247], v[172:175], a[108:111]\n"
    "buffer_load_dwordx4 v4, s[56:59], s71 offen offset:3072  lds\n"
    "v_mfma_f32_16x16x32_bf16 a[104:107], a[244:247], v[168:171], a[104:107]\n"
    "v_mfma_f32_16x16x32_bf16 a[100:103], a[244:247], v[164:167], a[100:103]\n"
    "v_mfma_f32_16x16x32_bf16 a[96:99], a[244:247], v[160:163], a[96:99]\n"
    "s_waitcnt vmcnt(22)\n"
    "s_barrier\n"
    "s_waitcnt lgkmcnt(2)\n"
    "v_mfma_f32_16x16x32_bf16 a[96:99], a[248:251], v[184:187], a[96:99]\n"
    "s_add_u32 m0, s64, 114688\n"
    "ds_read_b64_tr_b16 a[240:241], v11 offset:16384\n"
    "v_mfma_f32_16x16x32_bf16 a[100:103], a[248:251], v[188:191], a[100:103]\n"
    "s_add_u32 s71, s80, 164736\n"
    "ds_read_b64_tr_b16 a[242:243], v11 offset:18432\n"
    "v_mfma_f32_16x16x32_bf16 a[104:107], a[248:251], v[192:195], a[104:107]\n"
    "buffer_load_dwordx4 v1, s[56:59], s71 offen offset:0  lds\n"
    "v_mfma_f32_16x16x32_bf16 a[108:111], a[248:251], v[196:199], a[108:111]\n"
    "v_mfma_f32_16x16x32_bf16 a[112:115], a[248:251], v[200:203], a[112:115]\n"
    "v_mfma_f32_16x16x32_bf16 a[116:119], a[248:251], v[204:207], a[116:119]\n"
    "s_waitcnt lgkmcnt(2)\n"
    "v_mfma_f32_16x16x32_bf16 a[116:119], a[252:255], v[228:231], a[116:119]\n"
    "ds_read_b64_tr_b16 a[244:245], v11 offset:20480\n"
    "v_mfma_f32_16x16x32_bf16 a[112:115], a[252:255], v[224:227], a[112:115]\n"
    "ds_read_b64_tr_b16 a[246:247], v11 offset:22528\n"
    "v_mfma_f32_16x16x32_bf16 a[108:111], a[252:255], v[220:223], a[108:111]\n"
    "buffer_load_dwordx4 v2, s[56:59], s71 offen offset:1024  lds\n"
    "v_mfma_f32_16x16x32_bf16 a[104:107], a[252:255], v[216:219], a[104:107]\n"
    "v_mfma_f32_16x16x32_bf16 a[100:103], a[252:255], v[212:215], a[100:103]\n"
    "v_mfma_f32_16x16x32_bf16 a[96:99], a[252:255], v[208:211], a[96:99]\n"
    "s_waitcnt lgkmcnt(2)\n"
    "v_mfma_f32_16x16x32_bf16 a[120:123], a[240:243], v[40:43], a[120:123]\n"
    "ds_read_b64_tr_b16 a[248:249], v11 offset:24576\n"
    "v_mfma_f32_16x16x32_bf16 a[124:127], a[240:243], v[44:47], a[124:127]\n"
    "ds_read_b64_tr_b16 a[250:251], v11 offset:26624\n"
    "v_mfma_f32_16x16x32_bf16 a[128:131], a[240:243], v[48:51], a[128:131]\n"
    "buffer_load_dwordx4 v3, s[56:59], s71 offen offset:2048  lds\n"
    "v_mfma_f32_16x16x32_bf16 a[132:135], a[240:243], v[52:55], a[132:135]\n"
    "v_mfma_f32_16x16x32_bf16 a[136:139], a[240:243], v[56:59], a[136:139]\n"
    "v_mfma_f32_16x16x32_bf16 a[140:143], a[240:243], v[60:63], a[140:143]\n"
    "s_waitcnt lgkmcnt(2)\n"
    "v_mfma_f32_16x16x32_bf16 a[140:143], a[244:247], v[84:87], a[140:143]\n"
    "ds_read_b64_tr_b16 a[252:253], v11 offset:28672\n"
    "v_accvgpr_read_b32 v232, a96\n"
    "v_mfma_f32_16x16x32_bf16 a[136:139], a[244:247], v[80:83], a[136:139]\n"
    "ds_read_b64_tr_b16 a[254:255], v11 offset:30720\n"
    "v_accvgpr_read_b32 v233, a97\n"
    "v_mfma_f32_16x16x32_bf16 a[132:135], a[244:247], v[76:79], a[132:135]\n"
    "v_accvgpr_read_b32 v234, a98\n"
    "v_accvgpr_read_b32 v235, a99\n"
    "v_mfma_f32_16x16x32_bf16 a[128:131], a[244:247], v[72:75], a[128:131]\n"
    "buffer_load_dwordx4 v4, s[56:59], s71 offen offset:3072  lds\n"
    "v_cvt_pk_bf16_f32 v240, v232, v233\n"
    "v_mfma_f32_16x16x32_bf16 a[124:127], a[244:247], v[68:71], a[124:127]\n"
    "v_cvt_pk_bf16_f32 v241, v234, v235\n"
    "v_accvgpr_read_b32 v236, a100\n"
    "v_mfma_f32_16x16x32_bf16 a[120:123], a[244:247], v[64:67], a[120:123]\n"
    "v_accvgpr_read_b32 v237, a101\n"
    "v_accvgpr_read_b32 v238, a102\n"
    "s_waitcnt vmcnt(22)\n"
    "s_barrier\n"
    "s_waitcnt lgkmcnt(2)\n"
    "v_mfma_f32_16x16x32_bf16 a[120:123], a[248:251], v[88:91], a[120:123]\n"
    "s_add_u32 m0, s64, 131072\n"
    "ds_read_b64_tr_b16 a[240:241], v11 offset:32768\n"
    "v_accvgpr_read_b32 v239, a103\n"
    "v_mfma_f32_16x16x32_bf16 a[124:127], a[248:251], v[92:95], a[124:127]\n"
    "s_cmp_eq_u32 s36, 4\n"
    "s_cselect_b32 s62, 0, s74\n"
    "ds_read_b64_tr_b16 a[242:243], v11 offset:34816\n"
    "v_cvt_pk_bf16_f32 v242, v236, v237\n"
    "v_mfma_f32_16x16x32_bf16 a[128:131], a[248:251], v[96:99], a[128:131]\n"
    "s_add_u32 s71, s80, 1024\n"
    "v_cvt_pk_bf16_f32 v243, v238, v239\n"
    "buffer_store_dwordx4 v[240:243], v5, s[60:63], s82 offen sc1\n"
    "v_mfma_f32_16x16x32_bf16 a[132:135], a[248:251], v[100:103], a[132:135]\n"
    "s_add_u32 s82, s82, 4096\n"
    "v_accvgpr_read_b32 v232, a104\n"
    "buffer_load_dwordx4 v1, s[56:59], s71 offen offset:0  lds\n"
    "v_mfma_f32_16x16x32_bf16 a[136:139], a[248:251], v[104:107], a[136:139]\n"
    "v_accvgpr_read_b32 v233, a105\n"
    "v_accvgpr_read_b32 v234, a106\n"
    "v_mfma_f32_16x16x32_bf16 a[140:143], a[248:251], v[108:111], a[140:143]\n"
    "s_waitcnt lgkmcnt(2)\n"
    "v_accvgpr_read_b32 v235, a107\n"
    "v_cvt_pk_bf16_f32 v244, v232, v233\n"
    "v_mfma_f32_16x16x32_bf16 a[140:143], a[252:255], v[132:135], a[140:143]\n"
    "ds_read_b64_tr_b16 a[244:245], v11 offset:36864\n"
    "v_cvt_pk_bf16_f32 v245, v234, v235\n"
    "v_mfma_f32_16x16x32_bf16 a[136:139], a[252:255], v[128:131], a[136:139]\n"
    "ds_read_b64_tr_b16 a[246:247], v11 offset:38912\n"
    "v_accvgpr_read_b32 v236, a108\n"
    "v_accvgpr_read_b32 v237, a109\n"
    "v_mfma_f32_16x16x32_bf16 a[132:135], a[252:255], v[124:127], a[132:135]\n"
    "v_accvgpr_read_b32 v238, a110\n"
    "v_accvgpr_read_b32 v239, a111\n"
    "v_mfma_f32_16x16x32_bf16 a[128:131], a[252:255], v[120:123], a[128:131]\n"
    "s_cmp_eq_u32 s36, 5\n"
    "s_cselect_b32 s62, 0, s74\n"
    "v_cvt_pk_bf16_f32 v246, v236, v237\n"
    "v_mfma_f32_16x16x32_bf16 a[124:127], a[252:255], v[116:119], a[124:127]\n"
    "buffer_load_dwordx4 v2, s[56:59], s71 offen offset:1024  lds\n"
    "v_cvt_pk_bf16_f32 v247, v238, v239\n"
    "buffer_store_dwordx4 v[244:247], v5, s[60:63], s82 offen sc1\n"
    "v_mfma_f32_16x16x32_bf16 a[120:123], a[252:255], v[112:115], a[120:123]\n"
    "s_waitcnt lgkmcnt(2)\n"
    "s_add_u32 s82, s82, 4096\n"
    "v_accvgpr_read_b32 v232, a112\n"
    "v_accvgpr_read_b32 v233, a113\n"
    "v_mfma_f32_16x16x32_bf16 a[120:123], a[240:243], v[136:139], a[120:123]\n"
    "ds_read_b64_tr_b16 a[248:249], v11 offset:40960\n"
    "v_accvgpr_read_b32 v234, a114\n"
    "v_mfma_f32_16x16x32_bf16 a[124:127], a[240:243], v[140:143], a[124:127]\n"
    "ds_read_b64_tr_b16 a[250:251], v11 offset:43008\n"
    "v_accvgpr_read_b32 v235, a115\n"
    "v_cvt_pk_bf16_f32 v248, v232, v233\n"
    "v_mfma_f32_16x16x32_bf16 a[128:131], a[240:243], v[144:147], a[128:131]\n"
    "v_cvt_pk_bf16_f32 v249, v234, v235\n"
    "v_accvgpr_read_b32 v236, a116\n"
    "v_mfma_f32_16x16x32_bf16 a[132:135], a[240:243], v[148:151], a[132:135]\n"
    "v_accvgpr_read_b32 v237, a117\n"
    "v_accvgpr_read_b32 v238, a118\n"
    "v_mfma_f32_16x16x32_bf16 a[136:139], a[240:243], v[152:155], a[136:139]\n"
    "buffer_load_dwordx4 v3, s[56:59], s71 offen offset:2048  lds\n"
    "v_accvgpr_read_b32 v239, a119\n"
    "s_cmp_eq_u32 s36, 6\n"
    "s_cselect_b32 s62, 0, s74\n"
    "v_cvt_pk_bf16_f32 v250, v236, v237\n"
    "v_cvt_pk_bf16_f32 v251, v238, v239\n"
    "buffer_store_dwordx4 v[248:251], v5, s[60:63], s82 offen sc1\n"
    "s_add_u32 s82, s82, 4096\n"
    "v_mfma_f32_16x16x32_bf16 a[140:143], a[240:243], v[156:159], a[140:143]\n"
    "s_waitcnt lgkmcnt(2)\n"
    "v_mfma_f32_16x16x32_bf16 a[140:143], a[244:247], v[180:183], a[140:143]\n"
    "ds_read_b64_tr_b16 a[252:253], v11 offset:45056\n"
    "v_mfma_f32_16x16x32_bf16 a[136:139], a[244:247], v[176:179], a[136:139]\n"
    "ds_read_b64_tr_b16 a[254:255], v11 offset:47104\n"
    "v_mfma_f32_16x16x32_bf16 a[132:135], a[244:247], v[172:175], a[132:135]\n"
    "buffer_load_dwordx4 v4, s[56:59], s71 offen offset:3072  lds\n"
    "v_mfma_f32_16x16x32_bf16 a[128:131], a[244:247], v[168:171], a[128:131]\n"
    "v_mfma_f32_16x16x32_bf16 a[124:127], a[244:247], v[164:167], a[124:127]\n"
    "v_mfma_f32_16x16x32_bf16 a[120:123], a[244:247], v[160:163], a[120:123]\n"
    "s_waitcnt vmcnt(22)\n"
    "s_barrier\n"
    "s_waitcnt lgkmcnt(2)\n"
    "v_mfma_f32_16x16x32_bf16 a[120:123], a[248:251], v[184:187], a[120:123]\n"
    "s_add_u32 m0, s64, 0\n"
    "ds_read_b64_tr_b16 a[240:241], v16 offset:32768\n"
    "v_mfma_f32_16x16x32_bf16 a[124:127], a[248:251], v[188:191], a[124:127]\n"
    "s_add_u32 s71, s80, 164864\n"
    "ds_read_b64_tr_b16 a[242:243], v16 offset:34816\n"
    "v_mfma_f32_16x16x32_bf16 a[128:131], a[248:251], v[192:195], a[128:131]\n"
    "buffer_load_dwordx4 v1, s[56:59], s71 offen offset:0  lds\n"
    "v_mfma_f32_16x16x32_bf16 a[132:135], a[248:251], v[196:199], a[132:135]\n"
    "v_mfma_f32_16x16x32_bf16 a[136:139], a[248:251], v[200:203], a[136:139]\n"
    "v_mfma_f32_16x16x32_bf16 a[140:143], a[248:251], v[204:207], a[140:143]\n"
    "s_waitcnt lgkmcnt(2)\n"
    "v_mfma_f32_16x16x32_bf16 a[140:143], a[252:255], v[228:231], a[140:143]\n"
    "ds_read_b64_tr_b16 a[244:245], v16 offset:36864\n"
    "v_mfma_f32_16x16x32_bf16 a[136:139], a[252:255], v[224:227], a[136:139]\n"
    "ds_read_b64_tr_b16 a[246:247], v16 offset:38912\n"
    "v_mfma_f32_16x16x32_bf16 a[132:135], a[252:255], v[220:223], a[132:135]\n"
    "buffer_load_dwordx4 v2, s[56:59], s71 offen offset:1024  lds\n"
    "v_mfma_f32_16x16x32_bf16 a[128:131], a[252:255], v[216:219], a[128:131]\n"
    "v_mfma_f32_16x16x32_bf16 a[124:127], a[252:255], v[212:215], a[124:127]\n"
    "v_mfma_f32_16x16x32_bf16 a[120:123], a[252:255], v[208:211], a[120:123]\n"
    "s_waitcnt lgkmcnt(2)\n"
    "v_mfma_f32_16x16x32_bf16 a[144:147], a[240:243], v[40:43], a[144:147]\n"
    "ds_read_b64_tr_b16 a[248:249], v16 offset:40960\n"
    "v_mfma_f32_16x16x32_bf16 a[148:151], a[240:243], v[44:47], a[148:151]\n"
    "ds_read_b64_tr_b16 a[250:251], v16 offset:43008\n"
    "v_mfma_f32_16x16x32_bf16 a[152:155], a[240:243], v[48:51], a[152:155]\n"
    "buffer_load_dwordx4 v3, s[56:59], s71 offen offset:2048  lds\n"
    "v_mfma_f32_16x16x32_bf16 a[156:159], a[240:243], v[52:55], a[156:159]\n"
    "v_mfma_f32_16x16x32_bf16 a[160:163], a[240:243], v[56:59], a[160:163]\n"
    "v_mfma_f32_16x16x32_bf16 a[164:167], a[240:243], v[60:63], a[164:167]\n"
    "s_waitcnt lgkmcnt(2)\n"
    "v_mfma_f32_16x16x32_bf16 a[164:167], a[244:247], v[84:87], a[164:167]\n"
    "s_waitcnt vmcnt(26)\n"
    "v_mov_b32 v8, 3\n"
    "ds_read_b64_tr_b16 a[252:253], v16 offset:45056\n"
    "v_mfma_f32_16x16x32_bf16 a[160:163], a[244:247], v[80:83], a[160:163]\n"
    "ds_read_b64_tr_b16 a[254:255], v16 offset:47104\n"
    "v_accvgpr_read_b32 v232, a120\n"
    "v_mfma_f32_16x16x32_bf16 a[156:159], a[244:247], v[76:79], a[156:159]\n"
    "v_accvgpr_read_b32 v233, a121\n"
    "buffer_store_dword v8, v9, s[92:95], 0 offen offset:512 sc1\n"
    "v_mfma_f32_16x16x32_bf16 a[152:155], a[244:247], v[72:75], a[152:155]\n"
    "v_accvgpr_read_b32 v234, a122\n"
    "v_accvgpr_read_b32 v235, a123\n"
    "v_mfma_f32_16x16x32_bf16 a[148:151], a[244:247], v[68:71], a[148:151]\n"
    "buffer_load_dwordx4 v4, s[56:59], s71 offen offset:3072  lds\n"
    "v_cvt_pk_bf16_f32 v240, v232, v233\n"
    "v_cvt_pk_bf16_f32 v241, v234, v235\n"
    "v_mfma_f32_16x16x32_bf16 a[144:147], a[244:247], v[64:67], a[144:147]\n"
    "v_accvgpr_read_b32 v236, a124\n"
    "v_accvgpr_read_b32 v237, a125\n"
    "s_waitcnt vmcnt(23)\n"
    "s_barrier\n"
    "s_waitcnt lgkmcnt(2)\n"
    "v_mfma_f32_16x16x32_bf16 a[144:147], a[248:251], v[88:91], a[144:147]\n"
    "s_add_u32 m0, s64, 32768\n"
    "ds_read_b64_tr_b16 a[240:241], v0 offset:16384\n"
    "v_accvgpr_read_b32 v238, a126\n"
    "v_mfma_f32_16x16x32_bf16 a[148:151], a[248:251], v[92:95], a[148:151]\n"
    "ds_read_b64_tr_b16 a[242:243], v0 offset:18432\n"
    "v_accvgpr_read_b32 v239, a127\n"
    "s_cmp_eq_u32 s36, 7\n"
    "s_cselect_b32 s62, 0, s74\n"
    "v_cvt_pk_bf16_f32 v242, v236, v237\n"
    "v_mfma_f32_16x16x32_bf16 a[152:155], a[248:251], v[96:99], a[152:155]\n"
    "s_add_u32 s71, s80, 1152\n"
    "v_cvt_pk_bf16_f32 v243, v238, v239\n"
    "buffer_store_dwordx4 v[240:243], v5, s[60:63], s82 offen sc1\n"
    "v_mfma_f32_16x16x32_bf16 a[156:159], a[248:251], v[100:103], a[156:159]\n"
    "s_add_u32 s82, s82, 4096\n"
    "v_accvgpr_read_b32 v232, a128\n"
    "buffer_load_dwordx4 v1, s[56:59], s71 offen offset:0  lds\n"
    "v_mfma_f32_16x16x32_bf16 a[160:163], a[248:251], v[104:107], a[160:163]\n"
    "v_accvgpr_read_b32 v233, a129\n"
    "v_accvgpr_read_b32 v234, a130\n"
    "v_mfma_f32_16x16x32_bf16 a[164:167], a[248:251], v[108:111], a[164:167]\n"
    "s_waitcnt lgkmcnt(2)\n"
    "v_accvgpr_read_b32 v235, a131\n"
    "v_cvt_pk_bf16_f32 v244, v232, v233\n"
    "v_mfma_f32_16x16x32_bf16 a[164:167], a[252:255], v[132:135], a[164:167]\n"
    "ds_read_b64_tr_b16 a[244:245], v0 offset:20480\n"
    "v_cvt_pk_bf16_f32 v245, v234, v235\n"
    "v_mfma_f32_16x16x32_bf16 a[160:163], a[252:255], v[128:131], a[160:163]\n"
    "ds_read_b64_tr_b16 a[246:247], v0 offset:22528\n"
    "v_accvgpr_read_b32 v236, a132\n"
    "v_accvgpr_read_b32 v237, a133\n"
    "v_mfma_f32_16x16x32_bf16 a[156:159], a[252:255], v[124:127], a[156:159]\n"
    "v_accvgpr_read_b32 v238, a134\n"
    "v_accvgpr_read_b32 v239, a135\n"
    "v_mfma_f32_16x16x32_bf16 a[152:155], a[252:255], v[120:123], a[152:155]\n"
    "s_cmp_eq_u32 s36, 0\n"
    "s_cselect_b32 s62, 0, s74\n"
    "v_cvt_pk_bf16_f32 v246, v236, v237\n"
    "v_mfma_f32_16x16x32_bf16 a[148:151], a[252:255], v[116:119], a[148:151]\n"
    "buffer_load_dwordx4 v2, s[56:59], s71 offen offset:1024  lds\n"
    "v_cvt_pk_bf16_f32 v247, v238, v239\n"
    "buffer_store_dwordx4 v[244:247], v5, s[60:63], s82 offen sc1\n"
    "v_mfma_f32_16x16x32_bf16 a[144:147], a[252:255], v[112:115], a[144:147]\n"
    "s_waitcnt lgkmcnt(2)\n"
    "s_add_u32 s82, s82, 4096\n"
    "v_accvgpr_read_b32 v232, a136\n"
    "v_accvgpr_read_b32 v233, a137\n"
    "v_mfma_f32_16x16x32_bf16 a[144:147], a[240:243], v[136:139], a[144:147]\n"
    "ds_read_b64_tr_b16 a[248:249], v0 offset:24576\n"
    "v_accvgpr_read_b32 v234, a138\n"
    "v_mfma_f32_16x16x32_bf16 a[148:151], a[240:243], v[140:143], a[148:151]\n"
    "ds_read_b64_tr_b16 a[250:251], v0 offset:26624\n"
    "v_accvgpr_read_b32 v235, a139\n"
    "v_cvt_pk_bf16_f32 v248, v232, v233\n"
    "v_mfma_f32_16x16x32_bf16 a[152:155], a[240:243], v[144:147], a[152:155]\n"
    "v_cvt_pk_bf16_f32 v249, v234, v235\n"
    "v_accvgpr_read_b32 v236, a140\n"
    "v_mfma_f32_16x16x32_bf16 a[156:159], a[240:243], v[148:151], a[156:159]\n"
    "v_accvgpr_read_b32 v237, a141\n"
    "v_accvgpr_read_b32 v238, a142\n"
    "v_mfma_f32_16x16x32_bf16 a[160:163], a[240:243], v[152:155], a[160:163]\n"
    "buffer_load_dwordx4 v3, s[56:59], s71 offen offset:2048  lds\n"
    "v_accvgpr_read_b32 v239, a143\n"
    "s_cmp_eq_u32 s36, 1\n"
    "s_cselect_b32 s62, 0, s74\n"
    "v_cvt_pk_bf16_f32 v250, v236, v237\n"
    "v_cvt_pk_bf16_f32 v251, v238, v239\n"
    "buffer_store_dwordx4 v[248:251], v5, s[60:63], s82 offen sc1\n"
    "s_add_u32 s82, s82, 4096\n"
    "v_mfma_f32_16x16x32_bf16 a[164:167], a[240:243], v[156:159], a[164:167]\n"
    "s_waitcnt lgkmcnt(2)\n"
    "v_mfma_f32_16x16x32_bf16 a[164:167], a[244:247], v[180:183], a[164:167]\n"
    "ds_read_b64_tr_b16 a[252:253], v0 offset:28672\n"
    "v_mfma_f32_16x16x32_bf16 a[160:163], a[244:247], v[176:179], a[160:163]\n"
    "ds_read_b64_tr_b16 a[254:255], v0 offset:30720\n"
    "v_mfma_f32_16x16x32_bf16 a[156:159], a[244:247], v[172:175], a[156:159]\n"
    "buffer_load_dwordx4 v4, s[56:59], s71 offen offset:3072  lds\n"
    "v_mfma_f32_16x16x32_bf16 a[152:155], a[244:247], v[168:171], a[152:155]\n"
    "v_mfma_f32_16x16x32_bf16 a[148:151], a[244:247], v[164:167], a[148:151]\n"
    "v_mfma_f32_16x16x32_bf16 a[144:147], a[244:247], v[160:163], a[144:147]\n"
    "s_waitcnt vmcnt(23)\n"
    "s_barrier\n"
    "s_waitcnt lgkmcnt(2)\n"
    "v_mfma_f32_16x16x32_bf16 a[144:147], a[248:251], v[184:187], a[144:147]\n"
    "s_add_u32 m0, s64, 65536\n"
    "ds_read_b64_tr_b16 a[240:241], v0 offset:49152\n"
    "v_mfma_f32_16x16x32_bf16 a[148:151], a[248:251], v[188:191], a[148:151]\n"
    "s_add_u32 s71, s80, 164992\n"
    "ds_read_b64_tr_b16 a[242:243], v0 offset:51200\n"
    "v_mfma_f32_16x16x32_bf16 a[152:155], a[248:251], v[192:195], a[152:155]\n"
    "buffer_load_dwordx4 v1, s[56:59], s71 offen offset:0  lds\n"
    "v_mfma_f32_16x16x32_bf16 a[156:159], a[248:251], v[196:199], a[156:159]\n"
    "v_mfma_f32_16x16x32_bf16 a[160:163], a[248:251], v[200:203], a[160:163]\n"
    "v_mfma_f32_16x16x32_bf16 a[164:167], a[248:251], v[204:207], a[164:167]\n"
    "s_waitcnt lgkmcnt(2)\n"
    "v_mfma_f32_16x16x32_bf16 a[164:167], a[252:255], v[228:231], a[164:167]\n"
    "ds_read_b64_tr_b16 a[244:245], v0 offset:53248\n"
    "v_mfma_f32_16x16x32_bf16 a[160:163], a[252:255], v[224:227], a[160:163]\n"
    "ds_read_b64_tr_b16 a[246:247], v0 offset:55296\n"
    "v_mfma_f32_16x16x32_bf16 a[156:159], a[252:255], v[220:223], a[156:159]\n"
    "buffer_load_dwordx4 v2, s[56:59], s71 offen offset:1024  lds\n"
    "v_mfma_f32_16x16x32_bf16 a[152:155], a[252:255], v[216:219], a[152:155]\n"
    "v_mfma_f32_16x16x32_bf16 a[148:151], a[252:255], v[212:215], a[148:151]\n"
    "v_mfma_f32_16x16x32_bf16 a[144:147], a[252:255], v[208:211], a[144:147]\n"
    "s_waitcnt lgkmcnt(2)\n"
    "v_mfma_f32_16x16x32_bf16 a[168:171], a[240:243], v[40:43], a[168:171]\n"
    "ds_read_b64_tr_b16 a[248:249], v0 offset:57344\n"
    "v_mfma_f32_16x16x32_bf16 a[172:175], a[240:243], v[44:47], a[172:175]\n"
    "ds_read_b64_tr_b16 a[250:251], v0 offset:59392\n"
    "v_mfma_f32_16x16x32_bf16 a[176:179], a[240:243], v[48:51], a[176:179]\n"
    "buffer_load_dwordx4 v3, s[56:59], s71 offen offset:2048  lds\n"
    "v_mfma_f32_16x16x32_bf16 a[180:183], a[240:243], v[52:55], a[180:183]\n"
    "v_mfma_f32_16x16x32_bf16 a[184:187], a[240:243], v[56:59], a[184:187]\n"
    "v_mfma_f32_16x16x32_bf16 a[188:191], a[240:243], v[60:63], a[188:191]\n"
    "s_waitcnt lgkmcnt(2)\n"
    "v_mfma_f32_16x16x32_bf16 a[188:191], a[244:247], v[84:87], a[188:191]\n"
    "ds_read_b64_tr_b16 a[252:253], v0 offset:61440\n"
    "v_accvgpr_read_b32 v232, a144\n"
    "v_mfma_f32_16x16x32_bf16 a[184:187], a[244:247], v[80:83], a[184:187]\n"
    "ds_read_b64_tr_b16 a[254:255], v0 offset:63488\n"
    "v_accvgpr_read_b32 v233, a145\n"
    "v_mfma_f32_16x16x32_bf16 a[180:183], a[244:247], v[76:79], a[180:183]\n"
    "v_accvgpr_read_b32 v234, a146\n"
    "v_accvgpr_read_b32 v235, a147\n"
    "v_mfma_f32_16x16x32_bf16 a[176:179], a[244:247], v[72:75], a[176:179]\n"
    "buffer_load_dwordx4 v4, s[56:59], s71 offen offset:3072  lds\n"
    "v_cvt_pk_bf16_f32 v240, v232, v233\n"
    "v_mfma_f32_16x16x32_bf16 a[172:175], a[244:247], v[68:71], a[172:175]\n"
    "v_cvt_pk_bf16_f32 v241, v234, v235\n"
    "v_accvgpr_read_b32 v236, a148\n"
    "v_mfma_f32_16x16x32_bf16 a[168:171], a[244:247], v[64:67], a[168:171]\n"
    "v_accvgpr_read_b32 v237, a149\n"
    "v_accvgpr_read_b32 v238, a150\n"
    "s_waitcnt vmcnt(23)\n"
    "s_barrier\n"
    "s_waitcnt lgkmcnt(2)\n"
    "v_mfma_f32_16x16x32_bf16 a[168:171], a[248:251], v[88:91], a[168:171]\n"
    "ds_read_b64_tr_b16 a[240:241], v16 offset:0\n"
    "v_accvgpr_read_b32 v239, a151\n"
    "v_mfma_f32_16x16x32_bf16 a[172:175], a[248:251], v[92:95], a[172:175]\n"
    "s_cmp_eq_u32 s36, 2\n"
    "s_cselect_b32 s62, 0, s74\n"
    "ds_read_b64_tr_b16 a[242:243], v16 offset:2048\n"
    "v_cvt_pk_bf16_f32 v242, v236, v237\n"
    "v_mfma_f32_16x16x32_bf16 a[176:179], a[248:251], v[96:99], a[176:179]\n"
    "v_cvt_pk_bf16_f32 v243, v238, v239\n"
    "buffer_store_dwordx4 v[240:243], v5, s[60:63], s82 offen sc1\n"
    "s_add_u32 s82, s82, 4096\n"
    "v_mfma_f32_16x16x32_bf16 a[180:183], a[248:251], v[100:103], a[180:183]\n"
    "v_accvgpr_read_b32 v232, a152\n"
    "v_accvgpr_read_b32 v233, a153\n"
    "v_mfma_f32_16x16x32_bf16 a[184:187], a[248:251], v[104:107], a[184:187]\n"
    "v_accvgpr_read_b32 v234, a154\n"
    "v_accvgpr_read_b32 v235, a155\n"
    "v_mfma_f32_16x16x32_bf16 a[188:191], a[248:251], v[108:111], a[188:191]\n"
    "s_waitcnt lgkmcnt(2)\n"
    "v_cvt_pk_bf16_f32 v244, v232, v233\n"
    "v_cvt_pk_bf16_f32 v245, v234, v235\n"
    "v_mfma_f32_16x16x32_bf16 a[188:191], a[252:255], v[132:135], a[188:191]\n"
    "ds_read_b64_tr_b16 a[244:245], v16 offset:4096\n"
    "v_accvgpr_read_b32 v236, a156\n"
    "v_mfma_f32_16x16x32_bf16 a[184:187], a[252:255], v[128:131], a[184:187]\n"
    "ds_read_b64_tr_b16 a[246:247], v16 offset:6144\n"
    "v_accvgpr_read_b32 v237, a157\n"
    "v_mfma_f32_16x16x32_bf16 a[180:183], a[252:255], v[124:127], a[180:183]\n"
    "v_accvgpr_read_b32 v238, a158\n"
    "v_accvgpr_read_b32 v239, a159\n"
    "v_mfma_f32_16x16x32_bf16 a[176:179], a[252:255], v[120:123], a[176:179]\n"
    "s_cmp_eq_u32 s36, 3\n"
    "s_cselect_b32 s62, 0, s74\n"
    "v_cvt_pk_bf16_f32 v246, v236, v237\n"
    "v_mfma_f32_16x16x32_bf16 a[172:175], a[252:255], v[116:119], a[172:175]\n"
    "v_cvt_pk_bf16_f32 v247, v238, v239\n"
    "buffer_store_dwordx4 v[244:247], v5, s[60:63], s82 offen sc1\n"
    "s_add_u32 s82, s82, 4096\n"
    "v_mfma_f32_16x16x32_bf16 a[168:171], a[252:255], v[112:115], a[168:171]\n"
    "s_waitcnt lgkmcnt(2)\n"
    "v_accvgpr_read_b32 v232, a160\n"
    "v_accvgpr_read_b32 v233, a161\n"
    "v_mfma_f32_16x16x32_bf16 a[168:171], a[240:243], v[136:139], a[168:171]\n"
    "ds_read_b64_tr_b16 a[248:249], v16 offset:8192\n"
    "v_accvgpr_read_b32 v234, a162\n"
    "v_mfma_f32_16x16x32_bf16 a[172:175], a[240:243], v[140:143], a[172:175]\n"
    "ds_read_b64_tr_b16 a[250:251], v16 offset:10240\n"
    "v_accvgpr_read_b32 v235, a163\n"
    "v_cvt_pk_bf16_f32 v248, v232, v233\n"
    "v_mfma_f32_16x16x32_bf16 a[176:179], a[240:243], v[144:147], a[176:179]\n"
    "v_cvt_pk_bf16_f32 v249, v234, v235\n"
    "v_accvgpr_read_b32 v236, a164\n"
    "v_mfma_f32_16x16x32_bf16 a[180:183], a[240:243], v[148:151], a[180:183]\n"
    "v_accvgpr_read_b32 v237, a165\n"
    "v_accvgpr_read_b32 v238, a166\n"
    "v_mfma_f32_16x16x32_bf16 a[184:187], a[240:243], v[152:155], a[184:187]\n"
    "v_accvgpr_read_b32 v239, a167\n"
    "s_cmp_eq_u32 s36, 4\n"
    "s_cselect_b32 s62, 0, s74\n"
    "v_cvt_pk_bf16_f32 v250, v236, v237\n"
    "v_cvt_pk_bf16_f32 v251, v238, v239\n"
    "buffer_store_dwordx4 v[248:251], v5, s[60:63], s82 offen sc1\n"
    "s_add_u32 s82, s82, 4096\n"
    "v_mfma_f32_16x16x32_bf16 a[188:191], a[240:243], v[156:159], a[188:191]\n"
    "s_waitcnt lgkmcnt(2)\n"
    "v_mfma_f32_16x16x32_bf16 a[188:191], a[244:247], v[180:183], a[188:191]\n"
    "ds_read_b64_tr_b16 a[252:253], v16 offset:12288\n"
    "v_mfma_f32_16x16x32_bf16 a[184:187], a[244:247], v[176:179], a[184:187]\n"
    "ds_read_b64_tr_b16 a[254:255], v16 offset:14336\n"
    "v_mfma_f32_16x16x32_bf16 a[180:183], a[244:247], v[172:175], a[180:183]\n"
    "v_mfma_f32_16x16x32_bf16 a[176:179], a[244:247], v[168:171], a[176:179]\n"
    "v_mfma_f32_16x16x32_bf16 a[172:175], a[244:247], v[164:167], a[172:175]\n"
    "v_mfma_f32_16x16x32_bf16 a[168:171], a[244:247], v[160:163], a[168:171]\n"
    "s_waitcnt vmcnt(19)\n"
    "s_barrier\n"
    "s_waitcnt lgkmcnt(2)\n"
    "v_mfma_f32_16x16x32_bf16 a[168:171], a[248:251], v[184:187], a[168:171]\n"
    "ds_read_b64_tr_b16 a[240:241], v16 offset:16384\n"
    "v_mfma_f32_16x16x32_bf16 a[172:175], a[248:251], v[188:191], a[172:175]\n"
    "ds_read_b64_tr_b16 a[242:243], v16 offset:18432\n"
    "v_mfma_f32_16x16x32_bf16 a[176:179], a[248:251], v[192:195], a[176:179]\n"
    "v_mfma_f32_16x16x32_bf16 a[180:183], a[248:251], v[196:199], a[180:183]\n"
    "v_mfma_f32_16x16x32_bf16 a[184:187], a[248:251], v[200:203], a[184:187]\n"
    "v_mfma_f32_16x16x32_bf16 a[188:191], a[248:251], v[204:207], a[188:191]\n"
    "s_waitcnt lgkmcnt(2)\n"
    "v_mfma_f32_16x16x32_bf16 a[188:191], a[252:255], v[228:231], a[188:191]\n"
    "ds_read_b64_tr_b16 a[244:245], v16 offset:20480\n"
    "v_mfma_f32_16x16x32_bf16 a[184:187], a[252:255], v[224:227], a[184:187]\n"
    "ds_read_b64_tr_b16 a[246:247], v16 offset:22528\n"
    "v_mfma_f32_16x16x32_bf16 a[180:183], a[252:255], v[220:223], a[180:183]\n"
    "v_mfma_f32_16x16x32_bf16 a[176:179], a[252:255], v[216:219], a[176:179]\n"
    "v_mfma_f32_16x16x32_bf16 a[172:175], a[252:255], v[212:215], a[172:175]\n"
    "v_mfma_f32_16x16x32_bf16 a[168:171], a[252:255], v[208:211], a[168:171]\n"
    "s_waitcnt lgkmcnt(2)\n"
    "v_mfma_f32_16x16x32_bf16 a[192:195], a[240:243], v[40:43], a[192:195]\n"
    "ds_read_b64_tr_b16 a[248:249], v16 offset:24576\n"
    "v_mfma_f32_16x16x32_bf16 a[196:199], a[240:243], v[44:47], a[196:199]\n"
    "ds_read_b64_tr_b16 a[250:251], v16 offset:26624\n"
    "v_mfma_f32_16x16x32_bf16 a[200:203], a[240:243], v[48:51], a[200:203]\n"
    "v_mfma_f32_16x16x32_bf16 a[204:207], a[240:243], v[52:55], a[204:207]\n"
    "v_mfma_f32_16x16x32_bf16 a[208:211], a[240:243], v[56:59], a[208:211]\n"
    "v_mfma_f32_16x16x32_bf16 a[212:215], a[240:243], v[60:63], a[212:215]\n"
    "s_waitcnt lgkmcnt(2)\n"
    "v_mfma_f32_16x16x32_bf16 a[212:215], a[244:247], v[84:87], a[212:215]\n"
    "ds_read_b64_tr_b16 a[252:253], v16 offset:28672\n"
    "v_accvgpr_read_b32 v232, a168\n"
    "v_mfma_f32_16x16x32_bf16 a[208:211], a[244:247], v[80:83], a[208:211]\n"
    "ds_read_b64_tr_b16 a[254:255], v16 offset:30720\n"
    "v_accvgpr_read_b32 v233, a169\n"
    "v_mfma_f32_16x16x32_bf16 a[204:207], a[244:247], v[76:79], a[204:207]\n"
    "v_accvgpr_read_b32 v234, a170\n"
    "v_accvgpr_read_b32 v235, a171\n"
    "v_mfma_f32_16x16x32_bf16 a[200:203], a[244:247], v[72:75], a[200:203]\n"
    "v_cvt_pk_bf16_f32 v240, v232, v233\n"
    "v_cvt_pk_bf16_f32 v241, v234, v235\n"
    "v_mfma_f32_16x16x32_bf16 a[196:199], a[244:247], v[68:71], a[196:199]\n"
    "v_accvgpr_read_b32 v236, a172\n"
    "v_accvgpr_read_b32 v237, a173\n"
    "v_mfma_f32_16x16x32_bf16 a[192:195], a[244:247], v[64:67], a[192:195]\n"
    "v_accvgpr_read_b32 v238, a174\n"
    "v_accvgpr_read_b32 v239, a175\n"
    "s_waitcnt vmcnt(14)\n"
    "s_barrier\n"
    "s_waitcnt lgkmcnt(2)\n"
    "v_mfma_f32_16x16x32_bf16 a[192:195], a[248:251], v[88:91], a[192:195]\n"
    "s_cmp_eq_u32 s36, 5\n"
    "s_cselect_b32 s62, 0, s74\n"
    "ds_read_b64_tr_b16 a[240:241], v0 offset:0\n"
    "v_mfma_f32_16x16x32_bf16 a[196:199], a[248:251], v[92:95], a[196:199]\n"
    "ds_read_b64_tr_b16 a[242:243], v0 offset:2048\n"
    "v_cvt_pk_bf16_f32 v242, v236, v237\n"
    "v_mfma_f32_16x16x32_bf16 a[200:203], a[248:251], v[96:99], a[200:203]\n"
    "v_cvt_pk_bf16_f32 v243, v238, v239\n"
    "buffer_store_dwordx4 v[240:243], v5, s[60:63], s82 offen sc1\n"
    "s_add_u32 s82, s82, 4096\n"
    "v_mfma_f32_16x16x32_bf16 a[204:207], a[248:251], v[100:103], a[204:207]\n"
    "v_accvgpr_read_b32 v232, a176\n"
    "v_accvgpr_read_b32 v233, a177\n"
    "v_mfma_f32_16x16x32_bf16 a[208:211], a[248:251], v[104:107], a[208:211]\n"
    "v_accvgpr_read_b32 v234, a178\n"
    "v_accvgpr_read_b32 v235, a179\n"
    "v_mfma_f32_16x16x32_bf16 a[212:215], a[248:251], v[108:111], a[212:215]\n"
    "s_waitcnt lgkmcnt(2)\n"
    "v_cvt_pk_bf16_f32 v244, v232, v233\n"
    "v_cvt_pk_bf16_f32 v245, v234, v235\n"
    "v_mfma_f32_16x16x32_bf16 a[212:215], a[252:255], v[132:135], a[212:215]\n"
    "ds_read_b64_tr_b16 a[244:245], v0 offset:4096\n"
    "v_accvgpr_read_b32 v236, a180\n"
    "v_mfma_f32_16x16x32_bf16 a[208:211], a[252:255], v[128:131], a[208:211]\n"
    "ds_read_b64_tr_b16 a[246:247], v0 offset:6144\n"
    "v_accvgpr_read_b32 v237, a181\n"
    "v_mfma_f32_16x16x32_bf16 a[204:207], a[252:255], v[124:127], a[204:207]\n"
    "v_accvgpr_read_b32 v238, a182\n"
    "v_accvgpr_read_b32 v239, a183\n"
    "v_mfma_f32_16x16x32_bf16 a[200:203], a[252:255], v[120:123], a[200:203]\n"
    "s_cmp_eq_u32 s36, 6\n"
    "s_cselect_b32 s62, 0, s74\n"
    "v_cvt_pk_bf16_f32 v246, v236, v237\n"
    "v_mfma_f32_16x16x32_bf16 a[196:199], a[252:255], v[116:119], a[196:199]\n"
    "v_cvt_pk_bf16_f32 v247, v238, v239\n"
    "buffer_store_dwordx4 v[244:247], v5, s[60:63], s82 offen sc1\n"
    "s_add_u32 s82, s82, 4096\n"
    "v_mfma_f32_16x16x32_bf16 a[192:195], a[252:255], v[112:115], a[192:195]\n"
    "s_waitcnt lgkmcnt(2)\n"
    "v_accvgpr_read_b32 v232, a184\n"
    "v_accvgpr_read_b32 v233, a185\n"
    "v_mfma_f32_16x16x32_bf16 a[192:195], a[240:243], v[136:139], a[192:195]\n"
    "ds_read_b64_tr_b16 a[248:249], v0 offset:8192\n"
    "v_accvgpr_read_b32 v234, a186\n"
    "v_mfma_f32_16x16x32_bf16 a[196:199], a[240:243], v[140:143], a[196:199]\n"
    "ds_read_b64_tr_b16 a[250:251], v0 offset:10240\n"
    "v_accvgpr_read_b32 v235, a187\n"
    "v_cvt_pk_bf16_f32 v248, v232, v233\n"
    "v_mfma_f32_16x16x32_bf16 a[200:203], a[240:243], v[144:147], a[200:203]\n"
    "v_cvt_pk_bf16_f32 v249, v234, v235\n"
    "v_accvgpr_read_b32 v236, a188\n"
    "v_mfma_f32_16x16x32_bf16 a[204:207], a[240:243], v[148:151], a[204:207]\n"
    "v_accvgpr_read_b32 v237, a189\n"
    "v_accvgpr_read_b32 v238, a190\n"
    "v_mfma_f32_16x16x32_bf16 a[208:211], a[240:243], v[152:155], a[208:211]\n"
    "v_accvgpr_read_b32 v239, a191\n"
    "s_cmp_eq_u32 s36, 7\n"
    "s_cselect_b32 s62, 0, s74\n"
    "v_cvt_pk_bf16_f32 v250, v236, v237\n"
    "v_cvt_pk_bf16_f32 v251, v238, v239\n"
    "buffer_store_dwordx4 v[248:251], v5, s[60:63], s82 offen sc1\n"
    "s_add_u32 s82, s82, 4096\n"
    "v_mfma_f32_16x16x32_bf16 a[212:215], a[240:243], v[156:159], a[212:215]\n"
    "s_waitcnt lgkmcnt(2)\n"
    "buffer_load_dword v10, v7, s[92:95], 0 offen offset:512 sc1\n"
    "v_mfma_f32_16x16x32_bf16 a[212:215], a[244:247], v[180:183], a[212:215]\n"
    "ds_read_b64_tr_b16 a[252:253], v0 offset:12288\n"
    "v_mfma_f32_16x16x32_bf16 a[208:211], a[244:247], v[176:179], a[208:211]\n"
    "ds_read_b64_tr_b16 a[254:255], v0 offset:14336\n"
    "v_mfma_f32_16x16x32_bf16 a[204:207], a[244:247], v[172:175], a[204:207]\n"
    "v_mfma_f32_16x16x32_bf16 a[200:203], a[244:247], v[168:171], a[200:203]\n"
    "v_mfma_f32_16x16x32_bf16 a[196:199], a[244:247], v[164:167], a[196:199]\n"
    "v_mfma_f32_16x16x32_bf16 a[192:195], a[244:247], v[160:163], a[192:195]\n"
    "s_waitcnt vmcnt(11)\n"
    "s_barrier\n"
    "s_waitcnt lgkmcnt(2)\n"
    "v_mfma_f32_16x16x32_bf16 a[192:195], a[248:251], v[184:187], a[192:195]\n"
    "ds_read_b64_tr_b16 a[240:241], v0 offset:32768\n"
    "v_mfma_f32_16x16x32_bf16 a[196:199], a[248:251], v[188:191], a[196:199]\n"
    "ds_read_b64_tr_b16 a[242:243], v0 offset:34816\n"
    "v_mfma_f32_16x16x32_bf16 a[200:203], a[248:251], v[192:195], a[200:203]\n"
    "v_mfma_f32_16x16x32_bf16 a[204:207], a[248:251], v[196:199], a[204:207]\n"
    "v_mfma_f32_16x16x32_bf16 a[208:211], a[248:251], v[200:203], a[208:211]\n"
    "v_mfma_f32_16x16x32_bf16 a[212:215], a[248:251], v[204:207], a[212:215]\n"
    "s_waitcnt lgkmcnt(2)\n"
    "v_mfma_f32_16x16x32_bf16 a[212:215], a[252:255], v[228:231], a[212:215]\n"
    "ds_read_b64_tr_b16 a[244:245], v0 offset:36864\n"
    "v_mfma_f32_16x16x32_bf16 a[208:211], a[252:255], v[224:227], a[208:211]\n"
    "ds_read_b64_tr_b16 a[246:247], v0 offset:38912\n"
    "v_mfma_f32_16x16x32_bf16 a[204:207], a[252:255], v[220:223], a[204:207]\n"
    "v_mfma_f32_16x16x32_bf16 a[200:203], a[252:255], v[216:219], a[200:203]\n"
    "v_mfma_f32_16x16x32_bf16 a[196:199], a[252:255], v[212:215], a[196:199]\n"
    "v_mfma_f32_16x16x32_bf16 a[192:195], a[252:255], v[208:211], a[192:195]\n"
    "s_waitcnt lgkmcnt(2)\n"
    "v_mfma_f32_16x16x32_bf16 a[216:219], a[240:243], v[40:43], a[216:219]\n"
    "ds_read_b64_tr_b16 a[248:249], v0 offset:40960\n"
    "v_mfma_f32_16x16x32_bf16 a[220:223], a[240:243], v[44:47], a[220:223]\n"
    "ds_read_b64_tr_b16 a[250:251], v0 offset:43008\n"
    "v_mfma_f32_16x16x32_bf16 a[224:227], a[240:243], v[48:51], a[224:227]\n"
    "v_mfma_f32_16x16x32_bf16 a[228:231], a[240:243], v[52:55], a[228:231]\n"
    "v_mfma_f32_16x16x32_bf16 a[232:235], a[240:243], v[56:59], a[232:235]\n"
    "v_mfma_f32_16x16x32_bf16 a[236:239], a[240:243], v[60:63], a[236:239]\n"
    "s_waitcnt lgkmcnt(2)\n"
    "v_mfma_f32_16x16x32_bf16 a[236:239], a[244:247], v[84:87], a[236:239]\n"
    "ds_read_b64_tr_b16 a[252:253], v0 offset:45056\n"
    "v_accvgpr_read_b32 v232, a192\n"
    "v_mfma_f32_16x16x32_bf16 a[232:235], a[244:247], v[80:83], a[232:235]\n"
    "ds_read_b64_tr_b16 a[254:255], v0 offset:47104\n"
    "v_accvgpr_read_b32 v233, a193\n"
    "v_mfma_f32_16x16x32_bf16 a[228:231], a[244:247], v[76:79], a[228:231]\n"
    "v_accvgpr_read_b32 v234, a194\n"
    "v_accvgpr_read_b32 v235, a195\n"
    "v_mfma_f32_16x16x32_bf16 a[224:227], a[244:247], v[72:75], a[224:227]\n"
    "v_cvt_pk_bf16_f32 v240, v232, v233\n"
    "v_cvt_pk_bf16_f32 v241, v234, v235\n"
    "v_mfma_f32_16x16x32_bf16 a[220:223], a[244:247], v[68:71], a[220:223]\n"
    "v_accvgpr_read_b32 v236, a196\n"
    "v_accvgpr_read_b32 v237, a197\n"
    "v_mfma_f32_16x16x32_bf16 a[216:219], a[244:247], v[64:67], a[216:219]\n"
    "v_accvgpr_read_b32 v238, a198\n"
    "v_accvgpr_read_b32 v239, a199\n"
    "s_waitcnt vmcnt(7)\n"
    "s_barrier\n"
    "s_waitcnt lgkmcnt(2)\n"
    "v_mfma_f32_16x16x32_bf16 a[216:219], a[248:251], v[88:91], a[216:219]\n"
    "s_cmp_eq_u32 s36, 0\n"
    "s_cselect_b32 s62, 0, s74\n"
    "ds_read_b64_tr_b16 a[240:241], v11 offset:0\n"
    "v_mfma_f32_16x16x32_bf16 a[220:223], a[248:251], v[92:95], a[220:223]\n"
    "ds_read_b64_tr_b16 a[242:243], v11 offset:2048\n"
    "v_cvt_pk_bf16_f32 v242, v236, v237\n"
    "v_mfma_f32_16x16x32_bf16 a[224:227], a[248:251], v[96:99], a[224:227]\n"
    "v_cvt_pk_bf16_f32 v243, v238, v239\n"
    "buffer_store_dwordx4 v[240:243], v5, s[60:63], s82 offen sc1\n"
    "s_add_u32 s82, s82, 4096\n"
    "v_mfma_f32_16x16x32_bf16 a[228:231], a[248:251], v[100:103], a[228:231]\n"
    "v_accvgpr_read_b32 v232, a200\n"
    "v_accvgpr_read_b32 v233, a201\n"
    "v_mfma_f32_16x16x32_bf16 a[232:235], a[248:251], v[104:107], a[232:235]\n"
    "v_accvgpr_read_b32 v234, a202\n"
    "v_accvgpr_read_b32 v235, a203\n"
    "v_mfma_f32_16x16x32_bf16 a[236:239], a[248:251], v[108:111], a[236:239]\n"
    "s_waitcnt lgkmcnt(2)\n"
    "v_cvt_pk_bf16_f32 v244, v232, v233\n"
    "v_cvt_pk_bf16_f32 v245, v234, v235\n"
    "v_mfma_f32_16x16x32_bf16 a[236:239], a[252:255], v[132:135], a[236:239]\n"
    "ds_read_b64_tr_b16 a[244:245], v11 offset:4096\n"
    "v_accvgpr_read_b32 v236, a204\n"
    "v_mfma_f32_16x16x32_bf16 a[232:235], a[252:255], v[128:131], a[232:235]\n"
    "ds_read_b64_tr_b16 a[246:247], v11 offset:6144\n"
    "v_accvgpr_read_b32 v237, a205\n"
    "v_mfma_f32_16x16x32_bf16 a[228:231], a[252:255], v[124:127], a[228:231]\n"
    "v_accvgpr_read_b32 v238, a206\n"
    "v_accvgpr_read_b32 v239, a207\n"
    "v_mfma_f32_16x16x32_bf16 a[224:227], a[252:255], v[120:123], a[224:227]\n"
    "s_cmp_eq_u32 s36, 1\n"
    "s_cselect_b32 s62, 0, s74\n"
    "v_cvt_pk_bf16_f32 v246, v236, v237\n"
    "v_mfma_f32_16x16x32_bf16 a[220:223], a[252:255], v[116:119], a[220:223]\n"
    "v_cvt_pk_bf16_f32 v247, v238, v239\n"
    "buffer_store_dwordx4 v[244:247], v5, s[60:63], s82 offen sc1\n"
    "s_add_u32 s82, s82, 4096\n"
    "v_mfma_f32_16x16x32_bf16 a[216:219], a[252:255], v[112:115], a[216:219]\n"
    "s_waitcnt lgkmcnt(2)\n"
    "v_accvgpr_read_b32 v232, a208\n"
    "v_accvgpr_read_b32 v233, a209\n"
    "v_mfma_f32_16x16x32_bf16 a[216:219], a[240:243], v[136:139], a[216:219]\n"
    "ds_read_b64_tr_b16 a[248:249], v11 offset:8192\n"
    "v_accvgpr_read_b32 v234, a210\n"
    "v_mfma_f32_16x16x32_bf16 a[220:223], a[240:243], v[140:143], a[220:223]\n"
    "ds_read_b64_tr_b16 a[250:251], v11 offset:10240\n"
    "v_accvgpr_read_b32 v235, a211\n"
    "v_cvt_pk_bf16_f32 v248, v232, v233\n"
    "v_mfma_f32_16x16x32_bf16 a[224:227], a[240:243], v[144:147], a[224:227]\n"
    "v_cvt_pk_bf16_f32 v249, v234, v235\n"
    "v_accvgpr_read_b32 v236, a212\n"
    "v_mfma_f32_16x16x32_bf16 a[228:231], a[240:243], v[148:151], a[228:231]\n"
    "v_accvgpr_read_b32 v237, a213\n"
    "v_accvgpr_read_b32 v238, a214\n"
    "v_mfma_f32_16x16x32_bf16 a[232:235], a[240:243], v[152:155], a[232:235]\n"
    "v_accvgpr_read_b32 v239, a215\n"
    "s_cmp_eq_u32 s36, 2\n"
    "s_cselect_b32 s62, 0, s74\n"
    "v_cvt_pk_bf16_f32 v250, v236, v237\n"
    "v_cvt_pk_bf16_f32 v251, v238, v239\n"
    "buffer_store_dwordx4 v[248:251], v5, s[60:63], s82 offen sc1\n"
    "s_add_u32 s82, s82, 4096\n"
    "v_mfma_f32_16x16x32_bf16 a[236:239], a[240:243], v[156:159], a[236:239]\n"
    "s_waitcnt lgkmcnt(2)\n"
    "s_waitcnt vmcnt(15)\n"
    "v_mov_b32 v8, 6\n"
    "buffer_store_dword v8, v9, s[92:95], 0 offen offset:512 sc1\n"
    "v_mfma_f32_16x16x32_bf16 a[236:239], a[244:247], v[180:183], a[236:239]\n"
    "ds_read_b64_tr_b16 a[252:253], v11 offset:12288\n"
    "v_mfma_f32_16x16x32_bf16 a[232:235], a[244:247], v[176:179], a[232:235]\n"
    "ds_read_b64_tr_b16 a[254:255], v11 offset:14336\n"
    "v_mfma_f32_16x16x32_bf16 a[228:231], a[244:247], v[172:175], a[228:231]\n"
    "v_mfma_f32_16x16x32_bf16 a[224:227], a[244:247], v[168:171], a[224:227]\n"
    "v_mfma_f32_16x16x32_bf16 a[220:223], a[244:247], v[164:167], a[220:223]\n"
    "v_mfma_f32_16x16x32_bf16 a[216:219], a[244:247], v[160:163], a[216:219]\n"
    "s_waitcnt lgkmcnt(2)\n"
    "v_mfma_f32_16x16x32_bf16 a[216:219], a[248:251], v[184:187], a[216:219]\n"
    "v_mfma_f32_16x16x32_bf16 a[220:223], a[248:251], v[188:191], a[220:223]\n"
    "v_mfma_f32_16x16x32_bf16 a[224:227], a[248:251], v[192:195], a[224:227]\n"
    "v_mfma_f32_16x16x32_bf16 a[228:231], a[248:251], v[196:199], a[228:231]\n"
    "v_mfma_f32_16x16x32_bf16 a[232:235], a[248:251], v[200:203], a[232:235]\n"
    "v_mfma_f32_16x16x32_bf16 a[236:239], a[248:251], v[204:207], a[236:239]\n"
    "s_waitcnt lgkmcnt(0)\n"
    "v_mfma_f32_16x16x32_bf16 a[236:239], a[252:255], v[228:231], a[236:239]\n"
    "v_mfma_f32_16x16x32_bf16 a[232:235], a[252:255], v[224:227], a[232:235]\n"
    "v_mfma_f32_16x16x32_bf16 a[228:231], a[252:255], v[220:223], a[228:231]\n"
    "v_mfma_f32_16x16x32_bf16 a[224:227], a[252:255], v[216:219], a[224:227]\n"
    "v_mfma_f32_16x16x32_bf16 a[220:223], a[252:255], v[212:215], a[220:223]\n"
    "v_mfma_f32_16x16x32_bf16 a[216:219], a[252:255], v[208:211], a[216:219]\n"
    "s_nop 7\n"
    "v_accvgpr_read_b32 v232, a216\n"
    "v_accvgpr_read_b32 v233, a217\n"
    "v_accvgpr_read_b32 v234, a218\n"
    "v_accvgpr_read_b32 v235, a219\n"
    "v_cvt_pk_bf16_f32 v240, v232, v233\n"
    "v_cvt_pk_bf16_f32 v241, v234, v235\n"
    "v_accvgpr_read_b32 v236, a220\n"
    "v_accvgpr_read_b32 v237, a221\n"
    "v_accvgpr_read_b32 v238, a222\n"
    "v_accvgpr_read_b32 v239, a223\n"
    "s_cmp_eq_u32 s36, 3\n"
    "s_cselect_b32 s62, 0, s74\n"
    "v_cvt_pk_bf16_f32 v242, v236, v237\n"
    "v_cvt_pk_bf16_f32 v243, v238, v239\n"
    "buffer_store_dwordx4 v[240:243], v5, s[60:63], s82 offen sc1\n"
    "s_add_u32 s82, s82, 4096\n"
    "v_accvgpr_read_b32 v232, a224\n"
    "v_accvgpr_read_b32 v233, a225\n"
    "v_accvgpr_read_b32 v234, a226\n"
    "v_accvgpr_read_b32 v235, a227\n"
    "v_cvt_pk_bf16_f32 v244, v232, v233\n"
    "v_cvt_pk_bf16_f32 v245, v234, v235\n"
    "v_accvgpr_read_b32 v236, a228\n"
    "v_accvgpr_read_b32 v237, a229\n"
    "v_accvgpr_read_b32 v238, a230\n"
    "v_accvgpr_read_b32 v239, a231\n"
    "s_cmp_eq_u32 s36, 4\n"
    "s_cselect_b32 s62, 0, s74\n"
    "v_cvt_pk_bf16_f32 v246, v236, v237\n"
    "v_cvt_pk_bf16_f32 v247, v238, v239\n"
    "buffer_store_dwordx4 v[244:247], v5, s[60:63], s82 offen sc1\n"
    "s_add_u32 s82, s82, 4096\n"
    "v_accvgpr_read_b32 v232, a232\n"
    "v_accvgpr_read_b32 v233, a233\n"
    "v_accvgpr_read_b32 v234, a234\n"
    "v_accvgpr_read_b32 v235, a235\n"
    "v_cvt_pk_bf16_f32 v248, v232, v233\n"
    "v_cvt_pk_bf16_f32 v249, v234, v235\n"
    "v_accvgpr_read_b32 v236, a236\n"
    "v_accvgpr_read_b32 v237, a237\n"
    "v_accvgpr_read_b32 v238, a238\n"
    "v_accvgpr_read_b32 v239, a239\n"
    "s_cmp_eq_u32 s36, 5\n"
    "s_cselect_b32 s62, 0, s74\n"
    "v_cvt_pk_bf16_f32 v250, v236, v237\n"
    "v_cvt_pk_bf16_f32 v251, v238, v239\n"
    "buffer_store_dwordx4 v[248:251], v5, s[60:63], s82 offen sc1\n"
    "s_add_u32 s82, s82, 4096\n"
    "v_mov_b32 v8, 10\n"
    "s_waitcnt vmcnt(7)\n"
    "L_w0_%=:\n"
    "v_cmp_ge_u32 vcc, s70, v10\n"
    "s_and_b64 vcc, vcc, s[54:55]\n"
    "s_cbranch_vccz L_g0_%=\n"
    "buffer_load_dword v10, v7, s[92:95], 0 offen offset:512 sc1\n"
    "s_cmp_lg_u32 s52, 0\n"
    "s_cbranch_scc1 L_p0_%=\n"
    "s_waitcnt vmcnt(1)\n"
    "buffer_store_dword v8, v9, s[92:95], 0 offen offset:512 sc1\n"
    "s_mov_b32 s52, 1\n"
    "s_swappc_b64 s[90:91], s[88:89]\n"
    "s_waitcnt vmcnt(1)\n"
    "s_branch L_w0_%=\n"
    "L_p0_%=:\n"
    "s_add_u32 s37, s37, 1\n"
    "s_cmp_gt_u32 s37, 400000\n"
    "s_cbranch_scc1 L_end_%=\n"
    "s_waitcnt vmcnt(0)\n"
    "s_branch L_w0_%=\n"
    "L_g0_%=:\n"
    "s_add_u32 s69, s48, 1015808\n"
    "s_mul_i32 s67, s36, 126976\n"
    "s_add_u32 s67, s67, s48\n"
    "s_add_u32 s67, s67, 126976\n"
    "s_cmp_eq_u32 s67, s69\n"
    "s_cselect_b32 s67, s48, s67\n"
    "buffer_load_dwordx4 v[40:43], v5, s[44:47], s67 offen sc1\n"
    "s_add_u32 s67, s67, 126976\n"
    "s_cmp_eq_u32 s67, s69\n"
    "s_cselect_b32 s67, s48, s67\n"
    "buffer_load_dwordx4 v[44:47], v5, s[44:47], s67 offen sc1\n"
    "s_add_u32 s67, s67, 126976\n"
    "s_cmp_eq_u32 s67, s69\n"
    "s_cselect_b32 s67, s48, s67\n"
    "buffer_load_dwordx4 v[48:51], v5, s[44:47], s67 offen sc1\n"
    "s_add_u32 s67, s67, 126976\n"
    "s_cmp_eq_u32 s67, s69\n"
    "s_cselect_b32 s67, s48, s67\n"
    "buffer_load_dwordx4 v[52:55], v5, s[44:47], s67 offen sc1\n"
    "s_add_u32 s67, s67, 126976\n"
    "s_cmp_eq_u32 s67, s69\n"
    "s_cselect_b32 s67, s48, s67\n"
    "buffer_load_dwordx4 v[56:59], v5, s[44:47], s67 offen sc1\n"
    "s_add_u32 s67, s67, 126976\n"
    "s_cmp_eq_u32 s67, s69\n"
    "s_cselect_b32 s67, s48, s67\n"
    "buffer_load_dwordx4 v[60:63], v5, s[44:47], s67 offen sc1\n"
    "s_add_u32 s67, s67, 126976\n"
    "s_cmp_eq_u32 s67, s69\n"
    "s_cselect_b32 s67, s48, s67\n"
    "buffer_load_dwordx4 v[64:67], v5, s[44:47], s67 offen sc1\n"
    "L_w1_%=:\n"
    "v_cmp_ge_u32 vcc, s73, v10\n"
    "s_and_b64 vcc, vcc, s[54:55]\n"
    "s_cbranch_vccz L_g1_%=\n"
    "buffer_load_dword v10, v7, s[92:95], 0 offen offset:512 sc1\n"
    "s_cmp_lg_u32 s52, 0\n"
    "s_cbranch_scc1 L_p1_%=\n"
    "s_waitcnt vmcnt(8)\n"
    "buffer_store_dword v8, v9, s[92:95], 0 offen offset:512 sc1\n"
    "s_mov_b32 s52, 1\n"
    "s_swappc_b64 s[90:91], s[88:89]\n"
    "s_waitcnt vmcnt(1)\n"
    "s_branch L_w1_%=\n"
    "L_p1_%=:\n"
    "s_add_u32 s37, s37, 1\n"
    "s_cmp_gt_u32 s37, 400000\n"
    "s_cbranch_scc1 L_end_%=\n"
    "s_waitcnt vmcnt(0)\n"
    "s_branch L_w1_%=\n"
    "L_g1_%=:\n"
    "s_add_u32 s69, s49, 1015808\n"
    "s_mul_i32 s67, s36, 126976\n"
    "s_add_u32 s67, s67, s49\n"
    "s_add_u32 s67, s67, 126976\n"
    "s_cmp_eq_u32 s67, s69\n"
    "s_cselect_b32 s67, s49, s67\n"
    "buffer_load_dwordx4 v[72:75], v5, s[44:47], s67 offen sc1\n"
    "s_add_u32 s67, s67, 126976\n"
    "s_cmp_eq_u32 s67, s69\n"
    "s_cselect_b32 s67, s49, s67\n"
    "buffer_load_dwordx4 v[76:79], v5, s[44:47], s67 offen sc1\n"
    "s_add_u32 s67, s67, 126976\n"
    "s_cmp_eq_u32 s67, s69\n"
    "s_cselect_b32 s67, s49, s67\n"
    "buffer_load_dwordx4 v[80:83], v5, s[44:47], s67 offen sc1\n"
    "s_add_u32 s67, s67, 126976\n"
    "s_cmp_eq_u32 s67, s69\n"
    "s_cselect_b32 s67, s49, s67\n"
    "buffer_load_dwordx4 v[84:87], v5, s[44:47], s67 offen sc1\n"
    "s_add_u32 s67, s67, 126976\n"
    "s_cmp_eq_u32 s67, s69\n"
    "s_cselect_b32 s67, s49, s67\n"
    "buffer_load_dwordx4 v[88:91], v5, s[44:47], s67 offen sc1\n"
    "s_add_u32 s67, s67, 126976\n"
    "s_cmp_eq_u32 s67, s69\n"
    "s_cselect_b32 s67, s49, s67\n"
    "buffer_load_dwordx4 v[92:95], v5, s[44:47], s67 offen sc1\n"
    "s_add_u32 s67, s67, 126976\n"
    "s_cmp_eq_u32 s67, s69\n"
    "s_cselect_b32 s67, s49, s67\n"
    "buffer_load_dwordx4 v[96:99], v5, s[44:47], s67 offen sc1\n"
    "L_w2_%=:\n"
    "v_cmp_ge_u32 vcc, s81, v10\n"
    "s_and_b64 vcc, vcc, s[54:55]\n"
    "s_cbranch_vccz L_g2_%=\n"
    "buffer_load_dword v10, v7, s[92:95], 0 offen offset:512 sc1\n"
    "s_cmp_lg_u32 s52, 0\n"
    "s_cbranch_scc1 L_p2_%=\n"
    "s_waitcnt vmcnt(15)\n"
    "buffer_store_dword v8, v9, s[92:95], 0 offen offset:512 sc1\n"
    "s_mov_b32 s52, 1\n"
    "s_swappc_b64 s[90:91], s[88:89]\n"
    "s_waitcnt vmcnt(1)\n"
    "s_branch L_w2_%=\n"
    "L_p2_%=:\n"
    "s_add_u32 s37, s37, 1\n"
    "s_cmp_gt_u32 s37, 400000\n"
    "s_cbranch_scc1 L_end_%=\n"
    "s_waitcnt vmcnt(0)\n"
    "s_branch L_w2_%=\n"
    "L_g2_%=:\n"
    "s_add_u32 s69, s50, 1015808\n"
    "s_mul_i32 s67, s36, 126976\n"
    "s_add_u32 s67, s67, s50\n"
    "s_add_u32 s67, s67, 126976\n"
    "s_cmp_eq_u32 s67, s69\n"
    "s_cselect_b32 s67, s50, s67\n"
    "buffer_load_dwordx4 v[104:107], v5, s[44:47], s67 offen sc1\n"
    "s_add_u32 s67, s67, 126976\n"
    "s_cmp_eq_u32 s67, s69\n"
    "s_cselect_b32 s67, s50, s67\n"
    "buffer_load_dwordx4 v[108:111], v5, s[44:47], s67 offen sc1\n"
    "s_add_u32 s67, s67, 126976\n"
    "s_cmp_eq_u32 s67, s69\n"
    "s_cselect_b32 s67, s50, s67\n"
    "buffer_load_dwordx4 v[112:115], v5, s[44:47], s67 offen sc1\n"
    "s_add_u32 s67, s67, 126976\n"
    "s_cmp_eq_u32 s67, s69\n"
    "s_cselect_b32 s67, s50, s67\n"
    "buffer_load_dwordx4 v[116:119], v5, s[44:47], s67 offen sc1\n"
    "s_add_u32 s67, s67, 126976\n"
    "s_cmp_eq_u32 s67, s69\n"
    "s_cselect_b32 s67, s50, s67\n"
    "buffer_load_dwordx4 v[120:123], v5, s[44:47], s67 offen sc1\n"
    "s_add_u32 s67, s67, 126976\n"
    "s_cmp_eq_u32 s67, s69\n"
    "s_cselect_b32 s67, s50, s67\n"
    "buffer_load_dwordx4 v[124:127], v5, s[44:47], s67 offen sc1\n"
    "s_add_u32 s67, s67, 126976\n"
    "s_cmp_eq_u32 s67, s69\n"
    "s_cselect_b32 s67, s50, s67\n"
    "buffer_load_dwordx4 v[128:131], v5, s[44:47], s67 offen sc1\n"
    "s_cmp_ge_u32 s36, 6\n"
    "s_cbranch_scc1 L_k3_%=\n"
    "L_w3_%=:\n"
    "v_cmp_ge_u32 vcc, s85, v10\n"
    "s_and_b64 vcc, vcc, s[54:55]\n"
    "s_cbranch_vccz L_g3_%=\n"
    "buffer_load_dword v10, v7, s[92:95], 0 offen offset:512 sc1\n"
    "s_cmp_lg_u32 s52, 0\n"
    "s_cbranch_scc1 L_p3_%=\n"
    "s_waitcnt vmcnt(22)\n"
    "buffer_store_dword v8, v9, s[92:95], 0 offen offset:512 sc1\n"
    "s_mov_b32 s52, 1\n"
    "s_swappc_b64 s[90:91], s[88:89]\n"
    "s_waitcnt vmcnt(1)\n"
    "s_branch L_w3_%=\n"
    "L_p3_%=:\n"
    "s_add_u32 s37, s37, 1\n"
    "s_cmp_gt_u32 s37, 400000\n"
    "s_cbranch_scc1 L_end_%=\n"
    "s_waitcnt vmcnt(0)\n"
    "s_branch L_w3_%=\n"
    "L_g3_%=:\n"
    "s_add_u32 s69, s51, 1015808\n"
    "s_mul_i32 s67, s36, 126976\n"
    "s_add_u32 s67, s67, s51\n"
    "s_add_u32 s67, s67, 126976\n"
    "s_cmp_eq_u32 s67, s69\n"
    "s_cselect_b32 s67, s51, s67\n"
    "buffer_load_dwordx4 v[136:139], v5, s[44:47], s67 offen sc1\n"
    "s_add_u32 s67, s67, 126976\n"
    "s_cmp_eq_u32 s67, s69\n"
    "s_cselect_b32 s67, s51, s67\n"
    "buffer_load_dwordx4 v[140:143], v5, s[44:47], s67 offen sc1\n"
    "s_add_u32 s67, s67, 126976\n"
    "s_cmp_eq_u32 s67, s69\n"
    "s_cselect_b32 s67, s51, s67\n"
    "buffer_load_dwordx4 v[144:147], v5, s[44:47], s67 offen sc1\n"
    "s_add_u32 s67, s67, 126976\n"
    "s_cmp_eq_u32 s67, s69\n"
    "s_cselect_b32 s67, s51, s67\n"
    "buffer_load_dwordx4 v[148:151], v5, s[44:47], s67 offen sc1\n"
    "s_add_u32 s67, s67, 126976\n"
    "s_cmp_eq_u32 s67, s69\n"
    "s_cselect_b32 s67, s51, s67\n"
    "buffer_load_dwordx4 v[152:155], v5, s[44:47], s67 offen sc1\n"
    "s_add_u32 s67, s67, 126976\n"
    "s_cmp_eq_u32 s67, s69\n"
    "s_cselect_b32 s67, s51, s67\n"
    "buffer_load_dwordx4 v[156:159], v5, s[44:47], s67 offen sc1\n"
    "s_add_u32 s67, s67, 126976\n"
    "s_cmp_eq_u32 s67, s69\n"
    "s_cselect_b32 s67, s51, s67\n"
    "buffer_load_dwordx4 v[160:163], v5, s[44:47], s67 offen sc1\n"
    "s_cmp_lg_u32 s52, 0\n"
    "s_cbranch_scc1 L_q_%=\n"
    "s_waitcnt vmcnt(28)\n"
    "buffer_store_dword v8, v9, s[92:95], 0 offen offset:512 sc1\n"
    "s_mov_b32 s52, 1\n"
    "s_swappc_b64 s[90:91], s[88:89]\n"
    "L_q_%=:\n"
    "s_waitcnt vmcnt(14)\n"
    "s_branch L_s0_%=\n"
    "L_k3_%=:\n"
    "s_cmp_lg_u32 s52, 0\n"
    "s_cbranch_scc1 L_q3_%=\n"
    "s_waitcnt vmcnt(21)\n"
    "buffer_store_dword v8, v9, s[92:95], 0 offen offset:512 sc1\n"
    "s_mov_b32 s52, 1\n"
    "s_swappc_b64 s[90:91], s[88:89]\n"
    "L_q3_%=:\n"
    "s_waitcnt vmcnt(7)\n"
    "L_s0_%=:\n"
    "v_lshlrev_b32 v200, 16, v40\n"
    "v_and_b32 v201, s72, v40\n"
    "v_lshlrev_b32 v202, 16, v41\n"
    "v_and_b32 v203, s72, v41\n"
    "v_lshlrev_b32 v204, 16, v42\n"
    "v_and_b32 v205, s72, v42\n"
    "v_lshlrev_b32 v206, 16, v43\n"
    "v_and_b32 v207, s72, v43\n"
    "v_pk_add_f32 v[168:169], v[168:169], v[200:201]\n"
    "v_pk_add_f32 v[170:171], v[170:171], v[202:203]\n"
    "v_pk_add_f32 v[172:173], v[172:173], v[204:205]\n"
    "v_pk_add_f32 v[174:175], v[174:175], v[206:207]\n"
    "v_lshlrev_b32 v200, 16, v44\n"
    "v_and_b32 v201, s72, v44\n"
    "v_lshlrev_b32 v202, 16, v45\n"
    "v_and_b32 v203, s72, v45\n"
    "v_lshlrev_b32 v204, 16, v46\n"
    "v_and_b32 v205, s72, v46\n"
    "v_lshlrev_b32 v206, 16, v47\n"
    "v_and_b32 v207, s72, v47\n"
    "v_pk_add_f32 v[168:169], v[168:169], v[200:201]\n"
    "v_pk_add_f32 v[170:171], v[170:171], v[202:203]\n"
    "v_pk_add_f32 v[172:173], v[172:173], v[204:205]\n"
    "v_pk_add_f32 v[174:175], v[174:175], v[206:207]\n"
    "v_lshlrev_b32 v200, 16, v48\n"
    "v_and_b32 v201, s72, v48\n"
    "v_lshlrev_b32 v202, 16, v49\n"
    "v_and_b32 v203, s72, v49\n"
    "v_lshlrev_b32 v204, 16, v50\n"
    "v_and_b32 v205, s72, v50\n"
    "v_lshlrev_b32 v206, 16, v51\n"
    "v_and_b32 v207, s72, v51\n"
    "v_pk_add_f32 v[168:169], v[168:169], v[200:201]\n"
    "v_pk_add_f32 v[170:171], v[170:171], v[202:203]\n"
    "v_pk_add_f32 v[172:173], v[172:173], v[204:205]\n"
    "v_pk_add_f32 v[174:175], v[174:175], v[206:207]\n"
    "v_lshlrev_b32 v200, 16, v52\n"
    "v_and_b32 v201, s72, v52\n"
    "v_lshlrev_b32 v202, 16, v53\n"
    "v_and_b32 v203, s72, v53\n"
    "v_lshlrev_b32 v204, 16, v54\n"
    "v_and_b32 v205, s72, v54\n"
    "v_lshlrev_b32 v206, 16, v55\n"
    "v_and_b32 v207, s72, v55\n"
    "v_pk_add_f32 v[168:169], v[168:169], v[200:201]\n"
    "v_pk_add_f32 v[170:171], v[170:171], v[202:203]\n"
    "v_pk_add_f32 v[172:173], v[172:173], v[204:205]\n"
    "v_pk_add_f32 v[174:175], v[174:175], v[206:207]\n"
    "v_lshlrev_b32 v200, 16, v56\n"
    "v_and_b32 v201, s72, v56\n"
    "v_lshlrev_b32 v202, 16, v57\n"
    "v_and_b32 v203, s72, v57\n"
    "v_lshlrev_b32 v204, 16, v58\n"
    "v_and_b32 v205, s72, v58\n"
    "v_lshlrev_b32 v206, 16, v59\n"
    "v_and_b32 v207, s72, v59\n"
    "v_pk_add_f32 v[168:169], v[168:169], v[200:201]\n"
    "v_pk_add_f32 v[170:171], v[170:171], v[202:203]\n"
    "v_pk_add_f32 v[172:173], v[172:173], v[204:205]\n"
    "v_pk_add_f32 v[174:175], v[174:175], v[206:207]\n"
    "v_lshlrev_b32 v200, 16, v60\n"
    "v_and_b32 v201, s72, v60\n"
    "v_lshlrev_b32 v202, 16, v61\n"
    "v_and_b32 v203, s72, v61\n"
    "v_lshlrev_b32 v204, 16, v62\n"
    "v_and_b32 v205, s72, v62\n"
    "v_lshlrev_b32 v206, 16, v63\n"
    "v_and_b32 v207, s72, v63\n"
    "v_pk_add_f32 v[168:169], v[168:169], v[200:201]\n"
    "v_pk_add_f32 v[170:171], v[170:171], v[202:203]\n"
    "v_pk_add_f32 v[172:173], v[172:173], v[204:205]\n"
    "v_pk_add_f32 v[174:175], v[174:175], v[206:207]\n"
    "v_lshlrev_b32 v200, 16, v64\n"
    "v_and_b32 v201, s72, v64\n"
    "v_lshlrev_b32 v202, 16, v65\n"
    "v_and_b32 v203, s72, v65\n"
    "v_lshlrev_b32 v204, 16, v66\n"
    "v_and_b32 v205, s72, v66\n"
    "v_lshlrev_b32 v206, 16, v67\n"
    "v_and_b32 v207, s72, v67\n"
    "v_pk_add_f32 v[168:169], v[168:169], v[200:201]\n"
    "v_pk_add_f32 v[170:171], v[170:171], v[202:203]\n"
    "v_pk_add_f32 v[172:173], v[172:173], v[204:205]\n"
    "v_pk_add_f32 v[174:175], v[174:175], v[206:207]\n"
    "v_cvt_pk_bf16_f32 v168, v168, v169\n"
    "v_cvt_pk_bf16_f32 v169, v170, v171\n"
    "v_cvt_pk_bf16_f32 v170, v172, v173\n"
    "v_cvt_pk_bf16_f32 v171, v174, v175\n"
    "s_nop 1\n"
    "v_permlane16_swap_b32 v168, v170\n"
    "v_permlane16_swap_b32 v169, v171\n"
    "v_lshlrev_b32 v200, 16, v72\n"
    "v_and_b32 v201, s72, v72\n"
    "v_lshlrev_b32 v202, 16, v73\n"
    "v_and_b32 v203, s72, v73\n"
    "v_lshlrev_b32 v204, 16, v74\n"
    "v_and_b32 v205, s72, v74\n"
    "v_lshlrev_b32 v206, 16, v75\n"
    "v_and_b32 v207, s72, v75\n"
    "v_pk_add_f32 v[176:177], v[176:177], v[200:201]\n"
    "v_pk_add_f32 v[178:179], v[178:179], v[202:203]\n"
    "v_pk_add_f32 v[180:181], v[180:181], v[204:205]\n"
    "v_pk_add_f32 v[182:183], v[182:183], v[206:207]\n"
    "v_lshlrev_b32 v200, 16, v76\n"
    "v_and_b32 v201, s72, v76\n"
    "v_lshlrev_b32 v202, 16, v77\n"
    "v_and_b32 v203, s72, v77\n"
    "v_lshlrev_b32 v204, 16, v78\n"
    "v_and_b32 v205, s72, v78\n"
    "v_lshlrev_b32 v206, 16, v79\n"
    "v_and_b32 v207, s72, v79\n"
    "v_pk_add_f32 v[176:177], v[176:177], v[200:201]\n"
    "v_pk_add_f32 v[178:179], v[178:179], v[202:203]\n"
    "v_pk_add_f32 v[180:181], v[180:181], v[204:205]\n"
    "v_pk_add_f32 v[182:183], v[182:183], v[206:207]\n"
    "v_lshlrev_b32 v200, 16, v80\n"
    "v_and_b32 v201, s72, v80\n"
    "v_lshlrev_b32 v202, 16, v81\n"
    "v_and_b32 v203, s72, v81\n"
    "v_lshlrev_b32 v204, 16, v82\n"
    "v_and_b32 v205, s72, v82\n"
    "v_lshlrev_b32 v206, 16, v83\n"
    "v_and_b32 v207, s72, v83\n"
    "v_pk_add_f32 v[176:177], v[176:177], v[200:201]\n"
    "v_pk_add_f32 v[178:179], v[178:179], v[202:203]\n"
    "v_pk_add_f32 v[180:181], v[180:181], v[204:205]\n"
    "v_pk_add_f32 v[182:183], v[182:183], v[206:207]\n"
    "v_lshlrev_b32 v200, 16, v84\n"
    "v_and_b32 v201, s72, v84\n"
    "v_lshlrev_b32 v202, 16, v85\n"
    "v_and_b32 v203, s72, v85\n"
    "v_lshlrev_b32 v204, 16, v86\n"
    "v_and_b32 v205, s72, v86\n"
    "v_lshlrev_b32 v206, 16, v87\n"
    "v_and_b32 v207, s72, v87\n"
    "v_pk_add_f32 v[176:177], v[176:177], v[200:201]\n"
    "v_pk_add_f32 v[178:179], v[178:179], v[202:203]\n"
    "v_pk_add_f32 v[180:181], v[180:181], v[204:205]\n"
    "v_pk_add_f32 v[182:183], v[182:183], v[206:207]\n"
    "v_lshlrev_b32 v200, 16, v88\n"
    "v_and_b32 v201, s72, v88\n"
    "v_lshlrev_b32 v202, 16, v89\n"
    "v_and_b32 v203, s72, v89\n"
    "v_lshlrev_b32 v204, 16, v90\n"
    "v_and_b32 v205, s72, v90\n"
    "v_lshlrev_b32 v206, 16, v91\n"
    "v_and_b32 v207, s72, v91\n"
    "v_pk_add_f32 v[176:177], v[176:177], v[200:201]\n"
    "v_pk_add_f32 v[178:179], v[178:179], v[202:203]\n"
    "v_pk_add_f32 v[180:181], v[180:181], v[204:205]\n"
    "v_pk_add_f32 v[182:183], v[182:183], v[206:207]\n"
    "v_lshlrev_b32 v200, 16, v92\n"
    "v_and_b32 v201, s72, v92\n"
    "v_lshlrev_b32 v202, 16, v93\n"
    "v_and_b32 v203, s72, v93\n"
    "v_lshlrev_b32 v204, 16, v94\n"
    "v_and_b32 v205, s72, v94\n"
    "v_lshlrev_b32 v206, 16, v95\n"
    "v_and_b32 v207, s72, v95\n"
    "v_pk_add_f32 v[176:177], v[176:177], v[200:201]\n"
    "v_pk_add_f32 v[178:179], v[178:179], v[202:203]\n"
    "v_pk_add_f32 v[180:181], v[180:181], v[204:205]\n"
    "v_pk_add_f32 v[182:183], v[182:183], v[206:207]\n"
    "v_lshlrev_b32 v200, 16, v96\n"
    "v_and_b32 v201, s72, v96\n"
    "v_lshlrev_b32 v202, 16, v97\n"
    "v_and_b32 v203, s72, v97\n"
    "v_lshlrev_b32 v204, 16, v98\n"
    "v_and_b32 v205, s72, v98\n"
    "v_lshlrev_b32 v206, 16, v99\n"
    "v_and_b32 v207, s72, v99\n"
    "v_pk_add_f32 v[176:177], v[176:177], v[200:201]\n"
    "v_pk_add_f32 v[178:179], v[178:179], v[202:203]\n"
    "v_pk_add_f32 v[180:181], v[180:181], v[204:205]\n"
    "v_pk_add_f32 v[182:183], v[182:183], v[206:207]\n"
    "v_cvt_pk_bf16_f32 v176, v176, v177\n"
    "v_cvt_pk_bf16_f32 v177, v178, v179\n"
    "v_cvt_pk_bf16_f32 v178, v180, v181\n"
    "v_cvt_pk_bf16_f32 v179, v182, v183\n"
    "s_nop 1\n"
    "v_permlane16_swap_b32 v176, v178\n"
    "v_permlane16_swap_b32 v177, v179\n"
    "s_waitcnt vmcnt(7)\n"
    "s_cmp_lt_u32 s36, 6\n"
    "s_cbranch_scc1 L_s2_%=\n"
    "s_waitcnt vmcnt(0)\n"
    "L_s2_%=:\n"
    "v_lshlrev_b32 v200, 16, v104\n"
    "v_and_b32 v201, s72, v104\n"
    "v_lshlrev_b32 v202, 16, v105\n"
    "v_and_b32 v203, s72, v105\n"
    "v_lshlrev_b32 v204, 16, v106\n"
    "v_and_b32 v205, s72, v106\n"
    "v_lshlrev_b32 v206, 16, v107\n"
    "v_and_b32 v207, s72, v107\n"
    "v_pk_add_f32 v[184:185], v[184:185], v[200:201]\n"
    "v_pk_add_f32 v[186:187], v[186:187], v[202:203]\n"
    "v_pk_add_f32 v[188:189], v[188:189], v[204:205]\n"
    "v_pk_add_f32 v[190:191], v[190:191], v[206:207]\n"
    "v_lshlrev_b32 v200, 16, v108\n"
    "v_and_b32 v201, s72, v108\n"
    "v_lshlrev_b32 v202, 16, v109\n"
    "v_and_b32 v203, s72, v109\n"
    "v_lshlrev_b32 v204, 16, v110\n"
    "v_and_b32 v205, s72, v110\n"
    "v_lshlrev_b32 v206, 16, v111\n"
    "v_and_b32 v207, s72, v111\n"
    "v_pk_add_f32 v[184:185], v[184:185], v[200:201]\n"
    "v_pk_add_f32 v[186:187], v[186:187], v[202:203]\n"
    "v_pk_add_f32 v[188:189], v[188:189], v[204:205]\n"
    "v_pk_add_f32 v[190:191], v[190:191], v[206:207]\n"
    "v_lshlrev_b32 v200, 16, v112\n"
    "v_and_b32 v201, s72, v112\n"
    "v_lshlrev_b32 v202, 16, v113\n"
    "v_and_b32 v203, s72, v113\n"
    "v_lshlrev_b32 v204, 16, v114\n"
    "v_and_b32 v205, s72, v114\n"
    "v_lshlrev_b32 v206, 16, v115\n"
    "v_and_b32 v207, s72, v115\n"
    "v_pk_add_f32 v[184:185], v[184:185], v[200:201]\n"
    "v_pk_add_f32 v[186:187], v[186:187], v[202:203]\n"
    "v_pk_add_f32 v[188:189], v[188:189], v[204:205]\n"
    "v_pk_add_f32 v[190:191], v[190:191], v[206:207]\n"
    "v_lshlrev_b32 v200, 16, v116\n"
    "v_and_b32 v201, s72, v116\n"
    "v_lshlrev_b32 v202, 16, v117\n"
    "v_and_b32 v203, s72, v117\n"
    "v_lshlrev_b32 v204, 16, v118\n"
    "v_and_b32 v205, s72, v118\n"
    "v_lshlrev_b32 v206, 16, v119\n"
    "v_and_b32 v207, s72, v119\n"
    "v_pk_add_f32 v[184:185], v[184:185], v[200:201]\n"
    "v_pk_add_f32 v[186:187], v[186:187], v[202:203]\n"
    "v_pk_add_f32 v[188:189], v[188:189], v[204:205]\n"
    "v_pk_add_f32 v[190:191], v[190:191], v[206:207]\n"
    "v_lshlrev_b32 v200, 16, v120\n"
    "v_and_b32 v201, s72, v120\n"
    "v_lshlrev_b32 v202, 16, v121\n"
    "v_and_b32 v203, s72, v121\n"
    "v_lshlrev_b32 v204, 16, v122\n"
    "v_and_b32 v205, s72, v122\n"
    "v_lshlrev_b32 v206, 16, v123\n"
    "v_and_b32 v207, s72, v123\n"
    "v_pk_add_f32 v[184:185], v[184:185], v[200:201]\n"
    "v_pk_add_f32 v[186:187], v[186:187], v[202:203]\n"
    "v_pk_add_f32 v[188:189], v[188:189], v[204:205]\n"
    "v_pk_add_f32 v[190:191], v[190:191], v[206:207]\n"
    "v_lshlrev_b32 v200, 16, v124\n"
    "v_and_b32 v201, s72, v124\n"
    "v_lshlrev_b32 v202, 16, v125\n"
    "v_and_b32 v203, s72, v125\n"
    "v_lshlrev_b32 v204, 16, v126\n"
    "v_and_b32 v205, s72, v126\n"
    "v_lshlrev_b32 v206, 16, v127\n"
    "v_and_b32 v207, s72, v127\n"
    "v_pk_add_f32 v[184:185], v[184:185], v[200:201]\n"
    "v_pk_add_f32 v[186:187], v[186:187], v[202:203]\n"
    "v_pk_add_f32 v[188:189], v[188:189], v[204:205]\n"
    "v_pk_add_f32 v[190:191], v[190:191], v[206:207]\n"
    "v_lshlrev_b32 v200, 16, v128\n"
    "v_and_b32 v201, s72, v128\n"
    "v_lshlrev_b32 v202, 16, v129\n"
    "v_and_b32 v203, s72, v129\n"
    "v_lshlrev_b32 v204, 16, v130\n"
    "v_and_b32 v205, s72, v130\n"
    "v_lshlrev_b32 v206, 16, v131\n"
    "v_and_b32 v207, s72, v131\n"
    "v_pk_add_f32 v[184:185], v[184:185], v[200:201]\n"
    "v_pk_add_f32 v[186:187], v[186:187], v[202:203]\n"
    "v_pk_add_f32 v[188:189], v[188:189], v[204:205]\n"
    "v_pk_add_f32 v[190:191], v[190:191], v[206:207]\n"
    "v_cvt_pk_bf16_f32 v184, v184, v185\n"
    "v_cvt_pk_bf16_f32 v185, v186, v187\n"
    "v_cvt_pk_bf16_f32 v186, v188, v189\n"
    "v_cvt_pk_bf16_f32 v187, v190, v191\n"
    "s_nop 1\n"
    "v_permlane16_swap_b32 v184, v186\n"
    "v_permlane16_swap_b32 v185, v187\n"
    "buffer_store_dwordx4 v[168:171], v6, s[40:43], s76 offen \n"
    "buffer_store_dwordx4 v[176:179], v6, s[40:43], s77 offen \n"
    "buffer_store_dwordx4 v[184:187], v6, s[40:43], s78 offen \n"
    "s_cbranch_scc0 L_done_%=\n"
    "s_waitcnt vmcnt(3)\n"
    "v_lshlrev_b32 v200, 16, v136\n"
    "v_and_b32 v201, s72, v136\n"
    "v_lshlrev_b32 v202, 16, v137\n"
    "v_and_b32 v203, s72, v137\n"
    "v_lshlrev_b32 v204, 16, v138\n"
    "v_and_b32 v205, s72, v138\n"
    "v_lshlrev_b32 v206, 16, v139\n"
    "v_and_b32 v207, s72, v139\n"
    "v_pk_add_f32 v[192:193], v[192:193], v[200:201]\n"
    "v_pk_add_f32 v[194:195], v[194:195], v[202:203]\n"
    "v_pk_add_f32 v[196:197], v[196:197], v[204:205]\n"
    "v_pk_add_f32 v[198:199], v[198:199], v[206:207]\n"
    "v_lshlrev_b32 v200, 16, v140\n"
    "v_and_b32 v201, s72, v140\n"
    "v_lshlrev_b32 v202, 16, v141\n"
    "v_and_b32 v203, s72, v141\n"
    "v_lshlrev_b32 v204, 16, v142\n"
    "v_and_b32 v205, s72, v142\n"
    "v_lshlrev_b32 v206, 16, v143\n"
    "v_and_b32 v207, s72, v143\n"
    "v_pk_add_f32 v[192:193], v[192:193], v[200:201]\n"
    "v_pk_add_f32 v[194:195], v[194:195], v[202:203]\n"
    "v_pk_add_f32 v[196:197], v[196:197], v[204:205]\n"
    "v_pk_add_f32 v[198:199], v[198:199], v[206:207]\n"
    "v_lshlrev_b32 v200, 16, v144\n"
    "v_and_b32 v201, s72, v144\n"
    "v_lshlrev_b32 v202, 16, v145\n"
    "v_and_b32 v203, s72, v145\n"
    "v_lshlrev_b32 v204, 16, v146\n"
    "v_and_b32 v205, s72, v146\n"
    "v_lshlrev_b32 v206, 16, v147\n"
    "v_and_b32 v207, s72, v147\n"
    "v_pk_add_f32 v[192:193], v[192:193], v[200:201]\n"
    "v_pk_add_f32 v[194:195], v[194:195], v[202:203]\n"
    "v_pk_add_f32 v[196:197], v[196:197], v[204:205]\n"
    "v_pk_add_f32 v[198:199], v[198:199], v[206:207]\n"
    "v_lshlrev_b32 v200, 16, v148\n"
    "v_and_b32 v201, s72, v148\n"
    "v_lshlrev_b32 v202, 16, v149\n"
    "v_and_b32 v203, s72, v149\n"
    "v_lshlrev_b32 v204, 16, v150\n"
    "v_and_b32 v205, s72, v150\n"
    "v_lshlrev_b32 v206, 16, v151\n"
    "v_and_b32 v207, s72, v151\n"
    "v_pk_add_f32 v[192:193], v[192:193], v[200:201]\n"
    "v_pk_add_f32 v[194:195], v[194:195], v[202:203]\n"
    "v_pk_add_f32 v[196:197], v[196:197], v[204:205]\n"
    "v_pk_add_f32 v[198:199], v[198:199], v[206:207]\n"
    "v_lshlrev_b32 v200, 16, v152\n"
    "v_and_b32 v201, s72, v152\n"
    "v_lshlrev_b32 v202, 16, v153\n"
    "v_and_b32 v203, s72, v153\n"
    "v_lshlrev_b32 v204, 16, v154\n"
    "v_and_b32 v205, s72, v154\n"
    "v_lshlrev_b32 v206, 16, v155\n"
    "v_and_b32 v207, s72, v155\n"
    "v_pk_add_f32 v[192:193], v[192:193], v[200:201]\n"
    "v_pk_add_f32 v[194:195], v[194:195], v[202:203]\n"
    "v_pk_add_f32 v[196:197], v[196:197], v[204:205]\n"
    "v_pk_add_f32 v[198:199], v[198:199], v[206:207]\n"
    "v_lshlrev_b32 v200, 16, v156\n"
    "v_and_b32 v201, s72, v156\n"
    "v_lshlrev_b32 v202, 16, v157\n"
    "v_and_b32 v203, s72, v157\n"
    "v_lshlrev_b32 v204, 16, v158\n"
    "v_and_b32 v205, s72, v158\n"
    "v_lshlrev_b32 v206, 16, v159\n"
    "v_and_b32 v207, s72, v159\n"
    "v_pk_add_f32 v[192:193], v[192:193], v[200:201]\n"
    "v_pk_add_f32 v[194:195], v[194:195], v[202:203]\n"
    "v_pk_add_f32 v[196:197], v[196:197], v[204:205]\n"
    "v_pk_add_f32 v[198:199], v[198:199], v[206:207]\n"
    "v_lshlrev_b32 v200, 16, v160\n"
    "v_and_b32 v201, s72, v160\n"
    "v_lshlrev_b32 v202, 16, v161\n"
    "v_and_b32 v203, s72, v161\n"
    "v_lshlrev_b32 v204, 16, v162\n"
    "v_and_b32 v205, s72, v162\n"
    "v_lshlrev_b32 v206, 16, v163\n"
    "v_and_b32 v207, s72, v163\n"
    "v_pk_add_f32 v[192:193], v[192:193], v[200:201]\n"
    "v_pk_add_f32 v[194:195], v[194:195], v[202:203]\n"
    "v_pk_add_f32 v[196:197], v[196:197], v[204:205]\n"
    "v_pk_add_f32 v[198:199], v[198:199], v[206:207]\n"
    "v_cvt_pk_bf16_f32 v192, v192, v193\n"
    "v_cvt_pk_bf16_f32 v193, v194, v195\n"
    "v_cvt_pk_bf16_f32 v194, v196, v197\n"
    "v_cvt_pk_bf16_f32 v195, v198, v199\n"
    "s_nop 1\n"
    "v_permlane16_swap_b32 v192, v194\n"
    "v_permlane16_swap_b32 v193, v195\n"
    "buffer_store_dwordx4 v[192:195], v6, s[40:43], s79 offen \n"
    "L_done_%=:\n"
    "s_waitcnt vmcnt(0) lgkmcnt(0)\n"
    "s_branch L_end_%=\n"
    "L_own0_%=:\n"
    "v_accvgpr_read_b32 v168, a0\n"
    "v_accvgpr_read_b32 v169, a1\n"
    "v_accvgpr_read_b32 v170, a2\n"
    "v_accvgpr_read_b32 v171, a3\n"
    "v_accvgpr_read_b32 v172, a4\n"
    "v_accvgpr_read_b32 v173, a5\n"
    "v_accvgpr_read_b32 v174, a6\n"
    "v_accvgpr_read_b32 v175, a7\n"
    "v_accvgpr_read_b32 v176, a64\n"
    "v_accvgpr_read_b32 v177, a65\n"
    "v_accvgpr_read_b32 v178, a66\n"
    "v_accvgpr_read_b32 v179, a67\n"
    "v_accvgpr_read_b32 v180, a68\n"
    "v_accvgpr_read_b32 v181, a69\n"
    "v_accvgpr_read_b32 v182, a70\n"
    "v_accvgpr_read_b32 v183, a71\n"
    "v_accvgpr_read_b32 v184, a128\n"
    "v_accvgpr_read_b32 v185, a129\n"
    "v_accvgpr_read_b32 v186, a130\n"
    "v_accvgpr_read_b32 v187, a131\n"
    "v_accvgpr_read_b32 v188, a132\n"
    "v_accvgpr_read_b32 v189, a133\n"
    "v_accvgpr_read_b32 v190, a134\n"
    "v_accvgpr_read_b32 v191, a135\n"
    "v_accvgpr_read_b32 v192, a192\n"
    "v_accvgpr_read_b32 v193, a193\n"
    "v_accvgpr_read_b32 v194, a194\n"
    "v_accvgpr_read_b32 v195, a195\n"
    "v_accvgpr_read_b32 v196, a196\n"
    "v_accvgpr_read_b32 v197, a197\n"
    "v_accvgpr_read_b32 v198, a198\n"
    "v_accvgpr_read_b32 v199, a199\n"
    "s_setpc_b64 s[90:91]\n"
    "L_own1_%=:\n"
    "v_accvgpr_read_b32 v168, a8\n"
    "v_accvgpr_read_b32 v169, a9\n"
    "v_accvgpr_read_b32 v170, a10\n"
    "v_accvgpr_read_b32 v171, a11\n"
    "v_accvgpr_read_b32 v172, a12\n"
    "v_accvgpr_read_b32 v173, a13\n"
    "v_accvgpr_read_b32 v174, a14\n"
    "v_accvgpr_read_b32 v175, a15\n"
    "v_accvgpr_read_b32 v176, a72\n"
    "v_accvgpr_read_b32 v177, a73\n"
    "v_accvgpr_read_b32 v178, a74\n"
    "v_accvgpr_read_b32 v179, a75\n"
    "v_accvgpr_read_b32 v180, a76\n"
    "v_accvgpr_read_b32 v181, a77\n"
    "v_accvgpr_read_b32 v182, a78\n"
    "v_accvgpr_read_b32 v183, a79\n"
    "v_accvgpr_read_b32 v184, a136\n"
    "v_accvgpr_read_b32 v185, a137\n"
    "v_accvgpr_read_b32 v186, a138\n"
    "v_accvgpr_read_b32 v187, a139\n"
    "v_accvgpr_read_b32 v188, a140\n"
    "v_accvgpr_read_b32 v189, a141\n"
    "v_accvgpr_read_b32 v190, a142\n"
    "v_accvgpr_read_b32 v191, a143\n"
    "v_accvgpr_read_b32 v192, a200\n"
    "v_accvgpr_read_b32 v193, a201\n"
    "v_accvgpr_read_b32 v194, a202\n"
    "v_accvgpr_read_b32 v195, a203\n"
    "v_accvgpr_read_b32 v196, a204\n"
    "v_accvgpr_read_b32 v197, a205\n"
    "v_accvgpr_read_b32 v198, a206\n"
    "v_accvgpr_read_b32 v199, a207\n"
    "s_setpc_b64 s[90:91]\n"
    "L_own2_%=:\n"
    "v_accvgpr_read_b32 v168, a16\n"
    "v_accvgpr_read_b32 v169, a17\n"
    "v_accvgpr_read_b32 v170, a18\n"
    "v_accvgpr_read_b32 v171, a19\n"
    "v_accvgpr_read_b32 v172, a20\n"
    "v_accvgpr_read_b32 v173, a21\n"
    "v_accvgpr_read_b32 v174, a22\n"
    "v_accvgpr_read_b32 v175, a23\n"
    "v_accvgpr_read_b32 v176, a80\n"
    "v_accvgpr_read_b32 v177, a81\n"
    "v_accvgpr_read_b32 v178, a82\n"
    "v_accvgpr_read_b32 v179, a83\n"
    "v_accvgpr_read_b32 v180, a84\n"
    "v_accvgpr_read_b32 v181, a85\n"
    "v_accvgpr_read_b32 v182, a86\n"
    "v_accvgpr_read_b32 v183, a87\n"
    "v_accvgpr_read_b32 v184, a144\n"
    "v_accvgpr_read_b32 v185, a145\n"
    "v_accvgpr_read_b32 v186, a146\n"
    "v_accvgpr_read_b32 v187, a147\n"
    "v_accvgpr_read_b32 v188, a148\n"
    "v_accvgpr_read_b32 v189, a149\n"
    "v_accvgpr_read_b32 v190, a150\n"
    "v_accvgpr_read_b32 v191, a151\n"
    "v_accvgpr_read_b32 v192, a208\n"
    "v_accvgpr_read_b32 v193, a209\n"
    "v_accvgpr_read_b32 v194, a210\n"
    "v_accvgpr_read_b32 v195, a211\n"
    "v_accvgpr_read_b32 v196, a212\n"
    "v_accvgpr_read_b32 v197, a213\n"
    "v_accvgpr_read_b32 v198, a214\n"
    "v_accvgpr_read_b32 v199, a215\n"
    "s_setpc_b64 s[90:91]\n"
    "L_own3_%=:\n"
    "v_accvgpr_read_b32 v168, a24\n"
    "v_accvgpr_read_b32 v169, a25\n"
    "v_accvgpr_read_b32 v170, a26\n"
    "v_accvgpr_read_b32 v171, a27\n"
    "v_accvgpr_read_b32 v172, a28\n"
    "v_accvgpr_read_b32 v173, a29\n"
    "v_accvgpr_read_b32 v174, a30\n"
    "v_accvgpr_read_b32 v175, a31\n"
    "v_accvgpr_read_b32 v176, a88\n"
    "v_accvgpr_read_b32 v177, a89\n"
    "v_accvgpr_read_b32 v178, a90\n"
    "v_accvgpr_read_b32 v179, a91\n"
    "v_accvgpr_read_b32 v180, a92\n"
    "v_accvgpr_read_b32 v181, a93\n"
    "v_accvgpr_read_b32 v182, a94\n"
    "v_accvgpr_read_b32 v183, a95\n"
    "v_accvgpr_read_b32 v184, a152\n"
    "v_accvgpr_read_b32 v185, a153\n"
    "v_accvgpr_read_b32 v186, a154\n"
    "v_accvgpr_read_b32 v187, a155\n"
    "v_accvgpr_read_b32 v188, a156\n"
    "v_accvgpr_read_b32 v189, a157\n"
    "v_accvgpr_read_b32 v190, a158\n"
    "v_accvgpr_read_b32 v191, a159\n"
    "v_accvgpr_read_b32 v192, a216\n"
    "v_accvgpr_read_b32 v193, a217\n"
    "v_accvgpr_read_b32 v194, a218\n"
    "v_accvgpr_read_b32 v195, a219\n"
    "v_accvgpr_read_b32 v196, a220\n"
    "v_accvgpr_read_b32 v197, a221\n"
    "v_accvgpr_read_b32 v198, a222\n"
    "v_accvgpr_read_b32 v199, a223\n"
    "s_setpc_b64 s[90:91]\n"
    "L_own4_%=:\n"
    "v_accvgpr_read_b32 v168, a32\n"
    "v_accvgpr_read_b32 v169, a33\n"
    "v_accvgpr_read_b32 v170, a34\n"
    "v_accvgpr_read_b32 v171, a35\n"
    "v_accvgpr_read_b32 v172, a36\n"
    "v_accvgpr_read_b32 v173, a37\n"
    "v_accvgpr_read_b32 v174, a38\n"
    "v_accvgpr_read_b32 v175, a39\n"
    "v_accvgpr_read_b32 v176, a96\n"
    "v_accvgpr_read_b32 v177, a97\n"
    "v_accvgpr_read_b32 v178, a98\n"
    "v_accvgpr_read_b32 v179, a99\n"
    "v_accvgpr_read_b32 v180, a100\n"
    "v_accvgpr_read_b32 v181, a101\n"
    "v_accvgpr_read_b32 v182, a102\n"
    "v_accvgpr_read_b32 v183, a103\n"
    "v_accvgpr_read_b32 v184, a160\n"
    "v_accvgpr_read_b32 v185, a161\n"
    "v_accvgpr_read_b32 v186, a162\n"
    "v_accvgpr_read_b32 v187, a163\n"
    "v_accvgpr_read_b32 v188, a164\n"
    "v_accvgpr_read_b32 v189, a165\n"
    "v_accvgpr_read_b32 v190, a166\n"
    "v_accvgpr_read_b32 v191, a167\n"
    "v_accvgpr_read_b32 v192, a224\n"
    "v_accvgpr_read_b32 v193, a225\n"
    "v_accvgpr_read_b32 v194, a226\n"
    "v_accvgpr_read_b32 v195, a227\n"
    "v_accvgpr_read_b32 v196, a228\n"
    "v_accvgpr_read_b32 v197, a229\n"
    "v_accvgpr_read_b32 v198, a230\n"
    "v_accvgpr_read_b32 v199, a231\n"
    "s_setpc_b64 s[90:91]\n"
    "L_own5_%=:\n"
    "v_accvgpr_read_b32 v168, a40\n"
    "v_accvgpr_read_b32 v169, a41\n"
    "v_accvgpr_read_b32 v170, a42\n"
    "v_accvgpr_read_b32 v171, a43\n"
    "v_accvgpr_read_b32 v172, a44\n"
    "v_accvgpr_read_b32 v173, a45\n"
    "v_accvgpr_read_b32 v174, a46\n"
    "v_accvgpr_read_b32 v175, a47\n"
    "v_accvgpr_read_b32 v176, a104\n"
    "v_accvgpr_read_b32 v177, a105\n"
    "v_accvgpr_read_b32 v178, a106\n"
    "v_accvgpr_read_b32 v179, a107\n"
    "v_accvgpr_read_b32 v180, a108\n"
    "v_accvgpr_read_b32 v181, a109\n"
    "v_accvgpr_read_b32 v182, a110\n"
    "v_accvgpr_read_b32 v183, a111\n"
    "v_accvgpr_read_b32 v184, a168\n"
    "v_accvgpr_read_b32 v185, a169\n"
    "v_accvgpr_read_b32 v186, a170\n"
    "v_accvgpr_read_b32 v187, a171\n"
    "v_accvgpr_read_b32 v188, a172\n"
    "v_accvgpr_read_b32 v189, a173\n"
    "v_accvgpr_read_b32 v190, a174\n"
    "v_accvgpr_read_b32 v191, a175\n"
    "v_accvgpr_read_b32 v192, a232\n"
    "v_accvgpr_read_b32 v193, a233\n"
    "v_accvgpr_read_b32 v194, a234\n"
    "v_accvgpr_read_b32 v195, a235\n"
    "v_accvgpr_read_b32 v196, a236\n"
    "v_accvgpr_read_b32 v197, a237\n"
    "v_accvgpr_read_b32 v198, a238\n"
    "v_accvgpr_read_b32 v199, a239\n"
    "s_setpc_b64 s[90:91]\n"
    "L_own6_%=:\n"
    "v_accvgpr_read_b32 v168, a48\n"
    "v_accvgpr_read_b32 v169, a49\n"
    "v_accvgpr_read_b32 v170, a50\n"
    "v_accvgpr_read_b32 v171, a51\n"
    "v_accvgpr_read_b32 v172, a52\n"
    "v_accvgpr_read_b32 v173, a53\n"
    "v_accvgpr_read_b32 v174, a54\n"
    "v_accvgpr_read_b32 v175, a55\n"
    "v_accvgpr_read_b32 v176, a112\n"
    "v_accvgpr_read_b32 v177, a113\n"
    "v_accvgpr_read_b32 v178, a114\n"
    "v_accvgpr_read_b32 v179, a115\n"
    "v_accvgpr_read_b32 v180, a116\n"
    "v_accvgpr_read_b32 v181, a117\n"
    "v_accvgpr_read_b32 v182, a118\n"
    "v_accvgpr_read_b32 v183, a119\n"
    "v_accvgpr_read_b32 v184, a176\n"
    "v_accvgpr_read_b32 v185, a177\n"
    "v_accvgpr_read_b32 v186, a178\n"
    "v_accvgpr_read_b32 v187, a179\n"
    "v_accvgpr_read_b32 v188, a180\n"
    "v_accvgpr_read_b32 v189, a181\n"
    "v_accvgpr_read_b32 v190, a182\n"
    "v_accvgpr_read_b32 v191, a183\n"
    "v_accvgpr_read_b32 v192, a232\n"
    "v_accvgpr_read_b32 v193, a233\n"
    "v_accvgpr_read_b32 v194, a234\n"
    "v_accvgpr_read_b32 v195, a235\n"
    "v_accvgpr_read_b32 v196, a236\n"
    "v_accvgpr_read_b32 v197, a237\n"
    "v_accvgpr_read_b32 v198, a238\n"
    "v_accvgpr_read_b32 v199, a239\n"
    "s_setpc_b64 s[90:91]\n"
    "L_own7_%=:\n"
    "v_accvgpr_read_b32 v168, a56\n"
    "v_accvgpr_read_b32 v169, a57\n"
    "v_accvgpr_read_b32 v170, a58\n"
    "v_accvgpr_read_b32 v171, a59\n"
    "v_accvgpr_read_b32 v172, a60\n"
    "v_accvgpr_read_b32 v173, a61\n"
    "v_accvgpr_read_b32 v174, a62\n"
    "v_accvgpr_read_b32 v175, a63\n"
    "v_accvgpr_read_b32 v176, a120\n"
    "v_accvgpr_read_b32 v177, a121\n"
    "v_accvgpr_read_b32 v178, a122\n"
    "v_accvgpr_read_b32 v179, a123\n"
    "v_accvgpr_read_b32 v180, a124\n"
    "v_accvgpr_read_b32 v181, a125\n"
    "v_accvgpr_read_b32 v182, a126\n"
    "v_accvgpr_read_b32 v183, a127\n"
    "v_accvgpr_read_b32 v184, a184\n"
    "v_accvgpr_read_b32 v185, a185\n"
    "v_accvgpr_read_b32 v186, a186\n"
    "v_accvgpr_read_b32 v187, a187\n"
    "v_accvgpr_read_b32 v188, a188\n"
    "v_accvgpr_read_b32 v189, a189\n"
    "v_accvgpr_read_b32 v190, a190\n"
    "v_accvgpr_read_b32 v191, a191\n"
    "v_accvgpr_read_b32 v192, a232\n"
    "v_accvgpr_read_b32 v193, a233\n"
    "v_accvgpr_read_b32 v194, a234\n"
    "v_accvgpr_read_b32 v195, a235\n"
    "v_accvgpr_read_b32 v196, a236\n"
    "v_accvgpr_read_b32 v197, a237\n"
    "v_accvgpr_read_b32 v198, a238\n"
    "v_accvgpr_read_b32 v199, a239\n"
    "s_setpc_b64 s[90:91]\n"
    "L_stub110a_%=:\n"
    "s_call_b64 s[90:91], L_rare0_%=\n"
    "s_branch L_back110a_%=\n"
    "L_stub111a_%=:\n"
    "s_call_b64 s[90:91], L_rare1_%=\n"
    "s_branch L_back111a_%=\n"
    "L_stub112a_%=:\n"
    "s_call_b64 s[90:91], L_rare2_%=\n"
    "s_branch L_back112a_%=\n"
    "L_stub100a_%=:\n"
    "s_call_b64 s[90:91], L_rare0_%=\n"
    "s_branch L_back100a_%=\n"
    "L_stub101d_%=:\n"
    "s_call_b64 s[90:91], L_rare1_%=\n"
    "s_branch L_back101d_%=\n"
    "L_stub102d_%=:\n"
    "s_call_b64 s[90:91], L_rare2_%=\n"
    "s_branch L_back102d_%=\n"
    "L_stub110d_%=:\n"
    "s_call_b64 s[90:91], L_rare0_%=\n"
    "s_branch L_back110d_%=\n"
    "L_stub111d_%=:\n"
    "s_call_b64 s[90:91], L_rare1_%=\n"
    "s_branch L_back111d_%=\n"
    "L_stub112d_%=:\n"
    "s_call_b64 s[90:91], L_rare2_%=\n"
    "s_branch L_back112d_%=\n"
    "L_stub100d_%=:\n"
    "s_call_b64 s[90:91], L_rare0_%=\n"
    "s_branch L_back100d_%=\n"
    "L_stub101b_%=:\n"
    "s_call_b64 s[90:91], L_rare1_%=\n"
    "s_branch L_back101b_%=\n"
    "L_stub102b_%=:\n"
    "s_call_b64 s[90:91], L_rare2_%=\n"
    "s_branch L_back102b_%=\n"
    "L_stub110b_%=:\n"
    "s_call_b64 s[90:91], L_rare0_%=\n"
    "s_branch L_back110b_%=\n"
    "L_stub111b_%=:\n"
    "s_call_b64 s[90:91], L_rare1_%=\n"
    "s_branch L_back111b_%=\n"
    "L_stub112b_%=:\n"
    "s_call_b64 s[90:91], L_rare2_%=\n"
    "s_branch L_back112b_%=\n"
    "L_stub100b_%=:\n"
    "s_call_b64 s[90:91], L_rare0_%=\n"
    "s_branch L_back100b_%=\n"
    "L_stub101c_%=:\n"
    "s_call_b64 s[90:91], L_rare1_%=\n"
    "s_branch L_back101c_%=\n"
    "L_stub102c_%=:\n"
    "s_call_b64 s[90:91], L_rare2_%=\n"
    "s_branch L_back102c_%=\n"
    "L_stub110c_%=:\n"
    "s_call_b64 s[90:91], L_rare0_%=\n"
    "s_branch L_back110c_%=\n"
    "L_stub111c_%=:\n"
    "s_call_b64 s[90:91], L_rare1_%=\n"
    "s_branch L_back111c_%=\n"
    "L_stub112c_%=:\n"
    "s_call_b64 s[90:91], L_rare2_%=\n"
    "s_branch L_back112c_%=\n"
    "L_stub210a_%=:\n"
    "s_call_b64 s[90:91], L_rare0_%=\n"
    "s_branch L_back210a_%=\n"
    "L_stub211a_%=:\n"
    "s_call_b64 s[90:91], L_rare1_%=\n"
    "s_branch L_back211a_%=\n"
    "L_stub212a_%=:\n"
    "s_call_b64 s[90:91], L_rare2_%=\n"
    "s_branch L_back212a_%=\n"
    "L_stub200a_%=:\n"
    "s_call_b64 s[90:91], L_rare0_%=\n"
    "s_branch L_back200a_%=\n"
    "L_stub201b_%=:\n"
    "s_call_b64 s[90:91], L_rare1_%=\n"
    "s_branch L_back201b_%=\n"
    "L_stub202b_%=:\n"
    "s_call_b64 s[90:91], L_rare2_%=\n"
    "s_branch L_back202b_%=\n"
    "L_stub210b_%=:\n"
    "s_call_b64 s[90:91], L_rare0_%=\n"
    "s_branch L_back210b_%=\n"
    "L_stub211b_%=:\n"
    "s_call_b64 s[90:91], L_rare1_%=\n"
    "s_branch L_back211b_%=\n"
    "L_stub212b_%=:\n"
    "s_call_b64 s[90:91], L_rare2_%=\n"
    "s_branch L_back212b_%=\n"
    "L_stub200b_%=:\n"
    "s_call_b64 s[90:91], L_rare0_%=\n"
    "s_branch L_back200b_%=\n"
    "L_stub201c_%=:\n"
    "s_call_b64 s[90:91], L_rare1_%=\n"
    "s_branch L_back201c_%=\n"
    "L_stub202c_%=:\n"
    "s_call_b64 s[90:91], L_rare2_%=\n"
    "s_branch L_back202c_%=\n"
    "L_stub210c_%=:\n"
    "s_call_b64 s[90:91], L_rare0_%=\n"
    "s_branch L_back210c_%=\n"
    "L_stub211c_%=:\n"
    "s_call_b64 s[90:91], L_rare1_%=\n"
    "s_branch L_back211c_%=\n"
    "L_stub212c_%=:\n"
    "s_call_b64 s[90:91], L_rare2_%=\n"
    "s_branch L_back212c_%=\n"
    "L_hslow_%=:\n"
    "s_add_u32 s37, s37, 1\n"
    "s_cmp_gt_u32 s37, 400000\n"
    "s_cbranch_scc1 L_end_%=\n"
    "v_accvgpr_read_b32 v37, a214\n"
    "buffer_load_dword a212, v37, s[92:95], 0 offen offset:0 sc1\n"
    "s_waitcnt vmcnt(0)\n"
    "v_accvgpr_read_b32 v37, a213\n"
    "buffer_store_dword a212, v37, s[92:95], 0 offen offset:256 sc1\n"
    "s_waitcnt vmcnt(0)\n"
    "v_accvgpr_read_b32 v37, a214\n"
    "buffer_load_dword a212, v37, s[92:95], 0 offen offset:256 sc1\n"
    "s_waitcnt vmcnt(0)\n"
    "v_accvgpr_read_b32 v37, a211\n"
    "v_accvgpr_read_b32 v36, a212\n"
    "v_cmp_ne_u32 vcc, v37, v36\n"
    "s_and_b64 vcc, vcc, 0xff\n"
    "s_cbranch_vccnz L_hslow_%=\n"
    "s_branch L_hok_%=\n"
    "L_rare0_%=:\n"
    "v_mov_b32 v36, v37\n"
    "s_nop 1\n"
    "v_permlane32_swap_b32 v37, v36\n"
    "v_max_f32 v37, v37, v36\n"
    "v_mov_b32 v36, v37\n"
    "s_nop 1\n"
    "v_permlane16_swap_b32 v37, v36\n"
    "v_max_f32 v37, v37, v36\n"
    "v_max_f32 v37, 0, v37\n"
    "v_sub_f32 v36, 0, v37\n"
    "v_exp_f32 v36, v36\n"
    "v_sub_f32 v184, v184, v37\n"
    "v_sub_f32 v185, v185, v37\n"
    "v_sub_f32 v186, v186, v37\n"
    "v_sub_f32 v187, v187, v37\n"
    "v_sub_f32 v188, v188, v37\n"
    "v_sub_f32 v189, v189, v37\n"
    "v_sub_f32 v190, v190, v37\n"
    "v_sub_f32 v191, v191, v37\n"
    "v_sub_f32 v24, v24, v37\n"
    "v_sub_f32 v25, v25, v37\n"
    "v_sub_f32 v26, v26, v37\n"
    "v_sub_f32 v27, v27, v37\n"
    "v_mul_f32 v21, v21, v36\n"
    "v_pk_mul_f32 v[88:89], v[88:89], v[36:37] op_sel_hi:[1,0]\n"
    "v_pk_mul_f32 v[90:91], v[90:91], v[36:37] op_sel_hi:[1,0]\n"
    "v_pk_mul_f32 v[100:101], v[100:101], v[36:37] op_sel_hi:[1,0]\n"
    "v_pk_mul_f32 v[102:103], v[102:103], v[36:37] op_sel_hi:[1,0]\n"
    "v_pk_mul_f32 v[112:113], v[112:113], v[36:37] op_sel_hi:[1,0]\n"
    "v_pk_mul_f32 v[114:115], v[114:115], v[36:37] op_sel_hi:[1,0]\n"
    "v_pk_mul_f32 v[124:125], v[124:125], v[36:37] op_sel_hi:[1,0]\n"
    "v_pk_mul_f32 v[126:127], v[126:127], v[36:37] op_sel_hi:[1,0]\n"
    "v_pk_mul_f32 v[136:137], v[136:137], v[36:37] op_sel_hi:[1,0]\n"
    "v_pk_mul_f32 v[138:139], v[138:139], v[36:37] op_sel_hi:[1,0]\n"
    "v_pk_mul_f32 v[148:149], v[148:149], v[36:37] op_sel_hi:[1,0]\n"
    "v_pk_mul_f32 v[150:151], v[150:151], v[36:37] op_sel_hi:[1,0]\n"
    "v_pk_mul_f32 v[160:161], v[160:161], v[36:37] op_sel_hi:[1,0]\n"
    "v_pk_mul_f32 v[162:163], v[162:163], v[36:37] op_sel_hi:[1,0]\n"
    "v_pk_mul_f32 v[172:173], v[172:173], v[36:37] op_sel_hi:[1,0]\n"
    "v_pk_mul_f32 v[174:175], v[174:175], v[36:37] op_sel_hi:[1,0]\n"
    "s_setpc_b64 s[90:91]\n"
    "L_rare1_%=:\n"
    "v_mov_b32 v36, v37\n"
    "s_nop 1\n"
    "v_permlane32_swap_b32 v37, v36\n"
    "v_max_f32 v37, v37, v36\n"
    "v_mov_b32 v36, v37\n"
    "s_nop 1\n"
    "v_permlane16_swap_b32 v37, v36\n"
    "v_max_f32 v37, v37, v36\n"
    "v_max_f32 v37, 0, v37\n"
    "v_sub_f32 v36, 0, v37\n"
    "v_exp_f32 v36, v36\n"
    "v_sub_f32 v192, v192, v37\n"
    "v_sub_f32 v193, v193, v37\n"
    "v_sub_f32 v194, v194, v37\n"
    "v_sub_f32 v195, v195, v37\n"
    "v_sub_f32 v196, v196, v37\n"
    "v_sub_f32 v197, v197, v37\n"
    "v_sub_f32 v198, v198, v37\n"
    "v_sub_f32 v199, v199, v37\n"
    "v_sub_f32 v28, v28, v37\n"
    "v_sub_f32 v29, v29, v37\n"
    "v_sub_f32 v30, v30, v37\n"
    "v_sub_f32 v31, v31, v37\n"
    "v_mul_f32 v22, v22, v36\n"
    "v_pk_mul_f32 v[92:93], v[92:93], v[36:37] op_sel_hi:[1,0]\n"
    "v_pk_mul_f32 v[94:95], v[94:95], v[36:37] op_sel_hi:[1,0]\n"
    "v_pk_mul_f32 v[104:105], v[104:105], v[36:37] op_sel_hi:[1,0]\n"
    "v_pk_mul_f32 v[106:107], v[106:107], v[36:37] op_sel_hi:[1,0]\n"
    "v_pk_mul_f32 v[116:117], v[116:117], v[36:37] op_sel_hi:[1,0]\n"
    "v_pk_mul_f32 v[118:119], v[118:119], v[36:37] op_sel_hi:[1,0]\n"
    "v_pk_mul_f32 v[128:129], v[128:129], v[36:37] op_sel_hi:[1,0]\n"
    "v_pk_mul_f32 v[130:131], v[130:131], v[36:37] op_sel_hi:[1,0]\n"
    "v_pk_mul_f32 v[140:141], v[140:141], v[36:37] op_sel_hi:[1,0]\n"
    "v_pk_mul_f32 v[142:143], v[142:143], v[36:37] op_sel_hi:[1,0]\n"
    "v_pk_mul_f32 v[152:153], v[152:153], v[36:37] op_sel_hi:[1,0]\n"
    "v_pk_mul_f32 v[154:155], v[154:155], v[36:37] op_sel_hi:[1,0]\n"
    "v_pk_mul_f32 v[164:165], v[164:165], v[36:37] op_sel_hi:[1,0]\n"
    "v_pk_mul_f32 v[166:167], v[166:167], v[36:37] op_sel_hi:[1,0]\n"
    "v_pk_mul_f32 v[176:177], v[176:177], v[36:37] op_sel_hi:[1,0]\n"
    "v_pk_mul_f32 v[178:179], v[178:179], v[36:37] op_sel_hi:[1,0]\n"
    "s_setpc_b64 s[90:91]\n"
    "L_rare2_%=:\n"
    "v_mov_b32 v36, v37\n"
    "s_nop 1\n"
    "v_permlane32_swap_b32 v37, v36\n"
    "v_max_f32 v37, v37, v36\n"
    "v_mov_b32 v36, v37\n"
    "s_nop 1\n"
    "v_permlane16_swap_b32 v37, v36\n"
    "v_max_f32 v37, v37, v36\n"
    "v_max_f32 v37, 0, v37\n"
    "v_sub_f32 v36, 0, v37\n"
    "v_exp_f32 v36, v36\n"
    "v_sub_f32 v200, v200, v37\n"
    "v_sub_f32 v201, v201, v37\n"
    "v_sub_f32 v202, v202, v37\n"
    "v_sub_f32 v203, v203, v37\n"
    "v_sub_f32 v204, v204, v37\n"
    "v_sub_f32 v205, v205, v37\n"
    "v_sub_f32 v206, v206, v37\n"
    "v_sub_f32 v207, v207, v37\n"
    "v_sub_f32 v32, v32, v37\n"
    "v_sub_f32 v33, v33, v37\n"
    "v_sub_f32 v34, v34, v37\n"
    "v_sub_f32 v35, v35, v37\n"
    "v_mul_f32 v23, v23, v36\n"
    "v_pk_mul_f32 v[96:97], v[96:97], v[36:37] op_sel_hi:[1,0]\n"
    "v_pk_mul_f32 v[98:99], v[98:99], v[36:37] op_sel_hi:[1,0]\n"
    "v_pk_mul_f32 v[108:109], v[108:109], v[36:37] op_sel_hi:[1,0]\n"
    "v_pk_mul_f32 v[110:111], v[110:111], v[36:37] op_sel_hi:[1,0]\n"
    "v_pk_mul_f32 v[120:121], v[120:121], v[36:37] op_sel_hi:[1,0]\n"
    "v_pk_mul_f32 v[122:123], v[122:123], v[36:37] op_sel_hi:[1,0]\n"
    "v_pk_mul_f32 v[132:133], v[132:133], v[36:37] op_sel_hi:[1,0]\n"
    "v_pk_mul_f32 v[134:135], v[134:135], v[36:37] op_sel_hi:[1,0]\n"
    "v_pk_mul_f32 v[144:145], v[144:145], v[36:37] op_sel_hi:[1,0]\n"
    "v_pk_mul_f32 v[146:147], v[146:147], v[36:37] op_sel_hi:[1,0]\n"
    "v_pk_mul_f32 v[156:157], v[156:157], v[36:37] op_sel_hi:[1,0]\n"
    "v_pk_mul_f32 v[158:159], v[158:159], v[36:37] op_sel_hi:[1,0]\n"
    "v_pk_mul_f32 v[168:169], v[168:169], v[36:37] op_sel_hi:[1,0]\n"
    "v_pk_mul_f32 v[170:171], v[170:171], v[36:37] op_sel_hi:[1,0]\n"
    "v_pk_mul_f32 v[180:181], v[180:181], v[36:37] op_sel_hi:[1,0]\n"
    "v_pk_mul_f32 v[182:183], v[182:183], v[36:37] op_sel_hi:[1,0]\n"
    "s_setpc_b64 s[90:91]\n"
    "L_end_%=:\n"
    :: "s"(__builtin_amdgcn_kernarg_segment_ptr()), "s"(blockIdx.x), "s"(w), "s"((unsigned)(size_t)lds)
    : "memory", "vcc", "scc", "v0", "v1", "v2", "v3", "v4", "v5", "v6", "v7", "v8", "v9", "v10", "v11", "v12", "v13", "v14", "v15", "v16", "v17", "v18", "v19", "v20", "v21", "v22", "v23", "v24", "v25", "v26", "v27", "v28", "v29", "v30", "v31", "v32", "v33", "v34", "v35", "v36", "v37", "v38", "v39", "v40", "v41", "v42", "v43", "v44", "v45", "v46", "v47", "v48", "v49", "v50", "v51", "v52", "v53", "v54", "v55", "v56", "v57", "v58", "v59", "v60", "v61", "v62", "v63", "v64", "v65", "v66", "v67", "v68", "v69", "v70", "v71", "v72", "v73", "v74", "v75", "v76", "v77", "v78", "v79", "v80", "v81", "v82", "v83", "v84", "v85", "v86", "v87", "v88", "v89", "v90", "v91", "v92", "v93", "v94", "v95", "v96", "v97", "v98", "v99", "v100", "v101", "v102", "v103", "v104", "v105", "v106", "v107", "v108", "v109", "v110", "v111", "v112", "v113", "v114", "v115", "v116", "v117", "v118", "v119", "v120", "v121", "v122", "v123", "v124", "v125", "v126", "v127", "v128", "v129", "v130", "v131", "v132", "v133", "v134", "v135", "v136", "v137", "v138", "v139", "v140", "v141", "v142", "v143", "v144", "v145", "v146", "v147", "v148", "v149", "v150", "v151", "v152", "v153", "v154", "v155", "v156", "v157", "v158", "v159", "v160", "v161", "v162", "v163", "v164", "v165", "v166", "v167", "v168", "v169", "v170", "v171", "v172", "v173", "v174", "v175", "v176", "v177", "v178", "v179", "v180", "v181", "v182", "v183", "v184", "v185", "v186", "v187", "v188", "v189", "v190", "v191", "v192", "v193", "v194", "v195", "v196", "v197", "v198", "v199", "v200", "v201", "v202", "v203", "v204", "v205", "v206", "v207", "v208", "v209", "v210", "v211", "v212", "v213", "v214", "v215", "v216", "v217", "v218", "v219", "v220", "v221", "v222", "v223", "v224", "v225", "v226", "v227", "v228", "v229", "v230", "v231", "v232", "v233", "v234", "v235", "v236", "v237", "v238", "v239", "v240", "v241", "v242", "v243", "v244", "v245", "v246", "v247", "v248", "v249", "v250", "v251", "v252", "v253", "v254", "v255", "a0", "a1", "a2", "a3", "a4", "a5", "a6", "a7", "a8", "a9", "a10", "a11", "a12", "a13", "a14", "a15", "a16", "a17", "a18", "a19", "a20", "a21", "a22", "a23", "a24", "a25", "a26", "a27", "a28", "a29", "a30", "a31", "a32", "a33", "a34", "a35", "a36", "a37", "a38", "a39", "a40", "a41", "a42", "a43", "a44", "a45", "a46", "a47", "a48", "a49", "a50", "a51", "a52", "a53", "a54", "a55", "a56", "a57", "a58", "a59", "a60", "a61", "a62", "a63", "a64", "a65", "a66", "a67", "a68", "a69", "a70", "a71", "a72", "a73", "a74", "a75", "a76", "a77", "a78", "a79", "a80", "a81", "a82", "a83", "a84", "a85", "a86", "a87", "a88", "a89", "a90", "a91", "a92", "a93", "a94", "a95", "a96", "a97", "a98", "a99", "a100", "a101", "a102", "a103", "a104", "a105", "a106", "a107", "a108", "a109", "a110", "a111", "a112", "a113", "a114", "a115", "a116", "a117", "a118", "a119", "a120", "a121", "a122", "a123", "a124", "a125", "a126", "a127", "a128", "a129", "a130", "a131", "a132", "a133", "a134", "a135", "a136", "a137", "a138", "a139", "a140", "a141", "a142", "a143", "a144", "a145", "a146", "a147", "a148", "a149", "a150", "a151", "a152", "a153", "a154", "a155", "a156", "a157", "a158", "a159", "a160", "a161", "a162", "a163", "a164", "a165", "a166", "a167", "a168", "a169", "a170", "a171", "a172", "a173", "a174", "a175", "a176", "a177", "a178", "a179", "a180", "a181", "a182", "a183", "a184", "a185", "a186", "a187", "a188", "a189", "a190", "a191", "a192", "a193", "a194", "a195", "a196", "a197", "a198", "a199", "a200", "a201", "a202", "a203", "a204", "a205", "a206", "a207", "a208", "a209", "a210", "a211", "a212", "a213", "a214", "a215", "a216", "a217", "a218", "a219", "a220", "a221", "a222", "a223", "a224", "a225", "a226", "a227", "a228", "a229", "a230", "a231", "a232", "a233", "a234", "a235", "a236", "a237", "a238", "a239", "a240", "a241", "a242", "a243", "a244", "a245", "a246", "a247", "a248", "a249", "a250", "a251", "a252", "a253", "a254", "a255", "s4", "s5", "s6", "s7", "s8", "s9", "s10", "s11", "s12", "s13", "s14", "s15", "s16", "s17", "s18", "s19", "s20", "s21", "s22", "s23", "s24", "s25", "s26", "s27", "s28", "s29", "s36", "s37", "s38", "s39", "s40", "s41", "s42", "s43", "s44", "s45", "s46", "s47", "s48", "s49", "s50", "s51", "s52", "s53", "s54", "s55", "s56", "s57", "s58", "s59", "s60", "s61", "s62", "s63", "s64", "s65", "s66", "s67", "s68", "s69", "s70", "s71", "s72", "s73", "s74", "s75", "s76", "s77", "s78", "s79", "s80", "s81", "s82", "s83", "s84", "s85", "s86", "s87", "s88", "s89", "s90", "s91", "s92", "s93", "s94", "s95", "s96", "s97", "s98", "s99");
}

extern "C" void kernel_launch(void* const* d_in, const int* in_sizes, int n_in, void* d_out, int out_size, void* d_ws, size_t ws_size, hipStream_t stream) {
  Ptrs a;
  for (int i = 0; i < 10; ++i) a.p[i] = (const char*)d_in[i];
  a.ws = (char*)d_ws;
  a.out = (char*)d_out;
  fused_attn<<<256, 256, 0, stream>>>(a);
}
```
